```python
import torch

def _softmax(x, axis=-1):
    return torch.softmax(x, dim=axis)

def _gelu(x):
    return torch.nn.functional.gelu(x, approximate='tanh')

def _silu(x):
    return torch.nn.functional.silu(x)

def _sigmoid(x):
    return torch.sigmoid(x)

def _layernorm(x, axis=-1, eps=1e-05):
    m = x.mean(dim=axis, keepdim=True)
    v = x.var(dim=axis, keepdim=True, unbiased=False)
    return (x - m) / torch.sqrt(v + eps)

def _scatter_add(tgt, idx, src, axis=0):
    return tgt.clone().index_add_(axis, idx, src)

def _segment_sum(src, seg, n):
    out = torch.zeros((n,) + src.shape[1:], dtype=src.dtype)
    return out.index_add_(0, seg, src)

def _pad(x, pw):
    flat = tuple((v for pair in reversed(pw) for v in pair))
    return torch.nn.functional.pad(x, flat)
kernel_name = 'sequential_preLN__memory_aug_recurrent_mem_tr__swiglu_ffn__prompt'

def setup_inputs():

    _w = lambda a: (lambda t: t.to(torch.bfloat16) if t.is_floating_point() else t)(torch.as_tensor(a))

    def _A_setup():
        B, S, Dm = (1, 2048, 640)
        H, Dh = (32, 128)
        M, seg = (8, 32)
        seg = min(seg, S)
        S = S // seg * seg
        return {'x': _w((lambda *s: torch.randn(*s, dtype=torch.bfloat16) * 0.05)(B, S, Dm)), 'mem0': _w((lambda *s: torch.randn(*s, dtype=torch.bfloat16) * 0.2)(B, M, Dm)), 'Wq': _w((lambda *s: torch.randn(*s, dtype=torch.bfloat16) * 1.6)(Dm, H, Dh) * Dm ** (-0.5)), 'Wk': _w((lambda *s: torch.randn(*s, dtype=torch.bfloat16) * 1.6)(Dm, H, Dh) * Dm ** (-0.5)), 'Wv': _w((lambda *s: torch.randn(*s, dtype=torch.bfloat16) * 0.2)(Dm, H, Dh) * Dm ** (-0.5)), 'Wo': _w((lambda *s: torch.randn(*s, dtype=torch.bfloat16) * 0.2)(H, Dh, Dm) * (H * Dh) ** (-0.5)), 'seg_len': seg}
    _A_in = _A_setup()

    def _B_setup():
        B, S, D, F = (1, 2048, 640, 1728)
        return {'x': _w((lambda *s: torch.randn(*s, dtype=torch.bfloat16) * 0.05)(B, S, D)), 'w_gate': _w((lambda *s: torch.randn(*s, dtype=torch.bfloat16) * 0.05)(D, F)), 'w_up': _w((lambda *s: torch.randn(*s, dtype=torch.bfloat16) * 0.05)(D, F)), 'w_down': _w((lambda *s: torch.randn(*s, dtype=torch.bfloat16) * 0.05)(F, D))}
    _B_in = _B_setup()
    return {'A_x': _A_in['x'], 'A_mem0': _A_in['mem0'], 'A_Wq': _A_in['Wq'], 'A_Wk': _A_in['Wk'], 'A_Wv': _A_in['Wv'], 'A_Wo': _A_in['Wo'], 'A_seg_len': _A_in['seg_len'], 'B_x': _B_in['x'], 'B_w_gate': _B_in['w_gate'], 'B_w_up': _B_in['w_up'], 'B_w_down': _B_in['w_down']}

def reference(A_x, A_mem0, A_Wq, A_Wk, A_Wv, A_Wo, A_seg_len, B_x, B_w_gate, B_w_up, B_w_down):

    def _A_ref(x, mem0, Wq, Wk, Wv, Wo, seg_len):
        B, S, Dm = x.shape
        H, Dh = (Wq.shape[1], Wq.shape[2])
        M = mem0.shape[1]
        scale = Dh ** (-0.5)
        n_seg = S // seg_len
        L = M + seg_len
        idx = torch.arange(L)
        mask = (idx[:, None] < M) | (idx[:, None] >= idx[None, :])
        mem = mem0
        outs = []
        for s in range(n_seg):
            xs = x[:, s * seg_len:(s + 1) * seg_len, :].clone()
            h = torch.concatenate([mem, xs], axis=1)
            q = torch.einsum('btd,dhe->bhte', h, Wq)
            k = torch.einsum('btd,dhe->bhte', h, Wk)
            v = torch.einsum('btd,dhe->bhte', h, Wv)
            scores = torch.einsum('bhqe,bhke->bhqk', q, k) * scale
            scores = torch.where(mask[None, None], scores, -1000000000.0)
            p = _softmax(scores, axis=-1)
            o = torch.einsum('bhqk,bhke->bhqe', p, v)
            o = torch.einsum('bhte,hed->btd', o, Wo)
            mem = o[:, :M, :].clone()
            outs.append(o[:, M:, :])
        return torch.concatenate(outs, axis=1)

    def _B_ref(x, w_gate, w_up, w_down):
        g = _silu(x @ w_gate)
        u = x @ w_up
        return g * u @ w_down
    x = A_x
    h = x + _A_ref(_layernorm(x, axis=-1), A_mem0, A_Wq, A_Wk, A_Wv, A_Wo, A_seg_len)
    h2 = h + _B_ref(_layernorm(h, axis=-1), B_w_gate, B_w_up, B_w_down)
    return h2
```

```cpp
#include <hip/hip_runtime.h>
#include <stdint.h>

#define DEV __device__ __forceinline__
#define GAS __attribute__((address_space(1)))
#define LAS __attribute__((address_space(3)))

typedef __bf16 bf2 __attribute__((ext_vector_type(2)));
typedef float f2 __attribute__((ext_vector_type(2)));
typedef unsigned u2 __attribute__((ext_vector_type(2)));
typedef unsigned u4 __attribute__((ext_vector_type(4)));
typedef unsigned u8v __attribute__((ext_vector_type(8)));
typedef unsigned u16v __attribute__((ext_vector_type(16)));
typedef __amdgpu_buffer_rsrc_t rsrc_t;

constexpr int NSEG = 64;
constexpr int DM = 640;
constexpr int FF = 1728;

DEV unsigned o_slot(int h, int rg) { return (rg * 2 + ((h >> 3) & 1)) * 32768 + ((h >> 4) ^ 1) * 16384 + (h & 7) * 1024; }
DEV unsigned m_slot(int rg, int cg) { return 524288 + rg * 16384 + cg * 256; }
DEV unsigned ewb_word(int rg, int cg) { return 524288 + rg * 16384 + 5120 + cg * 128; }
DEV unsigned ewa_word(int h) { return 524288 + 32768 + (h >> 4) * 16384 + 5120 + (h & 15) * 128; }
constexpr unsigned EW_OX = 0, EW_H = 4096, EW_ACT = 4096 + 40 * 128, EW_LAST = EW_ACT + 27 * 128;
constexpr unsigned CTL_LN = 0xA0000;
constexpr unsigned CTL_QKV = 0xA4000;
constexpr unsigned CTL_ABORT = 0xA8000;
constexpr unsigned WS_INIT_BYTES = 0xB0000;
constexpr unsigned WS_CAL = 0x100000;
constexpr unsigned ST_EPOCH = 0x120000, ST_GO = ST_EPOCH + 256, ST_FLIP = ST_EPOCH + 128, ST_ARRIVE = ST_EPOCH + 4096, ST_CLAIM = ST_EPOCH + 8192;
constexpr unsigned WS_QKV = 2u << 20;
constexpr unsigned WS_OX = 50u << 20;
constexpr unsigned WS_H = 67u << 20;
constexpr unsigned WS_ACT = 71u << 20;
constexpr unsigned WS_XN = 84u << 20;
constexpr unsigned WS_END = 87u << 20;
constexpr unsigned OX_MT = 66 * 32768;

constexpr int LDS_BYTES = 163328;
constexpr unsigned SPIN_CAP = 400000u;
constexpr int SC1 = 16;
constexpr unsigned DROP = 0x80000000u;
#define LATE_START -1
#define LATE_WO -1

struct Params {
  const char* x; const char* mem0; const char* wq; const char* wk; const char* wv; const char* wo;
  const char* wg; const char* wu; const char* wd; char* out; char* ws;
};

DEV unsigned uni(unsigned v) { return (unsigned)__builtin_amdgcn_readfirstlane(v); }
DEV rsrc_t mk_rsrc(const void* p, unsigned bytes) { return __builtin_amdgcn_make_buffer_rsrc((void*)p, 0, bytes, 0x00020000); }
DEV u4 mk_rs(const void* q, unsigned bytes) { return (u4){uni((unsigned)(size_t)q), uni((unsigned)((size_t)q >> 32) & 0xffffu), bytes, 0x00020000u}; }
DEV u4 ld16(rsrc_t r, unsigned off, unsigned soff) { return __builtin_amdgcn_raw_buffer_load_b128(r, off, soff, 0); }
DEV u4 ld16c(rsrc_t r, unsigned off, unsigned soff) { return __builtin_amdgcn_raw_buffer_load_b128(r, off, soff, SC1); }
DEV void st16c(rsrc_t r, unsigned off, unsigned soff, u4 v) { unsigned o = off + soff; asm volatile("" : "+v"(o)); __builtin_amdgcn_raw_buffer_store_b128(v, r, o, 0, SC1); }
DEV void st8c(rsrc_t r, unsigned off, unsigned soff, u2 v) { __builtin_amdgcn_raw_buffer_store_b64(v, r, off, soff, SC1); }
DEV void st4c(rsrc_t r, unsigned off, unsigned soff, unsigned v) { __builtin_amdgcn_raw_buffer_store_b32(v, r, off, soff, SC1); }
DEV void drain() { asm volatile("s_waitcnt vmcnt(0)" ::: "memory"); }
DEV void bar() { asm volatile("s_waitcnt lgkmcnt(0)\n\ts_barrier" ::: "memory"); }
DEV void cfence() { asm volatile("" ::: "memory"); }
DEV unsigned pk(float a, float b) { f2 f = {a, b}; return __builtin_bit_cast(unsigned, __builtin_convertvector(f, bf2)); }
DEV float bflo(unsigned u) { return __builtin_bit_cast(float, u << 16); }
DEV float bfhi(unsigned u) { return __builtin_bit_cast(float, u & 0xffff0000u); }
template <int CTRL> DEV float dpp_add(float v) {
  return v + __builtin_bit_cast(float, __builtin_amdgcn_update_dpp(0u, __builtin_bit_cast(unsigned, v), CTRL, 0xF, 0xF, true));
}
DEV float sum8(float v) { return dpp_add<0x141>(dpp_add<0x4E>(dpp_add<0xB1>(v))); }
DEV unsigned ring_phys(unsigned o, unsigned flip) { return ((o >> 13) << 14) | (flip << 13) | (o & 8191u); }

template <int NL>
DEV bool wait_words(const char* wsp, unsigned off, int target) {
  const u4 rs = mk_rs(wsp, WS_END);
  unsigned n; int v;
  asm volatile(
      "s_mov_b64 exec, %[m]\n\ts_mov_b32 %[n], 0\n\ts_nop 2\n\t"
      "WL%=:\n\tbuffer_load_dword %[v], %[o], %[rs], 0 offen sc1\n\ts_add_u32 %[n], %[n], 1\n\ts_waitcnt vmcnt(0)\n\t"
      "v_cmp_gt_i32 vcc, %[t], %[v]\n\ts_cbranch_vccz WD%=\n\ts_sleep 2\n\ts_cmp_lt_u32 %[n], %[cap]\n\ts_cbranch_scc1 WL%=\n\t"
      "WD%=:\n\ts_mov_b64 exec, -1"
      : [n] "=&s"(n), [v] "=&v"(v)
      : [m] "s"(~0ull >> (64 - NL)), [o] "v"(off), [rs] "s"(rs), [t] "s"(target), [cap] "i"(SPIN_CAP)
      : "memory", "vcc", "scc");
  return n < SPIN_CAP;
}
DEV bool get_flip(rsrc_t ws, unsigned word, u2 tag, unsigned& flip) {
  for (unsigned spins = 0; spins < SPIN_CAP; ++spins) {
    cfence();
    const u2 v = __builtin_amdgcn_raw_buffer_load_b64(ws, word, 0, SC1);
    const unsigned lo = uni(v.x), hi = uni(v.y);
    if (lo == tag.x && ((hi ^ tag.y) << 2) == 0) { flip = ((hi ^ tag.y) >> 30) & 1; cfence(); return true; }
    __builtin_amdgcn_s_sleep(2);
  }
  return false;
}

enum { RM_NAT = 0, RM_HEAD = 1, RM_RED = 2 };
template <int RM> DEV int rowmap(int a, int g, int q) {
  if (RM == RM_NAT) return 8 * g + 4 * a + q;
  if (RM == RM_HEAD) return 16 * (q & 1) + 4 * (q >> 1) + 8 * (g >> 1) + 2 * (g & 1) + a;
  return 32 * (g >> 1) + 8 * (q >> 1) + 4 * (q & 1) + 2 * (g & 1) + a;
}
template <int RM> DEV u8v loader_regs(int lane, unsigned stride, unsigned stage, unsigned piece_mask) {
  const int g = lane >> 4, q = (lane >> 2) & 3, p = lane & 3, pc = p ^ (2 * (g & 1));
  u8v r;
  r[0] = rowmap<RM>(0, g, q) * stride + (pc & piece_mask) * 16;
  r[1] = rowmap<RM>(1, g, q) * stride + (pc & piece_mask) * 16 - 1024;
  r[2] = stage + (4 * g + q) * 64 + (g & 1) * 32 + 8 * p;
  r[3] = stage + (4 * g + q) * 64 + ((g & 1) ^ 1) * 32 + 8 * p;
  r[4] = stage + (4 * g + q) * 64 + 16 * pc;
  r[5] = 0; r[6] = 0; r[7] = 0;
  return r;
}

DEV bool do_ln(const Params& p, LAS char* lds, u2 tag, int seg) {
  const int lane = threadIdx.x & 63, w = uni(threadIdx.x >> 6);
  const rsrc_t ws = mk_rsrc(p.ws, WS_END);
  {
    const rsrc_t src = mk_rsrc(p.x, 2048 * DM * 2);
    const int lrow = lane >> 3, lp = lane & 7;
    u4 xin[10];
#pragma unroll
    for (int n = 0; n < 10; ++n) xin[n] = ld16(src, (8 * w + lrow) * (DM * 2) + lp * 16 + n * 128, seg * 32 * DM * 2);
    float s1 = 0.f;
#pragma unroll
    for (int n = 0; n < 10; ++n)
#pragma unroll
      for (int j = 0; j < 4; ++j) s1 += bflo(xin[n][j]) + bfhi(xin[n][j]);
    const float mean = sum8(s1) * (1.f / DM);
    float s2 = 0.f;
#pragma unroll
    for (int n = 0; n < 10; ++n)
#pragma unroll
      for (int j = 0; j < 4; ++j) { const float a = bflo(xin[n][j]) - mean, b = bfhi(xin[n][j]) - mean; s2 += a * a + b * b; }
    const float rstd = __builtin_amdgcn_rsqf(sum8(s2) * (1.f / DM) + 1e-5f);
    const unsigned tok = 8 * (w & 1) + lrow;
    const unsigned woff = (w >> 1) * 20480 + (lp >> 2) * 1024 + (lp & 3) * 256 + ((tok ^ (2 * (lp & 3) + (lp >> 2))) * 16);
#pragma unroll
    for (int n = 0; n < 10; ++n) {
      u4 o;
#pragma unroll
      for (int j = 0; j < 4; ++j) o[j] = pk((bflo(xin[n][j]) - mean) * rstd, (bfhi(xin[n][j]) - mean) * rstd);
      *(LAS u4*)(lds + woff + n * 2048) = o;
    }
    bar();
    const int g = lane >> 4, c = lane & 15;
#pragma unroll
    for (int n = 0; n < 10; ++n) {
      const int q = 4 * n + w;
      st16c(ws, WS_XN + q * 1024 + lane * 16, seg * 40960, *(LAS u4*)(lds + q * 1024 + g * 256 + ((c ^ (2 * g + (w & 1))) * 16)));
    }
  }
  unsigned unused;
  if (!get_flip(ws, ST_GO, tag, unused)) return false;
  drain();
  bar();
  if (threadIdx.x == 0) st4c(ws, CTL_LN + seg * 32, 0, tag.x & 0x7FFFFFFFu);
  return true;
}

DEV void role_qkv(const Params& p, LAS char* lds, u2 tag, int j) {
  const int lane = threadIdx.x & 63, w = uni(threadIdx.x >> 6);
  const int mat = (j < 32) ? (w >> 1) : 2, h = (j < 32) ? (j + (w >> 1)) & 31 : 2 * (j - 32) + (w >> 1);
  const unsigned lb = (unsigned)(size_t)lds, stage = lb + ((w < 3) ? 106496 + w * 16384 : 40960);
  u8v ad, ld = loader_regs<RM_NAT>(lane, 8192, stage, 3);
  ld[5] = CTL_LN + lane * 32;
  ad[0] = w * 1024 + lane * 16;
  ad[1] = lb + lane * 16;
  ad[2] = WS_QKV + h * (64 * 24576) + mat * 8192 + (w & 1) * ((j < 32) ? 2048 : 4096) + lane * 16;
  ad[4] = (lane == 0) ? 0u : DROP;
  ad[5] = 0;
  const u4 rs = mk_rs(p.ws, WS_END), rw = mk_rs((mat == 0) ? p.wq : (mat == 1) ? p.wk : p.wv, DM * 8192);
  const unsigned cb = uni(h * 256 + (w & 1) * 128), flo = uni(CTL_QKV + j * 128 + w * 4);
  if (j >= 32) {
    const int pm = (j - 32) * 4 + w;
    ad[3] = (pm < 45) ? WS_H + pm * 1024 + lane * 16 : DROP;
    ad[6] = WS_ACT + pm * 1024 + lane * 16;
    ad[7] = (pm < 44) ? WS_ACT + (64 + pm) * 1024 + lane * 16 : DROP;
    asm volatile(
      "s_lshl_b32 s54, %[w], 10\n\t"
      "s_mov_b32 s52, 88080384\n\t"
      "s_nop 4\n\t"
      "s_mov_b32 s39, m0\n\t"
      "s_cmp_eq_u32 %[w], 0\n\t"
      "s_cselect_b64 s[66:67], 1, 0\n\t"
      "s_add_u32 m0, %[st], 0\n\t"
      "s_add_u32 s43, %[cb], 0\n\t"
      "buffer_load_dwordx4 v16, %[rw], s43 offen lds\n\t"
      "buffer_load_dwordx4 v17, %[rw], s43 offen offset:1024 lds\n\t"
      "s_add_u32 m0, %[st], 2048\n\t"
      "s_add_u32 s43, %[cb], 64\n\t"
      "buffer_load_dwordx4 v16, %[rw], s43 offen lds\n\t"
      "buffer_load_dwordx4 v17, %[rw], s43 offen offset:1024 lds\n\t"
      "s_add_u32 m0, %[st], 4096\n\t"
      "s_add_u32 s43, %[cb], 262144\n\t"
      "buffer_load_dwordx4 v16, %[rw], s43 offen lds\n\t"
      "buffer_load_dwordx4 v17, %[rw], s43 offen offset:1024 lds\n\t"
      "s_add_u32 m0, %[st], 6144\n\t"
      "s_add_u32 s43, %[cb], 262208\n\t"
      "buffer_load_dwordx4 v16, %[rw], s43 offen lds\n\t"
      "buffer_load_dwordx4 v17, %[rw], s43 offen offset:1024 lds\n\t"
      "s_add_u32 m0, %[st], 8192\n\t"
      "s_add_u32 s43, %[cb], 524288\n\t"
      "buffer_load_dwordx4 v16, %[rw], s43 offen lds\n\t"
      "buffer_load_dwordx4 v17, %[rw], s43 offen offset:1024 lds\n\t"
      "s_add_u32 m0, %[st], 10240\n\t"
      "s_add_u32 s43, %[cb], 524352\n\t"
      "buffer_load_dwordx4 v16, %[rw], s43 offen lds\n\t"
      "buffer_load_dwordx4 v17, %[rw], s43 offen offset:1024 lds\n\t"
      "s_add_u32 m0, %[st], 12288\n\t"
      "s_add_u32 s43, %[cb], 786432\n\t"
      "buffer_load_dwordx4 v16, %[rw], s43 offen lds\n\t"
      "buffer_load_dwordx4 v17, %[rw], s43 offen offset:1024 lds\n\t"
      "s_add_u32 m0, %[st], 14336\n\t"
      "s_add_u32 s43, %[cb], 786496\n\t"
      "buffer_load_dwordx4 v16, %[rw], s43 offen lds\n\t"
      "buffer_load_dwordx4 v17, %[rw], s43 offen offset:1024 lds\n\t"
      "s_waitcnt vmcnt(12)\n\t"
      "ds_read_b64_tr_b16 a[0:1], v20 offset:0\n\t"
      "ds_read_b64_tr_b16 a[2:3], v20 offset:1024\n\t"
      "ds_read_b64_tr_b16 a[4:5], v20 offset:8\n\t"
      "ds_read_b64_tr_b16 a[6:7], v20 offset:1032\n\t"
      "ds_read_b64_tr_b16 a[8:9], v20 offset:2048\n\t"
      "ds_read_b64_tr_b16 a[10:11], v20 offset:3072\n\t"
      "ds_read_b64_tr_b16 a[12:13], v20 offset:2056\n\t"
      "ds_read_b64_tr_b16 a[14:15], v20 offset:3080\n\t"
      "s_waitcnt lgkmcnt(0)\n\t"
      "s_add_u32 m0, %[st], 0\n\t"
      "s_add_u32 s43, %[cb], 1048576\n\t"
      "buffer_load_dwordx4 v16, %[rw], s43 offen lds\n\t"
      "buffer_load_dwordx4 v17, %[rw], s43 offen offset:1024 lds\n\t"
      "s_add_u32 m0, %[st], 2048\n\t"
      "s_add_u32 s43, %[cb], 1048640\n\t"
      "buffer_load_dwordx4 v16, %[rw], s43 offen lds\n\t"
      "buffer_load_dwordx4 v17, %[rw], s43 offen offset:1024 lds\n\t"
      "s_waitcnt vmcnt(12)\n\t"
      "ds_read_b64_tr_b16 a[16:17], v20 offset:4096\n\t"
      "ds_read_b64_tr_b16 a[18:19], v20 offset:5120\n\t"
      "ds_read_b64_tr_b16 a[20:21], v20 offset:4104\n\t"
      "ds_read_b64_tr_b16 a[22:23], v20 offset:5128\n\t"
      "ds_read_b64_tr_b16 a[24:25], v20 offset:6144\n\t"
      "ds_read_b64_tr_b16 a[26:27], v20 offset:7168\n\t"
      "ds_read_b64_tr_b16 a[28:29], v20 offset:6152\n\t"
      "ds_read_b64_tr_b16 a[30:31], v20 offset:7176\n\t"
      "s_waitcnt lgkmcnt(0)\n\t"
      "s_add_u32 m0, %[st], 4096\n\t"
      "s_add_u32 s43, %[cb], 1310720\n\t"
      "buffer_load_dwordx4 v16, %[rw], s43 offen lds\n\t"
      "buffer_load_dwordx4 v17, %[rw], s43 offen offset:1024 lds\n\t"
      "s_add_u32 m0, %[st], 6144\n\t"
      "s_add_u32 s43, %[cb], 1310784\n\t"
      "buffer_load_dwordx4 v16, %[rw], s43 offen lds\n\t"
      "buffer_load_dwordx4 v17, %[rw], s43 offen offset:1024 lds\n\t"
      "s_waitcnt vmcnt(12)\n\t"
      "ds_read_b64_tr_b16 a[32:33], v20 offset:8192\n\t"
      "ds_read_b64_tr_b16 a[34:35], v20 offset:9216\n\t"
      "ds_read_b64_tr_b16 a[36:37], v20 offset:8200\n\t"
      "ds_read_b64_tr_b16 a[38:39], v20 offset:9224\n\t"
      "ds_read_b64_tr_b16 a[40:41], v20 offset:10240\n\t"
      "ds_read_b64_tr_b16 a[42:43], v20 offset:11264\n\t"
      "ds_read_b64_tr_b16 a[44:45], v20 offset:10248\n\t"
      "ds_read_b64_tr_b16 a[46:47], v20 offset:11272\n\t"
      "s_waitcnt lgkmcnt(0)\n\t"
      "s_add_u32 m0, %[st], 8192\n\t"
      "s_add_u32 s43, %[cb], 1572864\n\t"
      "buffer_load_dwordx4 v16, %[rw], s43 offen lds\n\t"
      "buffer_load_dwordx4 v17, %[rw], s43 offen offset:1024 lds\n\t"
      "s_add_u32 m0, %[st], 10240\n\t"
      "s_add_u32 s43, %[cb], 1572928\n\t"
      "buffer_load_dwordx4 v16, %[rw], s43 offen lds\n\t"
      "buffer_load_dwordx4 v17, %[rw], s43 offen offset:1024 lds\n\t"
      "s_waitcnt vmcnt(12)\n\t"
      "ds_read_b64_tr_b16 a[48:49], v20 offset:12288\n\t"
      "ds_read_b64_tr_b16 a[50:51], v20 offset:13312\n\t"
      "ds_read_b64_tr_b16 a[52:53], v20 offset:12296\n\t"
      "ds_read_b64_tr_b16 a[54:55], v20 offset:13320\n\t"
      "ds_read_b64_tr_b16 a[56:57], v20 offset:14336\n\t"
      "ds_read_b64_tr_b16 a[58:59], v20 offset:15360\n\t"
      "ds_read_b64_tr_b16 a[60:61], v20 offset:14344\n\t"
      "ds_read_b64_tr_b16 a[62:63], v20 offset:15368\n\t"
      "s_waitcnt lgkmcnt(0)\n\t"
      "s_add_u32 m0, %[st], 12288\n\t"
      "s_add_u32 s43, %[cb], 1835008\n\t"
      "buffer_load_dwordx4 v16, %[rw], s43 offen lds\n\t"
      "buffer_load_dwordx4 v17, %[rw], s43 offen offset:1024 lds\n\t"
      "s_add_u32 m0, %[st], 14336\n\t"
      "s_add_u32 s43, %[cb], 1835072\n\t"
      "buffer_load_dwordx4 v16, %[rw], s43 offen lds\n\t"
      "buffer_load_dwordx4 v17, %[rw], s43 offen offset:1024 lds\n\t"
      "s_waitcnt vmcnt(12)\n\t"
      "ds_read_b64_tr_b16 a[64:65], v20 offset:0\n\t"
      "ds_read_b64_tr_b16 a[66:67], v20 offset:1024\n\t"
      "ds_read_b64_tr_b16 a[68:69], v20 offset:8\n\t"
      "ds_read_b64_tr_b16 a[70:71], v20 offset:1032\n\t"
      "ds_read_b64_tr_b16 a[72:73], v20 offset:2048\n\t"
      "ds_read_b64_tr_b16 a[74:75], v20 offset:3072\n\t"
      "ds_read_b64_tr_b16 a[76:77], v20 offset:2056\n\t"
      "ds_read_b64_tr_b16 a[78:79], v20 offset:3080\n\t"
      "s_waitcnt lgkmcnt(0)\n\t"
      "s_add_u32 m0, %[st], 0\n\t"
      "s_add_u32 s43, %[cb], 2097152\n\t"
      "buffer_load_dwordx4 v16, %[rw], s43 offen lds\n\t"
      "buffer_load_dwordx4 v17, %[rw], s43 offen offset:1024 lds\n\t"
      "s_add_u32 m0, %[st], 2048\n\t"
      "s_add_u32 s43, %[cb], 2097216\n\t"
      "buffer_load_dwordx4 v16, %[rw], s43 offen lds\n\t"
      "buffer_load_dwordx4 v17, %[rw], s43 offen offset:1024 lds\n\t"
      "s_waitcnt vmcnt(12)\n\t"
      "ds_read_b64_tr_b16 a[80:81], v20 offset:4096\n\t"
      "ds_read_b64_tr_b16 a[82:83], v20 offset:5120\n\t"
      "ds_read_b64_tr_b16 a[84:85], v20 offset:4104\n\t"
      "ds_read_b64_tr_b16 a[86:87], v20 offset:5128\n\t"
      "ds_read_b64_tr_b16 a[88:89], v20 offset:6144\n\t"
      "ds_read_b64_tr_b16 a[90:91], v20 offset:7168\n\t"
      "ds_read_b64_tr_b16 a[92:93], v20 offset:6152\n\t"
      "ds_read_b64_tr_b16 a[94:95], v20 offset:7176\n\t"
      "s_waitcnt lgkmcnt(0)\n\t"
      "s_add_u32 m0, %[st], 4096\n\t"
      "s_add_u32 s43, %[cb], 2359296\n\t"
      "buffer_load_dwordx4 v16, %[rw], s43 offen lds\n\t"
      "buffer_load_dwordx4 v17, %[rw], s43 offen offset:1024 lds\n\t"
      "s_add_u32 m0, %[st], 6144\n\t"
      "s_add_u32 s43, %[cb], 2359360\n\t"
      "buffer_load_dwordx4 v16, %[rw], s43 offen lds\n\t"
      "buffer_load_dwordx4 v17, %[rw], s43 offen offset:1024 lds\n\t"
      "s_waitcnt vmcnt(12)\n\t"
      "ds_read_b64_tr_b16 a[96:97], v20 offset:8192\n\t"
      "ds_read_b64_tr_b16 a[98:99], v20 offset:9216\n\t"
      "ds_read_b64_tr_b16 a[100:101], v20 offset:8200\n\t"
      "ds_read_b64_tr_b16 a[102:103], v20 offset:9224\n\t"
      "ds_read_b64_tr_b16 a[104:105], v20 offset:10240\n\t"
      "ds_read_b64_tr_b16 a[106:107], v20 offset:11264\n\t"
      "ds_read_b64_tr_b16 a[108:109], v20 offset:10248\n\t"
      "ds_read_b64_tr_b16 a[110:111], v20 offset:11272\n\t"
      "s_waitcnt lgkmcnt(0)\n\t"
      "s_add_u32 m0, %[st], 8192\n\t"
      "s_add_u32 s43, %[cb], 2621440\n\t"
      "buffer_load_dwordx4 v16, %[rw], s43 offen lds\n\t"
      "buffer_load_dwordx4 v17, %[rw], s43 offen offset:1024 lds\n\t"
      "s_add_u32 m0, %[st], 10240\n\t"
      "s_add_u32 s43, %[cb], 2621504\n\t"
      "buffer_load_dwordx4 v16, %[rw], s43 offen lds\n\t"
      "buffer_load_dwordx4 v17, %[rw], s43 offen offset:1024 lds\n\t"
      "s_waitcnt vmcnt(12)\n\t"
      "ds_read_b64_tr_b16 a[112:113], v20 offset:12288\n\t"
      "ds_read_b64_tr_b16 a[114:115], v20 offset:13312\n\t"
      "ds_read_b64_tr_b16 a[116:117], v20 offset:12296\n\t"
      "ds_read_b64_tr_b16 a[118:119], v20 offset:13320\n\t"
      "ds_read_b64_tr_b16 a[120:121], v20 offset:14336\n\t"
      "ds_read_b64_tr_b16 a[122:123], v20 offset:15360\n\t"
      "ds_read_b64_tr_b16 a[124:125], v20 offset:14344\n\t"
      "ds_read_b64_tr_b16 a[126:127], v20 offset:15368\n\t"
      "s_waitcnt lgkmcnt(0)\n\t"
      "s_add_u32 m0, %[st], 12288\n\t"
      "s_add_u32 s43, %[cb], 2883584\n\t"
      "buffer_load_dwordx4 v16, %[rw], s43 offen lds\n\t"
      "buffer_load_dwordx4 v17, %[rw], s43 offen offset:1024 lds\n\t"
      "s_add_u32 m0, %[st], 14336\n\t"
      "s_add_u32 s43, %[cb], 2883648\n\t"
      "buffer_load_dwordx4 v16, %[rw], s43 offen lds\n\t"
      "buffer_load_dwordx4 v17, %[rw], s43 offen offset:1024 lds\n\t"
      "s_waitcnt vmcnt(12)\n\t"
      "ds_read_b64_tr_b16 a[128:129], v20 offset:0\n\t"
      "ds_read_b64_tr_b16 a[130:131], v20 offset:1024\n\t"
      "ds_read_b64_tr_b16 a[132:133], v20 offset:8\n\t"
      "ds_read_b64_tr_b16 a[134:135], v20 offset:1032\n\t"
      "ds_read_b64_tr_b16 a[136:137], v20 offset:2048\n\t"
      "ds_read_b64_tr_b16 a[138:139], v20 offset:3072\n\t"
      "ds_read_b64_tr_b16 a[140:141], v20 offset:2056\n\t"
      "ds_read_b64_tr_b16 a[142:143], v20 offset:3080\n\t"
      "s_waitcnt lgkmcnt(0)\n\t"
      "s_add_u32 m0, %[st], 0\n\t"
      "s_add_u32 s43, %[cb], 3145728\n\t"
      "buffer_load_dwordx4 v16, %[rw], s43 offen lds\n\t"
      "buffer_load_dwordx4 v17, %[rw], s43 offen offset:1024 lds\n\t"
      "s_add_u32 m0, %[st], 2048\n\t"
      "s_add_u32 s43, %[cb], 3145792\n\t"
      "buffer_load_dwordx4 v16, %[rw], s43 offen lds\n\t"
      "buffer_load_dwordx4 v17, %[rw], s43 offen offset:1024 lds\n\t"
      "s_waitcnt vmcnt(12)\n\t"
      "ds_read_b64_tr_b16 a[144:145], v20 offset:4096\n\t"
      "ds_read_b64_tr_b16 a[146:147], v20 offset:5120\n\t"
      "ds_read_b64_tr_b16 a[148:149], v20 offset:4104\n\t"
      "ds_read_b64_tr_b16 a[150:151], v20 offset:5128\n\t"
      "ds_read_b64_tr_b16 a[152:153], v20 offset:6144\n\t"
      "ds_read_b64_tr_b16 a[154:155], v20 offset:7168\n\t"
      "ds_read_b64_tr_b16 a[156:157], v20 offset:6152\n\t"
      "ds_read_b64_tr_b16 a[158:159], v20 offset:7176\n\t"
      "s_waitcnt lgkmcnt(0)\n\t"
      "s_add_u32 m0, %[st], 4096\n\t"
      "s_add_u32 s43, %[cb], 3407872\n\t"
      "buffer_load_dwordx4 v16, %[rw], s43 offen lds\n\t"
      "buffer_load_dwordx4 v17, %[rw], s43 offen offset:1024 lds\n\t"
      "s_add_u32 m0, %[st], 6144\n\t"
      "s_add_u32 s43, %[cb], 3407936\n\t"
      "buffer_load_dwordx4 v16, %[rw], s43 offen lds\n\t"
      "buffer_load_dwordx4 v17, %[rw], s43 offen offset:1024 lds\n\t"
      "s_waitcnt vmcnt(12)\n\t"
      "ds_read_b64_tr_b16 a[160:161], v20 offset:8192\n\t"
      "ds_read_b64_tr_b16 a[162:163], v20 offset:9216\n\t"
      "ds_read_b64_tr_b16 a[164:165], v20 offset:8200\n\t"
      "ds_read_b64_tr_b16 a[166:167], v20 offset:9224\n\t"
      "ds_read_b64_tr_b16 a[168:169], v20 offset:10240\n\t"
      "ds_read_b64_tr_b16 a[170:171], v20 offset:11264\n\t"
      "ds_read_b64_tr_b16 a[172:173], v20 offset:10248\n\t"
      "ds_read_b64_tr_b16 a[174:175], v20 offset:11272\n\t"
      "s_waitcnt lgkmcnt(0)\n\t"
      "s_add_u32 m0, %[st], 8192\n\t"
      "s_add_u32 s43, %[cb], 3670016\n\t"
      "buffer_load_dwordx4 v16, %[rw], s43 offen lds\n\t"
      "buffer_load_dwordx4 v17, %[rw], s43 offen offset:1024 lds\n\t"
      "s_add_u32 m0, %[st], 10240\n\t"
      "s_add_u32 s43, %[cb], 3670080\n\t"
      "buffer_load_dwordx4 v16, %[rw], s43 offen lds\n\t"
      "buffer_load_dwordx4 v17, %[rw], s43 offen offset:1024 lds\n\t"
      "buffer_load_dword v26, v21, %[rs], 0 offen sc1\n\t"
      "s_waitcnt vmcnt(13)\n\t"
      "ds_read_b64_tr_b16 a[176:177], v20 offset:12288\n\t"
      "ds_read_b64_tr_b16 a[178:179], v20 offset:13312\n\t"
      "ds_read_b64_tr_b16 a[180:181], v20 offset:12296\n\t"
      "ds_read_b64_tr_b16 a[182:183], v20 offset:13320\n\t"
      "ds_read_b64_tr_b16 a[184:185], v20 offset:14336\n\t"
      "ds_read_b64_tr_b16 a[186:187], v20 offset:15360\n\t"
      "ds_read_b64_tr_b16 a[188:189], v20 offset:14344\n\t"
      "ds_read_b64_tr_b16 a[190:191], v20 offset:15368\n\t"
      "s_waitcnt lgkmcnt(0)\n\t"
      "s_add_u32 m0, %[st], 12288\n\t"
      "s_add_u32 s43, %[cb], 3932160\n\t"
      "buffer_load_dwordx4 v16, %[rw], s43 offen lds\n\t"
      "buffer_load_dwordx4 v17, %[rw], s43 offen offset:1024 lds\n\t"
      "s_add_u32 m0, %[st], 14336\n\t"
      "s_add_u32 s43, %[cb], 3932224\n\t"
      "buffer_load_dwordx4 v16, %[rw], s43 offen lds\n\t"
      "buffer_load_dwordx4 v17, %[rw], s43 offen offset:1024 lds\n\t"
      "s_waitcnt vmcnt(13)\n\t"
      "ds_read_b64_tr_b16 a[192:193], v20 offset:0\n\t"
      "ds_read_b64_tr_b16 a[194:195], v20 offset:1024\n\t"
      "ds_read_b64_tr_b16 a[196:197], v20 offset:8\n\t"
      "ds_read_b64_tr_b16 a[198:199], v20 offset:1032\n\t"
      "ds_read_b64_tr_b16 a[200:201], v20 offset:2048\n\t"
      "ds_read_b64_tr_b16 a[202:203], v20 offset:3072\n\t"
      "ds_read_b64_tr_b16 a[204:205], v20 offset:2056\n\t"
      "ds_read_b64_tr_b16 a[206:207], v20 offset:3080\n\t"
      "s_waitcnt lgkmcnt(0)\n\t"
      "s_add_u32 m0, %[st], 0\n\t"
      "s_add_u32 s43, %[cb], 4194304\n\t"
      "buffer_load_dwordx4 v16, %[rw], s43 offen lds\n\t"
      "buffer_load_dwordx4 v17, %[rw], s43 offen offset:1024 lds\n\t"
      "s_add_u32 m0, %[st], 2048\n\t"
      "s_add_u32 s43, %[cb], 4194368\n\t"
      "buffer_load_dwordx4 v16, %[rw], s43 offen lds\n\t"
      "buffer_load_dwordx4 v17, %[rw], s43 offen offset:1024 lds\n\t"
      "s_waitcnt vmcnt(13)\n\t"
      "ds_read_b64_tr_b16 a[208:209], v20 offset:4096\n\t"
      "ds_read_b64_tr_b16 a[210:211], v20 offset:5120\n\t"
      "ds_read_b64_tr_b16 a[212:213], v20 offset:4104\n\t"
      "ds_read_b64_tr_b16 a[214:215], v20 offset:5128\n\t"
      "ds_read_b64_tr_b16 a[216:217], v20 offset:6144\n\t"
      "ds_read_b64_tr_b16 a[218:219], v20 offset:7168\n\t"
      "ds_read_b64_tr_b16 a[220:221], v20 offset:6152\n\t"
      "ds_read_b64_tr_b16 a[222:223], v20 offset:7176\n\t"
      "s_waitcnt lgkmcnt(0)\n\t"
      "s_add_u32 m0, %[st], 4096\n\t"
      "s_add_u32 s43, %[cb], 4456448\n\t"
      "buffer_load_dwordx4 v16, %[rw], s43 offen lds\n\t"
      "buffer_load_dwordx4 v17, %[rw], s43 offen offset:1024 lds\n\t"
      "s_add_u32 m0, %[st], 6144\n\t"
      "s_add_u32 s43, %[cb], 4456512\n\t"
      "buffer_load_dwordx4 v16, %[rw], s43 offen lds\n\t"
      "buffer_load_dwordx4 v17, %[rw], s43 offen offset:1024 lds\n\t"
      "s_waitcnt vmcnt(13)\n\t"
      "ds_read_b64_tr_b16 a[224:225], v20 offset:8192\n\t"
      "ds_read_b64_tr_b16 a[226:227], v20 offset:9216\n\t"
      "ds_read_b64_tr_b16 a[228:229], v20 offset:8200\n\t"
      "ds_read_b64_tr_b16 a[230:231], v20 offset:9224\n\t"
      "ds_read_b64_tr_b16 a[232:233], v20 offset:10240\n\t"
      "ds_read_b64_tr_b16 a[234:235], v20 offset:11264\n\t"
      "ds_read_b64_tr_b16 a[236:237], v20 offset:10248\n\t"
      "ds_read_b64_tr_b16 a[238:239], v20 offset:11272\n\t"
      "s_waitcnt lgkmcnt(0)\n\t"
      "s_add_u32 m0, %[st], 8192\n\t"
      "s_add_u32 s43, %[cb], 4718592\n\t"
      "buffer_load_dwordx4 v16, %[rw], s43 offen lds\n\t"
      "buffer_load_dwordx4 v17, %[rw], s43 offen offset:1024 lds\n\t"
      "s_add_u32 m0, %[st], 10240\n\t"
      "s_add_u32 s43, %[cb], 4718656\n\t"
      "buffer_load_dwordx4 v16, %[rw], s43 offen lds\n\t"
      "buffer_load_dwordx4 v17, %[rw], s43 offen offset:1024 lds\n\t"
      "s_waitcnt vmcnt(12)\n\t"
      "ds_read_b64_tr_b16 a[240:241], v20 offset:12288\n\t"
      "ds_read_b64_tr_b16 a[242:243], v20 offset:13312\n\t"
      "ds_read_b64_tr_b16 a[244:245], v20 offset:12296\n\t"
      "ds_read_b64_tr_b16 a[246:247], v20 offset:13320\n\t"
      "ds_read_b64_tr_b16 a[248:249], v20 offset:14336\n\t"
      "ds_read_b64_tr_b16 a[250:251], v20 offset:15360\n\t"
      "ds_read_b64_tr_b16 a[252:253], v20 offset:14344\n\t"
      "ds_read_b64_tr_b16 a[254:255], v20 offset:15368\n\t"
      "s_waitcnt lgkmcnt(0)\n\t"
      "s_add_u32 m0, %[st], 12288\n\t"
      "s_add_u32 s43, %[cb], 4980736\n\t"
      "buffer_load_dwordx4 v16, %[rw], s43 offen lds\n\t"
      "buffer_load_dwordx4 v17, %[rw], s43 offen offset:1024 lds\n\t"
      "s_add_u32 m0, %[st], 14336\n\t"
      "s_add_u32 s43, %[cb], 4980800\n\t"
      "buffer_load_dwordx4 v16, %[rw], s43 offen lds\n\t"
      "buffer_load_dwordx4 v17, %[rw], s43 offen offset:1024 lds\n\t"
      "v_cmp_ne_u32 vcc, %[t0], v26\n\t"
      "s_add_u32 m0, s54, 0\n\t"
      "s_add_u32 s43, s52, 0\n\t"
      "buffer_load_dwordx4 v0, %[rs], s43 offen sc1 lds\n\t"
      "s_add_u32 m0, s54, 4096\n\t"
      "s_add_u32 s43, s52, 4096\n\t"
      "buffer_load_dwordx4 v0, %[rs], s43 offen sc1 lds\n\t"
      "s_add_u32 m0, s54, 8192\n\t"
      "s_add_u32 s43, s52, 8192\n\t"
      "buffer_load_dwordx4 v0, %[rs], s43 offen sc1 lds\n\t"
      "s_add_u32 m0, s54, 12288\n\t"
      "s_add_u32 s43, s52, 12288\n\t"
      "buffer_load_dwordx4 v0, %[rs], s43 offen sc1 lds\n\t"
      "s_add_u32 m0, s54, 16384\n\t"
      "s_add_u32 s43, s52, 16384\n\t"
      "buffer_load_dwordx4 v0, %[rs], s43 offen sc1 lds\n\t"
      "s_add_u32 m0, s54, 20480\n\t"
      "s_add_u32 s43, s52, 20480\n\t"
      "buffer_load_dwordx4 v0, %[rs], s43 offen sc1 lds\n\t"
      "s_add_u32 m0, s54, 24576\n\t"
      "s_add_u32 s43, s52, 24576\n\t"
      "buffer_load_dwordx4 v0, %[rs], s43 offen sc1 lds\n\t"
      "s_add_u32 m0, s54, 28672\n\t"
      "s_add_u32 s43, s52, 28672\n\t"
      "buffer_load_dwordx4 v0, %[rs], s43 offen sc1 lds\n\t"
      "s_add_u32 m0, s54, 32768\n\t"
      "s_add_u32 s43, s52, 32768\n\t"
      "buffer_load_dwordx4 v0, %[rs], s43 offen sc1 lds\n\t"
      "s_add_u32 m0, s54, 36864\n\t"
      "s_add_u32 s43, s52, 36864\n\t"
      "buffer_load_dwordx4 v0, %[rs], s43 offen sc1 lds\n\t"
      "s_waitcnt vmcnt(22)\n\t"
      "ds_read_b64_tr_b16 v[128:129], v20 offset:0\n\t"
      "ds_read_b64_tr_b16 v[130:131], v20 offset:1024\n\t"
      "ds_read_b64_tr_b16 v[132:133], v20 offset:8\n\t"
      "ds_read_b64_tr_b16 v[134:135], v20 offset:1032\n\t"
      "ds_read_b64_tr_b16 v[136:137], v20 offset:2048\n\t"
      "ds_read_b64_tr_b16 v[138:139], v20 offset:3072\n\t"
      "ds_read_b64_tr_b16 v[140:141], v20 offset:2056\n\t"
      "ds_read_b64_tr_b16 v[142:143], v20 offset:3080\n\t"
      "s_waitcnt lgkmcnt(0)\n\t"
      "s_waitcnt vmcnt(18)\n\t"
      "ds_read_b64_tr_b16 v[144:145], v20 offset:4096\n\t"
      "ds_read_b64_tr_b16 v[146:147], v20 offset:5120\n\t"
      "ds_read_b64_tr_b16 v[148:149], v20 offset:4104\n\t"
      "ds_read_b64_tr_b16 v[150:151], v20 offset:5128\n\t"
      "ds_read_b64_tr_b16 v[152:153], v20 offset:6144\n\t"
      "ds_read_b64_tr_b16 v[154:155], v20 offset:7168\n\t"
      "ds_read_b64_tr_b16 v[156:157], v20 offset:6152\n\t"
      "ds_read_b64_tr_b16 v[158:159], v20 offset:7176\n\t"
      "s_waitcnt lgkmcnt(0)\n\t"
      "s_waitcnt vmcnt(14)\n\t"
      "ds_read_b64_tr_b16 v[160:161], v20 offset:8192\n\t"
      "ds_read_b64_tr_b16 v[162:163], v20 offset:9216\n\t"
      "ds_read_b64_tr_b16 v[164:165], v20 offset:8200\n\t"
      "ds_read_b64_tr_b16 v[166:167], v20 offset:9224\n\t"
      "ds_read_b64_tr_b16 v[168:169], v20 offset:10240\n\t"
      "ds_read_b64_tr_b16 v[170:171], v20 offset:11264\n\t"
      "ds_read_b64_tr_b16 v[172:173], v20 offset:10248\n\t"
      "ds_read_b64_tr_b16 v[174:175], v20 offset:11272\n\t"
      "s_waitcnt lgkmcnt(0)\n\t"
      "s_waitcnt vmcnt(10)\n\t"
      "ds_read_b64_tr_b16 v[176:177], v20 offset:12288\n\t"
      "ds_read_b64_tr_b16 v[178:179], v20 offset:13312\n\t"
      "ds_read_b64_tr_b16 v[180:181], v20 offset:12296\n\t"
      "ds_read_b64_tr_b16 v[182:183], v20 offset:13320\n\t"
      "ds_read_b64_tr_b16 v[184:185], v20 offset:14336\n\t"
      "ds_read_b64_tr_b16 v[186:187], v20 offset:15360\n\t"
      "ds_read_b64_tr_b16 v[188:189], v20 offset:14344\n\t"
      "ds_read_b64_tr_b16 v[190:191], v20 offset:15368\n\t"
      "s_waitcnt lgkmcnt(0)\n\t"
      "s_waitcnt lgkmcnt(0)\n\t"
      "s_barrier\n\t"
      "s_cbranch_vccz LOK%=\n\t"
      "s_mov_b32 s42, 0\n\t"
      "LLN%=:\n\t"
      "buffer_load_dword v24, v21, %[rs], 0 offen sc1\n\t"
      "s_add_u32 s42, s42, 1\n\t"
      "s_cmp_gt_u32 s42, 400000\n\t"
      "s_cbranch_scc1 LFAIL%=\n\t"
      "s_waitcnt vmcnt(0)\n\t"
      "v_cmp_ne_u32 vcc, %[t0], v24\n\t"
      "s_cbranch_vccnz LLN%=\n\t"
      "s_add_u32 m0, s54, 0\n\t"
      "s_add_u32 s43, s52, 0\n\t"
      "buffer_load_dwordx4 v0, %[rs], s43 offen sc1 lds\n\t"
      "s_add_u32 m0, s54, 4096\n\t"
      "s_add_u32 s43, s52, 4096\n\t"
      "buffer_load_dwordx4 v0, %[rs], s43 offen sc1 lds\n\t"
      "s_add_u32 m0, s54, 8192\n\t"
      "s_add_u32 s43, s52, 8192\n\t"
      "buffer_load_dwordx4 v0, %[rs], s43 offen sc1 lds\n\t"
      "s_add_u32 m0, s54, 12288\n\t"
      "s_add_u32 s43, s52, 12288\n\t"
      "buffer_load_dwordx4 v0, %[rs], s43 offen sc1 lds\n\t"
      "s_add_u32 m0, s54, 16384\n\t"
      "s_add_u32 s43, s52, 16384\n\t"
      "buffer_load_dwordx4 v0, %[rs], s43 offen sc1 lds\n\t"
      "s_add_u32 m0, s54, 20480\n\t"
      "s_add_u32 s43, s52, 20480\n\t"
      "buffer_load_dwordx4 v0, %[rs], s43 offen sc1 lds\n\t"
      "s_add_u32 m0, s54, 24576\n\t"
      "s_add_u32 s43, s52, 24576\n\t"
      "buffer_load_dwordx4 v0, %[rs], s43 offen sc1 lds\n\t"
      "s_add_u32 m0, s54, 28672\n\t"
      "s_add_u32 s43, s52, 28672\n\t"
      "buffer_load_dwordx4 v0, %[rs], s43 offen sc1 lds\n\t"
      "s_add_u32 m0, s54, 32768\n\t"
      "s_add_u32 s43, s52, 32768\n\t"
      "buffer_load_dwordx4 v0, %[rs], s43 offen sc1 lds\n\t"
      "s_add_u32 m0, s54, 36864\n\t"
      "s_add_u32 s43, s52, 36864\n\t"
      "buffer_load_dwordx4 v0, %[rs], s43 offen sc1 lds\n\t"
      "LOK%=:\n\t"
      "s_mov_b32 s40, 0\n\t"
      "s_mov_b32 s53, 0\n\t"
      "v_mov_b64 v[8:9], -1\n\t"
      "v_mov_b64 v[10:11], -1\n\t"
      "s_waitcnt vmcnt(0)\n\t"
      "s_barrier\n\t"
      "ds_read_b128 v[56:59], v1 offset:0\n\t"
      "ds_read_b128 v[60:63], v1 offset:20480\n\t"
      "ds_read_b128 v[64:67], v1 offset:1024\n\t"
      "ds_read_b128 v[68:71], v1 offset:21504\n\t"
      "s_waitcnt lgkmcnt(3)\n\t"
      "v_mfma_f32_16x16x32_bf16 v[24:27], v[56:59], a[0:3], 0\n\t"
      "ds_read_b128 v[72:75], v1 offset:2048\n\t"
      "v_mfma_f32_16x16x32_bf16 v[32:35], v[56:59], a[4:7], 0\n\t"
      "ds_read_b128 v[76:79], v1 offset:22528\n\t"
      "v_mfma_f32_16x16x32_bf16 v[40:43], v[56:59], a[8:11], 0\n\t"
      "s_xor_b32 s54, s54, 65536\n\t"
      "s_add_u32 s43, s40, 1\n\t"
      "v_mfma_f32_16x16x32_bf16 v[48:51], v[56:59], a[12:15], 0\n\t"
      "s_min_u32 s43, s43, 63\n\t"
      "s_mul_i32 s43, s43, 40960\n\t"
      "s_waitcnt lgkmcnt(4)\n\t"
      "v_mfma_f32_16x16x32_bf16 v[28:31], v[60:63], a[0:3], 0\n\t"
      "s_add_u32 s52, s43, 88080384\n\t"
      "s_mul_i32 s57, s40, %[ms]\n\t"
      "v_mfma_f32_16x16x32_bf16 v[36:39], v[60:63], a[4:7], 0\n\t"
      "s_mul_i32 s58, s40, 110592\n\t"
      "s_sub_u32 s56, s40, 1\n\t"
      "v_mfma_f32_16x16x32_bf16 v[44:47], v[60:63], a[8:11], 0\n\t"
      "s_mul_i32 s53, s56, 24576\n\t"
      "s_add_u32 s55, s53, 4096\n\t"
      "s_waitcnt lgkmcnt(2)\n\t"
      "v_mfma_f32_16x16x32_bf16 v[52:55], v[60:63], a[12:15], 0\n\t"
      "v_mov_b32 v5, s56\n\t"
      "v_xor_b32 v13, 65536, v1\n\t"
      "v_mfma_f32_16x16x32_bf16 v[24:27], v[64:67], a[16:19], v[24:27]\n\t"
      "ds_read_b128 v[80:83], v1 offset:3072\n\t"
      "v_mfma_f32_16x16x32_bf16 v[32:35], v[64:67], a[20:23], v[32:35]\n\t"
      "ds_read_b128 v[84:87], v1 offset:23552\n\t"
      "v_mfma_f32_16x16x32_bf16 v[40:43], v[64:67], a[24:27], v[40:43]\n\t"
      "v_mfma_f32_16x16x32_bf16 v[48:51], v[64:67], a[28:31], v[48:51]\n\t"
      "v_mfma_f32_16x16x32_bf16 v[28:31], v[68:71], a[16:19], v[28:31]\n\t"
      "v_mfma_f32_16x16x32_bf16 v[36:39], v[68:71], a[20:23], v[36:39]\n\t"
      "s_add_u32 m0, s54, 0\n\t"
      "s_add_u32 s43, s52, 0\n\t"
      "buffer_load_dwordx4 v0, %[rs], s43 offen sc1 lds\n\t"
      "v_mfma_f32_16x16x32_bf16 v[44:47], v[68:71], a[24:27], v[44:47]\n\t"
      "s_waitcnt lgkmcnt(2)\n\t"
      "v_mfma_f32_16x16x32_bf16 v[52:55], v[68:71], a[28:31], v[52:55]\n\t"
      "v_mfma_f32_16x16x32_bf16 v[24:27], v[72:75], a[32:35], v[24:27]\n\t"
      "ds_read_b128 v[56:59], v1 offset:4096\n\t"
      "v_mfma_f32_16x16x32_bf16 v[32:35], v[72:75], a[36:39], v[32:35]\n\t"
      "ds_read_b128 v[60:63], v1 offset:24576\n\t"
      "v_mfma_f32_16x16x32_bf16 v[40:43], v[72:75], a[40:43], v[40:43]\n\t"
      "v_mfma_f32_16x16x32_bf16 v[48:51], v[72:75], a[44:47], v[48:51]\n\t"
      "v_mfma_f32_16x16x32_bf16 v[28:31], v[76:79], a[32:35], v[28:31]\n\t"
      "v_mfma_f32_16x16x32_bf16 v[36:39], v[76:79], a[36:39], v[36:39]\n\t"
      "s_add_u32 m0, s54, 4096\n\t"
      "s_add_u32 s43, s52, 4096\n\t"
      "buffer_load_dwordx4 v0, %[rs], s43 offen sc1 lds\n\t"
      "v_mfma_f32_16x16x32_bf16 v[44:47], v[76:79], a[40:43], v[44:47]\n\t"
      "s_waitcnt lgkmcnt(2)\n\t"
      "v_mfma_f32_16x16x32_bf16 v[52:55], v[76:79], a[44:47], v[52:55]\n\t"
      "v_mfma_f32_16x16x32_bf16 v[24:27], v[80:83], a[48:51], v[24:27]\n\t"
      "ds_read_b128 v[64:67], v1 offset:5120\n\t"
      "v_mfma_f32_16x16x32_bf16 v[32:35], v[80:83], a[52:55], v[32:35]\n\t"
      "ds_read_b128 v[68:71], v1 offset:25600\n\t"
      "v_mfma_f32_16x16x32_bf16 v[40:43], v[80:83], a[56:59], v[40:43]\n\t"
      "v_mfma_f32_16x16x32_bf16 v[48:51], v[80:83], a[60:63], v[48:51]\n\t"
      "v_mfma_f32_16x16x32_bf16 v[28:31], v[84:87], a[48:51], v[28:31]\n\t"
      "v_mfma_f32_16x16x32_bf16 v[36:39], v[84:87], a[52:55], v[36:39]\n\t"
      "s_add_u32 m0, s54, 8192\n\t"
      "s_add_u32 s43, s52, 8192\n\t"
      "buffer_load_dwordx4 v0, %[rs], s43 offen sc1 lds\n\t"
      "v_mfma_f32_16x16x32_bf16 v[44:47], v[84:87], a[56:59], v[44:47]\n\t"
      "s_waitcnt lgkmcnt(2)\n\t"
      "v_mfma_f32_16x16x32_bf16 v[52:55], v[84:87], a[60:63], v[52:55]\n\t"
      "v_mfma_f32_16x16x32_bf16 v[24:27], v[56:59], a[64:67], v[24:27]\n\t"
      "ds_read_b128 v[72:75], v1 offset:6144\n\t"
      "v_mfma_f32_16x16x32_bf16 v[32:35], v[56:59], a[68:71], v[32:35]\n\t"
      "ds_read_b128 v[76:79], v1 offset:26624\n\t"
      "v_mfma_f32_16x16x32_bf16 v[40:43], v[56:59], a[72:75], v[40:43]\n\t"
      "v_mfma_f32_16x16x32_bf16 v[48:51], v[56:59], a[76:79], v[48:51]\n\t"
      "v_mfma_f32_16x16x32_bf16 v[28:31], v[60:63], a[64:67], v[28:31]\n\t"
      "v_mfma_f32_16x16x32_bf16 v[36:39], v[60:63], a[68:71], v[36:39]\n\t"
      "s_add_u32 m0, s54, 12288\n\t"
      "s_add_u32 s43, s52, 12288\n\t"
      "buffer_load_dwordx4 v0, %[rs], s43 offen sc1 lds\n\t"
      "v_mfma_f32_16x16x32_bf16 v[44:47], v[60:63], a[72:75], v[44:47]\n\t"
      "s_waitcnt lgkmcnt(2)\n\t"
      "v_mfma_f32_16x16x32_bf16 v[52:55], v[60:63], a[76:79], v[52:55]\n\t"
      "v_mfma_f32_16x16x32_bf16 v[24:27], v[64:67], a[80:83], v[24:27]\n\t"
      "ds_read_b128 v[80:83], v1 offset:7168\n\t"
      "v_mfma_f32_16x16x32_bf16 v[32:35], v[64:67], a[84:87], v[32:35]\n\t"
      "ds_read_b128 v[84:87], v1 offset:27648\n\t"
      "v_mfma_f32_16x16x32_bf16 v[40:43], v[64:67], a[88:91], v[40:43]\n\t"
      "v_mfma_f32_16x16x32_bf16 v[48:51], v[64:67], a[92:95], v[48:51]\n\t"
      "v_mfma_f32_16x16x32_bf16 v[28:31], v[68:71], a[80:83], v[28:31]\n\t"
      "v_mfma_f32_16x16x32_bf16 v[36:39], v[68:71], a[84:87], v[36:39]\n\t"
      "s_add_u32 m0, s54, 16384\n\t"
      "s_add_u32 s43, s52, 16384\n\t"
      "buffer_load_dwordx4 v0, %[rs], s43 offen sc1 lds\n\t"
      "v_mfma_f32_16x16x32_bf16 v[44:47], v[68:71], a[88:91], v[44:47]\n\t"
      "s_waitcnt lgkmcnt(2)\n\t"
      "v_mfma_f32_16x16x32_bf16 v[52:55], v[68:71], a[92:95], v[52:55]\n\t"
      "v_mfma_f32_16x16x32_bf16 v[24:27], v[72:75], a[96:99], v[24:27]\n\t"
      "ds_read_b128 v[56:59], v1 offset:8192\n\t"
      "v_mfma_f32_16x16x32_bf16 v[32:35], v[72:75], a[100:103], v[32:35]\n\t"
      "ds_read_b128 v[60:63], v1 offset:28672\n\t"
      "v_mfma_f32_16x16x32_bf16 v[40:43], v[72:75], a[104:107], v[40:43]\n\t"
      "v_mfma_f32_16x16x32_bf16 v[48:51], v[72:75], a[108:111], v[48:51]\n\t"
      "v_mfma_f32_16x16x32_bf16 v[28:31], v[76:79], a[96:99], v[28:31]\n\t"
      "v_mfma_f32_16x16x32_bf16 v[36:39], v[76:79], a[100:103], v[36:39]\n\t"
      "s_add_u32 m0, s54, 20480\n\t"
      "s_add_u32 s43, s52, 20480\n\t"
      "buffer_load_dwordx4 v0, %[rs], s43 offen sc1 lds\n\t"
      "v_mfma_f32_16x16x32_bf16 v[44:47], v[76:79], a[104:107], v[44:47]\n\t"
      "s_waitcnt lgkmcnt(2)\n\t"
      "v_mfma_f32_16x16x32_bf16 v[52:55], v[76:79], a[108:111], v[52:55]\n\t"
      "v_mfma_f32_16x16x32_bf16 v[24:27], v[80:83], a[112:115], v[24:27]\n\t"
      "ds_read_b128 v[64:67], v1 offset:9216\n\t"
      "v_mfma_f32_16x16x32_bf16 v[32:35], v[80:83], a[116:119], v[32:35]\n\t"
      "ds_read_b128 v[68:71], v1 offset:29696\n\t"
      "v_mfma_f32_16x16x32_bf16 v[40:43], v[80:83], a[120:123], v[40:43]\n\t"
      "v_mfma_f32_16x16x32_bf16 v[48:51], v[80:83], a[124:127], v[48:51]\n\t"
      "v_mfma_f32_16x16x32_bf16 v[28:31], v[84:87], a[112:115], v[28:31]\n\t"
      "v_mfma_f32_16x16x32_bf16 v[36:39], v[84:87], a[116:119], v[36:39]\n\t"
      "s_add_u32 m0, s54, 24576\n\t"
      "s_add_u32 s43, s52, 24576\n\t"
      "buffer_load_dwordx4 v0, %[rs], s43 offen sc1 lds\n\t"
      "v_mfma_f32_16x16x32_bf16 v[44:47], v[84:87], a[120:123], v[44:47]\n\t"
      "s_waitcnt lgkmcnt(2)\n\t"
      "v_mfma_f32_16x16x32_bf16 v[52:55], v[84:87], a[124:127], v[52:55]\n\t"
      "v_mfma_f32_16x16x32_bf16 v[24:27], v[56:59], a[128:131], v[24:27]\n\t"
      "ds_read_b128 v[72:75], v1 offset:10240\n\t"
      "v_mfma_f32_16x16x32_bf16 v[32:35], v[56:59], a[132:135], v[32:35]\n\t"
      "ds_read_b128 v[76:79], v1 offset:30720\n\t"
      "v_mfma_f32_16x16x32_bf16 v[40:43], v[56:59], a[136:139], v[40:43]\n\t"
      "v_mfma_f32_16x16x32_bf16 v[48:51], v[56:59], a[140:143], v[48:51]\n\t"
      "v_mfma_f32_16x16x32_bf16 v[28:31], v[60:63], a[128:131], v[28:31]\n\t"
      "v_mfma_f32_16x16x32_bf16 v[36:39], v[60:63], a[132:135], v[36:39]\n\t"
      "s_add_u32 m0, s54, 28672\n\t"
      "s_add_u32 s43, s52, 28672\n\t"
      "buffer_load_dwordx4 v0, %[rs], s43 offen sc1 lds\n\t"
      "v_mfma_f32_16x16x32_bf16 v[44:47], v[60:63], a[136:139], v[44:47]\n\t"
      "s_waitcnt lgkmcnt(2)\n\t"
      "v_mfma_f32_16x16x32_bf16 v[52:55], v[60:63], a[140:143], v[52:55]\n\t"
      "v_mfma_f32_16x16x32_bf16 v[24:27], v[64:67], a[144:147], v[24:27]\n\t"
      "ds_read_b128 v[80:83], v1 offset:11264\n\t"
      "v_mfma_f32_16x16x32_bf16 v[32:35], v[64:67], a[148:151], v[32:35]\n\t"
      "ds_read_b128 v[84:87], v1 offset:31744\n\t"
      "v_mfma_f32_16x16x32_bf16 v[40:43], v[64:67], a[152:155], v[40:43]\n\t"
      "v_mfma_f32_16x16x32_bf16 v[48:51], v[64:67], a[156:159], v[48:51]\n\t"
      "v_mfma_f32_16x16x32_bf16 v[28:31], v[68:71], a[144:147], v[28:31]\n\t"
      "v_mfma_f32_16x16x32_bf16 v[36:39], v[68:71], a[148:151], v[36:39]\n\t"
      "s_add_u32 m0, s54, 32768\n\t"
      "s_add_u32 s43, s52, 32768\n\t"
      "buffer_load_dwordx4 v0, %[rs], s43 offen sc1 lds\n\t"
      "v_mfma_f32_16x16x32_bf16 v[44:47], v[68:71], a[152:155], v[44:47]\n\t"
      "s_waitcnt lgkmcnt(2)\n\t"
      "v_mfma_f32_16x16x32_bf16 v[52:55], v[68:71], a[156:159], v[52:55]\n\t"
      "v_mfma_f32_16x16x32_bf16 v[24:27], v[72:75], a[160:163], v[24:27]\n\t"
      "ds_read_b128 v[56:59], v1 offset:12288\n\t"
      "v_mfma_f32_16x16x32_bf16 v[32:35], v[72:75], a[164:167], v[32:35]\n\t"
      "ds_read_b128 v[60:63], v1 offset:32768\n\t"
      "v_mfma_f32_16x16x32_bf16 v[40:43], v[72:75], a[168:171], v[40:43]\n\t"
      "v_mfma_f32_16x16x32_bf16 v[48:51], v[72:75], a[172:175], v[48:51]\n\t"
      "v_mfma_f32_16x16x32_bf16 v[28:31], v[76:79], a[160:163], v[28:31]\n\t"
      "v_mfma_f32_16x16x32_bf16 v[36:39], v[76:79], a[164:167], v[36:39]\n\t"
      "s_add_u32 m0, s54, 36864\n\t"
      "s_add_u32 s43, s52, 36864\n\t"
      "buffer_load_dwordx4 v0, %[rs], s43 offen sc1 lds\n\t"
      "v_mfma_f32_16x16x32_bf16 v[44:47], v[76:79], a[168:171], v[44:47]\n\t"
      "s_waitcnt lgkmcnt(2)\n\t"
      "v_mfma_f32_16x16x32_bf16 v[52:55], v[76:79], a[172:175], v[52:55]\n\t"
      "v_mfma_f32_16x16x32_bf16 v[24:27], v[80:83], a[176:179], v[24:27]\n\t"
      "ds_read_b128 v[64:67], v1 offset:13312\n\t"
      "v_mfma_f32_16x16x32_bf16 v[32:35], v[80:83], a[180:183], v[32:35]\n\t"
      "ds_read_b128 v[68:71], v1 offset:33792\n\t"
      "v_mfma_f32_16x16x32_bf16 v[40:43], v[80:83], a[184:187], v[40:43]\n\t"
      "v_mfma_f32_16x16x32_bf16 v[48:51], v[80:83], a[188:191], v[48:51]\n\t"
      "v_mfma_f32_16x16x32_bf16 v[28:31], v[84:87], a[176:179], v[28:31]\n\t"
      "v_mfma_f32_16x16x32_bf16 v[36:39], v[84:87], a[180:183], v[36:39]\n\t"
      "v_mfma_f32_16x16x32_bf16 v[44:47], v[84:87], a[184:187], v[44:47]\n\t"
      "s_waitcnt lgkmcnt(2)\n\t"
      "v_mfma_f32_16x16x32_bf16 v[52:55], v[84:87], a[188:191], v[52:55]\n\t"
      "v_mfma_f32_16x16x32_bf16 v[24:27], v[56:59], a[192:195], v[24:27]\n\t"
      "ds_read_b128 v[72:75], v1 offset:14336\n\t"
      "v_mfma_f32_16x16x32_bf16 v[32:35], v[56:59], a[196:199], v[32:35]\n\t"
      "ds_read_b128 v[76:79], v1 offset:34816\n\t"
      "v_mfma_f32_16x16x32_bf16 v[40:43], v[56:59], a[200:203], v[40:43]\n\t"
      "v_mfma_f32_16x16x32_bf16 v[48:51], v[56:59], a[204:207], v[48:51]\n\t"
      "v_mfma_f32_16x16x32_bf16 v[28:31], v[60:63], a[192:195], v[28:31]\n\t"
      "v_mfma_f32_16x16x32_bf16 v[36:39], v[60:63], a[196:199], v[36:39]\n\t"
      "v_mfma_f32_16x16x32_bf16 v[44:47], v[60:63], a[200:203], v[44:47]\n\t"
      "s_waitcnt lgkmcnt(2)\n\t"
      "v_mfma_f32_16x16x32_bf16 v[52:55], v[60:63], a[204:207], v[52:55]\n\t"
      "v_mfma_f32_16x16x32_bf16 v[24:27], v[64:67], a[208:211], v[24:27]\n\t"
      "ds_read_b128 v[80:83], v1 offset:15360\n\t"
      "v_mfma_f32_16x16x32_bf16 v[32:35], v[64:67], a[212:215], v[32:35]\n\t"
      "ds_read_b128 v[84:87], v1 offset:35840\n\t"
      "v_mfma_f32_16x16x32_bf16 v[40:43], v[64:67], a[216:219], v[40:43]\n\t"
      "v_mfma_f32_16x16x32_bf16 v[48:51], v[64:67], a[220:223], v[48:51]\n\t"
      "buffer_store_dwordx4 v[8:11], v3, %[rs], s57 offen sc1\n\t"
      "v_mfma_f32_16x16x32_bf16 v[28:31], v[68:71], a[208:211], v[28:31]\n\t"
      "v_mfma_f32_16x16x32_bf16 v[36:39], v[68:71], a[212:215], v[36:39]\n\t"
      "v_mfma_f32_16x16x32_bf16 v[44:47], v[68:71], a[216:219], v[44:47]\n\t"
      "s_waitcnt lgkmcnt(2)\n\t"
      "v_mfma_f32_16x16x32_bf16 v[52:55], v[68:71], a[220:223], v[52:55]\n\t"
      "v_mfma_f32_16x16x32_bf16 v[24:27], v[72:75], a[224:227], v[24:27]\n\t"
      "ds_read_b128 v[56:59], v1 offset:16384\n\t"
      "v_mfma_f32_16x16x32_bf16 v[32:35], v[72:75], a[228:231], v[32:35]\n\t"
      "ds_read_b128 v[60:63], v1 offset:36864\n\t"
      "v_mfma_f32_16x16x32_bf16 v[40:43], v[72:75], a[232:235], v[40:43]\n\t"
      "v_mfma_f32_16x16x32_bf16 v[48:51], v[72:75], a[236:239], v[48:51]\n\t"
      "buffer_store_dwordx4 v[8:11], v6, %[rs], s58 offen sc1\n\t"
      "v_mfma_f32_16x16x32_bf16 v[28:31], v[76:79], a[224:227], v[28:31]\n\t"
      "v_mfma_f32_16x16x32_bf16 v[36:39], v[76:79], a[228:231], v[36:39]\n\t"
      "v_mfma_f32_16x16x32_bf16 v[44:47], v[76:79], a[232:235], v[44:47]\n\t"
      "s_waitcnt lgkmcnt(2)\n\t"
      "v_mfma_f32_16x16x32_bf16 v[52:55], v[76:79], a[236:239], v[52:55]\n\t"
      "v_mfma_f32_16x16x32_bf16 v[24:27], v[80:83], a[240:243], v[24:27]\n\t"
      "ds_read_b128 v[64:67], v1 offset:17408\n\t"
      "v_mfma_f32_16x16x32_bf16 v[32:35], v[80:83], a[244:247], v[32:35]\n\t"
      "ds_read_b128 v[68:71], v1 offset:37888\n\t"
      "v_mfma_f32_16x16x32_bf16 v[40:43], v[80:83], a[248:251], v[40:43]\n\t"
      "v_mfma_f32_16x16x32_bf16 v[48:51], v[80:83], a[252:255], v[48:51]\n\t"
      "buffer_store_dwordx4 v[8:11], v7, %[rs], s58 offen sc1\n\t"
      "v_mfma_f32_16x16x32_bf16 v[28:31], v[84:87], a[240:243], v[28:31]\n\t"
      "v_mfma_f32_16x16x32_bf16 v[36:39], v[84:87], a[244:247], v[36:39]\n\t"
      "v_mfma_f32_16x16x32_bf16 v[44:47], v[84:87], a[248:251], v[44:47]\n\t"
      "s_waitcnt lgkmcnt(2)\n\t"
      "v_mfma_f32_16x16x32_bf16 v[52:55], v[84:87], a[252:255], v[52:55]\n\t"
      "v_mfma_f32_16x16x32_bf16 v[24:27], v[56:59], v[128:131], v[24:27]\n\t"
      "ds_read_b128 v[72:75], v1 offset:18432\n\t"
      "v_mfma_f32_16x16x32_bf16 v[32:35], v[56:59], v[132:135], v[32:35]\n\t"
      "ds_read_b128 v[76:79], v1 offset:38912\n\t"
      "v_mfma_f32_16x16x32_bf16 v[40:43], v[56:59], v[136:139], v[40:43]\n\t"
      "v_mfma_f32_16x16x32_bf16 v[48:51], v[56:59], v[140:143], v[48:51]\n\t"
      "v_mfma_f32_16x16x32_bf16 v[28:31], v[60:63], v[128:131], v[28:31]\n\t"
      "v_mfma_f32_16x16x32_bf16 v[36:39], v[60:63], v[132:135], v[36:39]\n\t"
      "v_mfma_f32_16x16x32_bf16 v[44:47], v[60:63], v[136:139], v[44:47]\n\t"
      "s_waitcnt lgkmcnt(2)\n\t"
      "v_mfma_f32_16x16x32_bf16 v[52:55], v[60:63], v[140:143], v[52:55]\n\t"
      "v_mfma_f32_16x16x32_bf16 v[24:27], v[64:67], v[144:147], v[24:27]\n\t"
      "ds_read_b128 v[80:83], v1 offset:19456\n\t"
      "v_mfma_f32_16x16x32_bf16 v[32:35], v[64:67], v[148:151], v[32:35]\n\t"
      "ds_read_b128 v[84:87], v1 offset:39936\n\t"
      "v_mfma_f32_16x16x32_bf16 v[40:43], v[64:67], v[152:155], v[40:43]\n\t"
      "v_mfma_f32_16x16x32_bf16 v[48:51], v[64:67], v[156:159], v[48:51]\n\t"
      "v_mfma_f32_16x16x32_bf16 v[28:31], v[68:71], v[144:147], v[28:31]\n\t"
      "v_mfma_f32_16x16x32_bf16 v[36:39], v[68:71], v[148:151], v[36:39]\n\t"
      "v_mfma_f32_16x16x32_bf16 v[44:47], v[68:71], v[152:155], v[44:47]\n\t"
      "s_waitcnt lgkmcnt(2)\n\t"
      "v_mfma_f32_16x16x32_bf16 v[52:55], v[68:71], v[156:159], v[52:55]\n\t"
      "s_waitcnt vmcnt(3) lgkmcnt(0)\n\t"
      "s_barrier\n\t"
      "v_mfma_f32_16x16x32_bf16 v[24:27], v[72:75], v[160:163], v[24:27]\n\t"
      "ds_read_b128 v[56:59], v13 offset:0\n\t"
      "v_mfma_f32_16x16x32_bf16 v[32:35], v[72:75], v[164:167], v[32:35]\n\t"
      "ds_read_b128 v[60:63], v13 offset:20480\n\t"
      "v_mfma_f32_16x16x32_bf16 v[40:43], v[72:75], v[168:171], v[40:43]\n\t"
      "v_mfma_f32_16x16x32_bf16 v[48:51], v[72:75], v[172:175], v[48:51]\n\t"
      "v_mfma_f32_16x16x32_bf16 v[28:31], v[76:79], v[160:163], v[28:31]\n\t"
      "v_mfma_f32_16x16x32_bf16 v[36:39], v[76:79], v[164:167], v[36:39]\n\t"
      "v_mfma_f32_16x16x32_bf16 v[44:47], v[76:79], v[168:171], v[44:47]\n\t"
      "v_mfma_f32_16x16x32_bf16 v[52:55], v[76:79], v[172:175], v[52:55]\n\t"
      "v_mfma_f32_16x16x32_bf16 v[24:27], v[80:83], v[176:179], v[24:27]\n\t"
      "ds_read_b128 v[64:67], v13 offset:1024\n\t"
      "v_mfma_f32_16x16x32_bf16 v[32:35], v[80:83], v[180:183], v[32:35]\n\t"
      "ds_read_b128 v[68:71], v13 offset:21504\n\t"
      "v_mfma_f32_16x16x32_bf16 v[40:43], v[80:83], v[184:187], v[40:43]\n\t"
      "v_mfma_f32_16x16x32_bf16 v[48:51], v[80:83], v[188:191], v[48:51]\n\t"
      "v_mov_b32 v1, v13\n\t"
      "v_mfma_f32_16x16x32_bf16 v[28:31], v[84:87], v[176:179], v[28:31]\n\t"
      "v_mfma_f32_16x16x32_bf16 v[36:39], v[84:87], v[180:183], v[36:39]\n\t"
      "s_add_u32 s40, s40, 1\n\t"
      "s_cmp_lt_u32 s40, 63\n\t"
      "v_mfma_f32_16x16x32_bf16 v[44:47], v[84:87], v[184:187], v[44:47]\n\t"
      "s_waitcnt lgkmcnt(2)\n\t"
      "v_mfma_f32_16x16x32_bf16 v[52:55], v[84:87], v[188:191], v[52:55]\n\t"
      "LOOP%=:\n\t"
      "v_mfma_f32_16x16x32_bf16 v[192:195], v[56:59], a[0:3], 0\n\t"
      "ds_read_b128 v[72:75], v1 offset:2048\n\t"
      "v_mfma_f32_16x16x32_bf16 v[200:203], v[56:59], a[4:7], 0\n\t"
      "ds_read_b128 v[76:79], v1 offset:22528\n\t"
      "v_mfma_f32_16x16x32_bf16 v[208:211], v[56:59], a[8:11], 0\n\t"
      "s_xor_b32 s54, s54, 65536\n\t"
      "s_add_u32 s43, s40, 1\n\t"
      "v_mfma_f32_16x16x32_bf16 v[216:219], v[56:59], a[12:15], 0\n\t"
      "s_min_u32 s43, s43, 63\n\t"
      "s_mul_i32 s43, s43, 40960\n\t"
      "v_mfma_f32_16x16x32_bf16 v[196:199], v[60:63], a[0:3], 0\n\t"
      "s_add_u32 s52, s43, 88080384\n\t"
      "s_mul_i32 s57, s40, %[ms]\n\t"
      "v_mfma_f32_16x16x32_bf16 v[204:207], v[60:63], a[4:7], 0\n\t"
      "s_mul_i32 s58, s40, 110592\n\t"
      "s_sub_u32 s56, s40, 1\n\t"
      "v_mfma_f32_16x16x32_bf16 v[212:215], v[60:63], a[8:11], 0\n\t"
      "s_mul_i32 s53, s56, 24576\n\t"
      "s_add_u32 s55, s53, 4096\n\t"
      "s_waitcnt lgkmcnt(2)\n\t"
      "v_mfma_f32_16x16x32_bf16 v[220:223], v[60:63], a[12:15], 0\n\t"
      "v_mov_b32 v5, s56\n\t"
      "v_xor_b32 v13, 65536, v1\n\t"
      "v_mfma_f32_16x16x32_bf16 v[192:195], v[64:67], a[16:19], v[192:195]\n\t"
      "ds_read_b128 v[80:83], v1 offset:3072\n\t"
      "v_cvt_pk_bf16_f32 v88, v24, v25\n\t"
      "v_cvt_pk_bf16_f32 v89, v26, v27\n\t"
      "v_mfma_f32_16x16x32_bf16 v[200:203], v[64:67], a[20:23], v[200:203]\n\t"
      "ds_read_b128 v[84:87], v1 offset:23552\n\t"
      "v_cvt_pk_bf16_f32 v90, v28, v29\n\t"
      "v_cvt_pk_bf16_f32 v91, v30, v31\n\t"
      "v_mfma_f32_16x16x32_bf16 v[208:211], v[64:67], a[24:27], v[208:211]\n\t"
      "buffer_store_dwordx4 v[88:91], v2, %[rs], s53 offen offset:0 sc1\n\t"
      "v_mfma_f32_16x16x32_bf16 v[216:219], v[64:67], a[28:31], v[216:219]\n\t"
      "v_mfma_f32_16x16x32_bf16 v[196:199], v[68:71], a[16:19], v[196:199]\n\t"
      "v_mfma_f32_16x16x32_bf16 v[204:207], v[68:71], a[20:23], v[204:207]\n\t"
      "s_add_u32 m0, s54, 0\n\t"
      "s_add_u32 s43, s52, 0\n\t"
      "buffer_load_dwordx4 v0, %[rs], s43 offen sc1 lds\n\t"
      "v_mfma_f32_16x16x32_bf16 v[212:215], v[68:71], a[24:27], v[212:215]\n\t"
      "s_waitcnt lgkmcnt(2)\n\t"
      "v_mfma_f32_16x16x32_bf16 v[220:223], v[68:71], a[28:31], v[220:223]\n\t"
      "v_mfma_f32_16x16x32_bf16 v[192:195], v[72:75], a[32:35], v[192:195]\n\t"
      "ds_read_b128 v[56:59], v1 offset:4096\n\t"
      "v_cvt_pk_bf16_f32 v92, v32, v33\n\t"
      "v_cvt_pk_bf16_f32 v93, v34, v35\n\t"
      "v_mfma_f32_16x16x32_bf16 v[200:203], v[72:75], a[36:39], v[200:203]\n\t"
      "ds_read_b128 v[60:63], v1 offset:24576\n\t"
      "v_cvt_pk_bf16_f32 v94, v36, v37\n\t"
      "v_cvt_pk_bf16_f32 v95, v38, v39\n\t"
      "v_mfma_f32_16x16x32_bf16 v[208:211], v[72:75], a[40:43], v[208:211]\n\t"
      "buffer_store_dwordx4 v[92:95], v2, %[rs], s53 offen offset:1024 sc1\n\t"
      "v_mfma_f32_16x16x32_bf16 v[216:219], v[72:75], a[44:47], v[216:219]\n\t"
      "v_mfma_f32_16x16x32_bf16 v[196:199], v[76:79], a[32:35], v[196:199]\n\t"
      "v_mfma_f32_16x16x32_bf16 v[204:207], v[76:79], a[36:39], v[204:207]\n\t"
      "s_add_u32 m0, s54, 4096\n\t"
      "s_add_u32 s43, s52, 4096\n\t"
      "buffer_load_dwordx4 v0, %[rs], s43 offen sc1 lds\n\t"
      "v_mfma_f32_16x16x32_bf16 v[212:215], v[76:79], a[40:43], v[212:215]\n\t"
      "s_waitcnt lgkmcnt(2)\n\t"
      "v_mfma_f32_16x16x32_bf16 v[220:223], v[76:79], a[44:47], v[220:223]\n\t"
      "v_mfma_f32_16x16x32_bf16 v[192:195], v[80:83], a[48:51], v[192:195]\n\t"
      "ds_read_b128 v[64:67], v1 offset:5120\n\t"
      "v_cvt_pk_bf16_f32 v96, v40, v41\n\t"
      "v_cvt_pk_bf16_f32 v97, v42, v43\n\t"
      "v_mfma_f32_16x16x32_bf16 v[200:203], v[80:83], a[52:55], v[200:203]\n\t"
      "ds_read_b128 v[68:71], v1 offset:25600\n\t"
      "v_cvt_pk_bf16_f32 v98, v44, v45\n\t"
      "v_cvt_pk_bf16_f32 v99, v46, v47\n\t"
      "v_mfma_f32_16x16x32_bf16 v[208:211], v[80:83], a[56:59], v[208:211]\n\t"
      "buffer_store_dwordx4 v[96:99], v2, %[rs], s53 offen offset:2048 sc1\n\t"
      "v_mfma_f32_16x16x32_bf16 v[216:219], v[80:83], a[60:63], v[216:219]\n\t"
      "v_mfma_f32_16x16x32_bf16 v[196:199], v[84:87], a[48:51], v[196:199]\n\t"
      "v_mfma_f32_16x16x32_bf16 v[204:207], v[84:87], a[52:55], v[204:207]\n\t"
      "s_add_u32 m0, s54, 8192\n\t"
      "s_add_u32 s43, s52, 8192\n\t"
      "buffer_load_dwordx4 v0, %[rs], s43 offen sc1 lds\n\t"
      "v_mfma_f32_16x16x32_bf16 v[212:215], v[84:87], a[56:59], v[212:215]\n\t"
      "s_waitcnt lgkmcnt(2)\n\t"
      "v_mfma_f32_16x16x32_bf16 v[220:223], v[84:87], a[60:63], v[220:223]\n\t"
      "v_mfma_f32_16x16x32_bf16 v[192:195], v[56:59], a[64:67], v[192:195]\n\t"
      "ds_read_b128 v[72:75], v1 offset:6144\n\t"
      "v_cvt_pk_bf16_f32 v100, v48, v49\n\t"
      "v_cvt_pk_bf16_f32 v101, v50, v51\n\t"
      "v_mfma_f32_16x16x32_bf16 v[200:203], v[56:59], a[68:71], v[200:203]\n\t"
      "ds_read_b128 v[76:79], v1 offset:26624\n\t"
      "v_cvt_pk_bf16_f32 v102, v52, v53\n\t"
      "v_cvt_pk_bf16_f32 v103, v54, v55\n\t"
      "v_mfma_f32_16x16x32_bf16 v[208:211], v[56:59], a[72:75], v[208:211]\n\t"
      "buffer_store_dwordx4 v[100:103], v2, %[rs], s53 offen offset:3072 sc1\n\t"
      "v_mfma_f32_16x16x32_bf16 v[216:219], v[56:59], a[76:79], v[216:219]\n\t"
      "v_mfma_f32_16x16x32_bf16 v[196:199], v[60:63], a[64:67], v[196:199]\n\t"
      "v_mfma_f32_16x16x32_bf16 v[204:207], v[60:63], a[68:71], v[204:207]\n\t"
      "s_add_u32 m0, s54, 12288\n\t"
      "s_add_u32 s43, s52, 12288\n\t"
      "buffer_load_dwordx4 v0, %[rs], s43 offen sc1 lds\n\t"
      "v_mfma_f32_16x16x32_bf16 v[212:215], v[60:63], a[72:75], v[212:215]\n\t"
      "s_waitcnt lgkmcnt(2)\n\t"
      "v_mfma_f32_16x16x32_bf16 v[220:223], v[60:63], a[76:79], v[220:223]\n\t"
      "v_mfma_f32_16x16x32_bf16 v[192:195], v[64:67], a[80:83], v[192:195]\n\t"
      "ds_read_b128 v[80:83], v1 offset:7168\n\t"
      "v_mfma_f32_16x16x32_bf16 v[200:203], v[64:67], a[84:87], v[200:203]\n\t"
      "ds_read_b128 v[84:87], v1 offset:27648\n\t"
      "v_mfma_f32_16x16x32_bf16 v[208:211], v[64:67], a[88:91], v[208:211]\n\t"
      "v_mfma_f32_16x16x32_bf16 v[216:219], v[64:67], a[92:95], v[216:219]\n\t"
      "v_mfma_f32_16x16x32_bf16 v[196:199], v[68:71], a[80:83], v[196:199]\n\t"
      "v_mfma_f32_16x16x32_bf16 v[204:207], v[68:71], a[84:87], v[204:207]\n\t"
      "s_add_u32 m0, s54, 16384\n\t"
      "s_add_u32 s43, s52, 16384\n\t"
      "buffer_load_dwordx4 v0, %[rs], s43 offen sc1 lds\n\t"
      "v_mfma_f32_16x16x32_bf16 v[212:215], v[68:71], a[88:91], v[212:215]\n\t"
      "s_waitcnt lgkmcnt(2)\n\t"
      "v_mfma_f32_16x16x32_bf16 v[220:223], v[68:71], a[92:95], v[220:223]\n\t"
      "v_mfma_f32_16x16x32_bf16 v[192:195], v[72:75], a[96:99], v[192:195]\n\t"
      "ds_read_b128 v[56:59], v1 offset:8192\n\t"
      "v_mfma_f32_16x16x32_bf16 v[200:203], v[72:75], a[100:103], v[200:203]\n\t"
      "ds_read_b128 v[60:63], v1 offset:28672\n\t"
      "v_mfma_f32_16x16x32_bf16 v[208:211], v[72:75], a[104:107], v[208:211]\n\t"
      "v_mfma_f32_16x16x32_bf16 v[216:219], v[72:75], a[108:111], v[216:219]\n\t"
      "v_mfma_f32_16x16x32_bf16 v[196:199], v[76:79], a[96:99], v[196:199]\n\t"
      "v_mfma_f32_16x16x32_bf16 v[204:207], v[76:79], a[100:103], v[204:207]\n\t"
      "s_add_u32 m0, s54, 20480\n\t"
      "s_add_u32 s43, s52, 20480\n\t"
      "buffer_load_dwordx4 v0, %[rs], s43 offen sc1 lds\n\t"
      "v_mfma_f32_16x16x32_bf16 v[212:215], v[76:79], a[104:107], v[212:215]\n\t"
      "s_waitcnt lgkmcnt(2)\n\t"
      "v_mfma_f32_16x16x32_bf16 v[220:223], v[76:79], a[108:111], v[220:223]\n\t"
      "v_mfma_f32_16x16x32_bf16 v[192:195], v[80:83], a[112:115], v[192:195]\n\t"
      "ds_read_b128 v[64:67], v1 offset:9216\n\t"
      "v_mfma_f32_16x16x32_bf16 v[200:203], v[80:83], a[116:119], v[200:203]\n\t"
      "ds_read_b128 v[68:71], v1 offset:29696\n\t"
      "v_mfma_f32_16x16x32_bf16 v[208:211], v[80:83], a[120:123], v[208:211]\n\t"
      "v_mfma_f32_16x16x32_bf16 v[216:219], v[80:83], a[124:127], v[216:219]\n\t"
      "v_mfma_f32_16x16x32_bf16 v[196:199], v[84:87], a[112:115], v[196:199]\n\t"
      "v_mfma_f32_16x16x32_bf16 v[204:207], v[84:87], a[116:119], v[204:207]\n\t"
      "s_add_u32 m0, s54, 24576\n\t"
      "s_add_u32 s43, s52, 24576\n\t"
      "buffer_load_dwordx4 v0, %[rs], s43 offen sc1 lds\n\t"
      "v_mfma_f32_16x16x32_bf16 v[212:215], v[84:87], a[120:123], v[212:215]\n\t"
      "s_waitcnt lgkmcnt(2)\n\t"
      "v_mfma_f32_16x16x32_bf16 v[220:223], v[84:87], a[124:127], v[220:223]\n\t"
      "v_mfma_f32_16x16x32_bf16 v[192:195], v[56:59], a[128:131], v[192:195]\n\t"
      "ds_read_b128 v[72:75], v1 offset:10240\n\t"
      "v_mfma_f32_16x16x32_bf16 v[200:203], v[56:59], a[132:135], v[200:203]\n\t"
      "ds_read_b128 v[76:79], v1 offset:30720\n\t"
      "v_mfma_f32_16x16x32_bf16 v[208:211], v[56:59], a[136:139], v[208:211]\n\t"
      "v_mfma_f32_16x16x32_bf16 v[216:219], v[56:59], a[140:143], v[216:219]\n\t"
      "v_mfma_f32_16x16x32_bf16 v[196:199], v[60:63], a[128:131], v[196:199]\n\t"
      "v_mfma_f32_16x16x32_bf16 v[204:207], v[60:63], a[132:135], v[204:207]\n\t"
      "s_add_u32 m0, s54, 28672\n\t"
      "s_add_u32 s43, s52, 28672\n\t"
      "buffer_load_dwordx4 v0, %[rs], s43 offen sc1 lds\n\t"
      "v_mfma_f32_16x16x32_bf16 v[212:215], v[60:63], a[136:139], v[212:215]\n\t"
      "s_waitcnt lgkmcnt(2)\n\t"
      "v_mfma_f32_16x16x32_bf16 v[220:223], v[60:63], a[140:143], v[220:223]\n\t"
      "v_mfma_f32_16x16x32_bf16 v[192:195], v[64:67], a[144:147], v[192:195]\n\t"
      "ds_read_b128 v[80:83], v1 offset:11264\n\t"
      "v_mfma_f32_16x16x32_bf16 v[200:203], v[64:67], a[148:151], v[200:203]\n\t"
      "ds_read_b128 v[84:87], v1 offset:31744\n\t"
      "v_mfma_f32_16x16x32_bf16 v[208:211], v[64:67], a[152:155], v[208:211]\n\t"
      "v_mfma_f32_16x16x32_bf16 v[216:219], v[64:67], a[156:159], v[216:219]\n\t"
      "v_mfma_f32_16x16x32_bf16 v[196:199], v[68:71], a[144:147], v[196:199]\n\t"
      "v_mfma_f32_16x16x32_bf16 v[204:207], v[68:71], a[148:151], v[204:207]\n\t"
      "s_add_u32 m0, s54, 32768\n\t"
      "s_add_u32 s43, s52, 32768\n\t"
      "buffer_load_dwordx4 v0, %[rs], s43 offen sc1 lds\n\t"
      "v_mfma_f32_16x16x32_bf16 v[212:215], v[68:71], a[152:155], v[212:215]\n\t"
      "s_waitcnt lgkmcnt(2)\n\t"
      "v_mfma_f32_16x16x32_bf16 v[220:223], v[68:71], a[156:159], v[220:223]\n\t"
      "v_mfma_f32_16x16x32_bf16 v[192:195], v[72:75], a[160:163], v[192:195]\n\t"
      "ds_read_b128 v[56:59], v1 offset:12288\n\t"
      "v_mfma_f32_16x16x32_bf16 v[200:203], v[72:75], a[164:167], v[200:203]\n\t"
      "ds_read_b128 v[60:63], v1 offset:32768\n\t"
      "v_mfma_f32_16x16x32_bf16 v[208:211], v[72:75], a[168:171], v[208:211]\n\t"
      "v_mfma_f32_16x16x32_bf16 v[216:219], v[72:75], a[172:175], v[216:219]\n\t"
      "v_mfma_f32_16x16x32_bf16 v[196:199], v[76:79], a[160:163], v[196:199]\n\t"
      "v_mfma_f32_16x16x32_bf16 v[204:207], v[76:79], a[164:167], v[204:207]\n\t"
      "s_add_u32 m0, s54, 36864\n\t"
      "s_add_u32 s43, s52, 36864\n\t"
      "buffer_load_dwordx4 v0, %[rs], s43 offen sc1 lds\n\t"
      "v_mfma_f32_16x16x32_bf16 v[212:215], v[76:79], a[168:171], v[212:215]\n\t"
      "s_waitcnt lgkmcnt(2)\n\t"
      "v_mfma_f32_16x16x32_bf16 v[220:223], v[76:79], a[172:175], v[220:223]\n\t"
      "v_mfma_f32_16x16x32_bf16 v[192:195], v[80:83], a[176:179], v[192:195]\n\t"
      "ds_read_b128 v[64:67], v1 offset:13312\n\t"
      "v_mfma_f32_16x16x32_bf16 v[200:203], v[80:83], a[180:183], v[200:203]\n\t"
      "ds_read_b128 v[68:71], v1 offset:33792\n\t"
      "v_mfma_f32_16x16x32_bf16 v[208:211], v[80:83], a[184:187], v[208:211]\n\t"
      "v_mfma_f32_16x16x32_bf16 v[216:219], v[80:83], a[188:191], v[216:219]\n\t"
      "s_waitcnt vmcnt(7)\n\t"
      "buffer_store_dword v5, v4, %[rs], %[flo] offen sc1\n\t"
      "v_mfma_f32_16x16x32_bf16 v[196:199], v[84:87], a[176:179], v[196:199]\n\t"
      "v_mfma_f32_16x16x32_bf16 v[204:207], v[84:87], a[180:183], v[204:207]\n\t"
      "v_mfma_f32_16x16x32_bf16 v[212:215], v[84:87], a[184:187], v[212:215]\n\t"
      "s_waitcnt lgkmcnt(2)\n\t"
      "v_mfma_f32_16x16x32_bf16 v[220:223], v[84:87], a[188:191], v[220:223]\n\t"
      "v_mfma_f32_16x16x32_bf16 v[192:195], v[56:59], a[192:195], v[192:195]\n\t"
      "ds_read_b128 v[72:75], v1 offset:14336\n\t"
      "v_mfma_f32_16x16x32_bf16 v[200:203], v[56:59], a[196:199], v[200:203]\n\t"
      "ds_read_b128 v[76:79], v1 offset:34816\n\t"
      "v_mfma_f32_16x16x32_bf16 v[208:211], v[56:59], a[200:203], v[208:211]\n\t"
      "v_mfma_f32_16x16x32_bf16 v[216:219], v[56:59], a[204:207], v[216:219]\n\t"
      "v_mfma_f32_16x16x32_bf16 v[196:199], v[60:63], a[192:195], v[196:199]\n\t"
      "v_mfma_f32_16x16x32_bf16 v[204:207], v[60:63], a[196:199], v[204:207]\n\t"
      "v_mfma_f32_16x16x32_bf16 v[212:215], v[60:63], a[200:203], v[212:215]\n\t"
      "s_waitcnt lgkmcnt(2)\n\t"
      "v_mfma_f32_16x16x32_bf16 v[220:223], v[60:63], a[204:207], v[220:223]\n\t"
      "v_mfma_f32_16x16x32_bf16 v[192:195], v[64:67], a[208:211], v[192:195]\n\t"
      "ds_read_b128 v[80:83], v1 offset:15360\n\t"
      "v_mfma_f32_16x16x32_bf16 v[200:203], v[64:67], a[212:215], v[200:203]\n\t"
      "ds_read_b128 v[84:87], v1 offset:35840\n\t"
      "v_mfma_f32_16x16x32_bf16 v[208:211], v[64:67], a[216:219], v[208:211]\n\t"
      "v_mfma_f32_16x16x32_bf16 v[216:219], v[64:67], a[220:223], v[216:219]\n\t"
      "buffer_store_dwordx4 v[8:11], v3, %[rs], s57 offen sc1\n\t"
      "v_mfma_f32_16x16x32_bf16 v[196:199], v[68:71], a[208:211], v[196:199]\n\t"
      "v_mfma_f32_16x16x32_bf16 v[204:207], v[68:71], a[212:215], v[204:207]\n\t"
      "v_mfma_f32_16x16x32_bf16 v[212:215], v[68:71], a[216:219], v[212:215]\n\t"
      "s_waitcnt lgkmcnt(2)\n\t"
      "v_mfma_f32_16x16x32_bf16 v[220:223], v[68:71], a[220:223], v[220:223]\n\t"
      "v_mfma_f32_16x16x32_bf16 v[192:195], v[72:75], a[224:227], v[192:195]\n\t"
      "ds_read_b128 v[56:59], v1 offset:16384\n\t"
      "v_mfma_f32_16x16x32_bf16 v[200:203], v[72:75], a[228:231], v[200:203]\n\t"
      "ds_read_b128 v[60:63], v1 offset:36864\n\t"
      "v_mfma_f32_16x16x32_bf16 v[208:211], v[72:75], a[232:235], v[208:211]\n\t"
      "v_mfma_f32_16x16x32_bf16 v[216:219], v[72:75], a[236:239], v[216:219]\n\t"
      "buffer_store_dwordx4 v[8:11], v6, %[rs], s58 offen sc1\n\t"
      "v_mfma_f32_16x16x32_bf16 v[196:199], v[76:79], a[224:227], v[196:199]\n\t"
      "v_mfma_f32_16x16x32_bf16 v[204:207], v[76:79], a[228:231], v[204:207]\n\t"
      "v_mfma_f32_16x16x32_bf16 v[212:215], v[76:79], a[232:235], v[212:215]\n\t"
      "s_waitcnt lgkmcnt(2)\n\t"
      "v_mfma_f32_16x16x32_bf16 v[220:223], v[76:79], a[236:239], v[220:223]\n\t"
      "v_mfma_f32_16x16x32_bf16 v[192:195], v[80:83], a[240:243], v[192:195]\n\t"
      "ds_read_b128 v[64:67], v1 offset:17408\n\t"
      "v_mfma_f32_16x16x32_bf16 v[200:203], v[80:83], a[244:247], v[200:203]\n\t"
      "ds_read_b128 v[68:71], v1 offset:37888\n\t"
      "v_mfma_f32_16x16x32_bf16 v[208:211], v[80:83], a[248:251], v[208:211]\n\t"
      "v_mfma_f32_16x16x32_bf16 v[216:219], v[80:83], a[252:255], v[216:219]\n\t"
      "buffer_store_dwordx4 v[8:11], v7, %[rs], s58 offen sc1\n\t"
      "v_mfma_f32_16x16x32_bf16 v[196:199], v[84:87], a[240:243], v[196:199]\n\t"
      "v_mfma_f32_16x16x32_bf16 v[204:207], v[84:87], a[244:247], v[204:207]\n\t"
      "v_mfma_f32_16x16x32_bf16 v[212:215], v[84:87], a[248:251], v[212:215]\n\t"
      "s_waitcnt lgkmcnt(2)\n\t"
      "v_mfma_f32_16x16x32_bf16 v[220:223], v[84:87], a[252:255], v[220:223]\n\t"
      "v_mfma_f32_16x16x32_bf16 v[192:195], v[56:59], v[128:131], v[192:195]\n\t"
      "ds_read_b128 v[72:75], v1 offset:18432\n\t"
      "v_mfma_f32_16x16x32_bf16 v[200:203], v[56:59], v[132:135], v[200:203]\n\t"
      "ds_read_b128 v[76:79], v1 offset:38912\n\t"
      "v_mfma_f32_16x16x32_bf16 v[208:211], v[56:59], v[136:139], v[208:211]\n\t"
      "v_mfma_f32_16x16x32_bf16 v[216:219], v[56:59], v[140:143], v[216:219]\n\t"
      "v_mfma_f32_16x16x32_bf16 v[196:199], v[60:63], v[128:131], v[196:199]\n\t"
      "v_mfma_f32_16x16x32_bf16 v[204:207], v[60:63], v[132:135], v[204:207]\n\t"
      "v_mfma_f32_16x16x32_bf16 v[212:215], v[60:63], v[136:139], v[212:215]\n\t"
      "s_waitcnt lgkmcnt(2)\n\t"
      "v_mfma_f32_16x16x32_bf16 v[220:223], v[60:63], v[140:143], v[220:223]\n\t"
      "v_mfma_f32_16x16x32_bf16 v[192:195], v[64:67], v[144:147], v[192:195]\n\t"
      "ds_read_b128 v[80:83], v1 offset:19456\n\t"
      "v_mfma_f32_16x16x32_bf16 v[200:203], v[64:67], v[148:151], v[200:203]\n\t"
      "ds_read_b128 v[84:87], v1 offset:39936\n\t"
      "v_mfma_f32_16x16x32_bf16 v[208:211], v[64:67], v[152:155], v[208:211]\n\t"
      "v_mfma_f32_16x16x32_bf16 v[216:219], v[64:67], v[156:159], v[216:219]\n\t"
      "v_mfma_f32_16x16x32_bf16 v[196:199], v[68:71], v[144:147], v[196:199]\n\t"
      "v_mfma_f32_16x16x32_bf16 v[204:207], v[68:71], v[148:151], v[204:207]\n\t"
      "v_mfma_f32_16x16x32_bf16 v[212:215], v[68:71], v[152:155], v[212:215]\n\t"
      "s_waitcnt lgkmcnt(2)\n\t"
      "v_mfma_f32_16x16x32_bf16 v[220:223], v[68:71], v[156:159], v[220:223]\n\t"
      "s_waitcnt vmcnt(4) lgkmcnt(0)\n\t"
      "s_barrier\n\t"
      "v_mfma_f32_16x16x32_bf16 v[192:195], v[72:75], v[160:163], v[192:195]\n\t"
      "ds_read_b128 v[56:59], v13 offset:0\n\t"
      "v_mfma_f32_16x16x32_bf16 v[200:203], v[72:75], v[164:167], v[200:203]\n\t"
      "ds_read_b128 v[60:63], v13 offset:20480\n\t"
      "v_mfma_f32_16x16x32_bf16 v[208:211], v[72:75], v[168:171], v[208:211]\n\t"
      "v_mfma_f32_16x16x32_bf16 v[216:219], v[72:75], v[172:175], v[216:219]\n\t"
      "v_mfma_f32_16x16x32_bf16 v[196:199], v[76:79], v[160:163], v[196:199]\n\t"
      "v_mfma_f32_16x16x32_bf16 v[204:207], v[76:79], v[164:167], v[204:207]\n\t"
      "v_mfma_f32_16x16x32_bf16 v[212:215], v[76:79], v[168:171], v[212:215]\n\t"
      "v_mfma_f32_16x16x32_bf16 v[220:223], v[76:79], v[172:175], v[220:223]\n\t"
      "v_mfma_f32_16x16x32_bf16 v[192:195], v[80:83], v[176:179], v[192:195]\n\t"
      "ds_read_b128 v[64:67], v13 offset:1024\n\t"
      "v_mfma_f32_16x16x32_bf16 v[200:203], v[80:83], v[180:183], v[200:203]\n\t"
      "ds_read_b128 v[68:71], v13 offset:21504\n\t"
      "v_mfma_f32_16x16x32_bf16 v[208:211], v[80:83], v[184:187], v[208:211]\n\t"
      "v_mfma_f32_16x16x32_bf16 v[216:219], v[80:83], v[188:191], v[216:219]\n\t"
      "v_mov_b32 v1, v13\n\t"
      "v_mfma_f32_16x16x32_bf16 v[196:199], v[84:87], v[176:179], v[196:199]\n\t"
      "v_mfma_f32_16x16x32_bf16 v[204:207], v[84:87], v[180:183], v[204:207]\n\t"
      "s_add_u32 s40, s40, 1\n\t"
      "s_cmp_lt_u32 s40, 63\n\t"
      "v_mfma_f32_16x16x32_bf16 v[212:215], v[84:87], v[184:187], v[212:215]\n\t"
      "s_waitcnt lgkmcnt(2)\n\t"
      "v_mfma_f32_16x16x32_bf16 v[220:223], v[84:87], v[188:191], v[220:223]\n\t"
      "s_cbranch_scc0 LLAST%=\n\t"
      "v_mfma_f32_16x16x32_bf16 v[24:27], v[56:59], a[0:3], 0\n\t"
      "ds_read_b128 v[72:75], v1 offset:2048\n\t"
      "v_mfma_f32_16x16x32_bf16 v[32:35], v[56:59], a[4:7], 0\n\t"
      "ds_read_b128 v[76:79], v1 offset:22528\n\t"
      "v_mfma_f32_16x16x32_bf16 v[40:43], v[56:59], a[8:11], 0\n\t"
      "s_xor_b32 s54, s54, 65536\n\t"
      "s_add_u32 s43, s40, 1\n\t"
      "v_mfma_f32_16x16x32_bf16 v[48:51], v[56:59], a[12:15], 0\n\t"
      "s_min_u32 s43, s43, 63\n\t"
      "s_mul_i32 s43, s43, 40960\n\t"
      "v_mfma_f32_16x16x32_bf16 v[28:31], v[60:63], a[0:3], 0\n\t"
      "s_add_u32 s52, s43, 88080384\n\t"
      "s_mul_i32 s57, s40, %[ms]\n\t"
      "v_mfma_f32_16x16x32_bf16 v[36:39], v[60:63], a[4:7], 0\n\t"
      "s_mul_i32 s58, s40, 110592\n\t"
      "s_sub_u32 s56, s40, 1\n\t"
      "v_mfma_f32_16x16x32_bf16 v[44:47], v[60:63], a[8:11], 0\n\t"
      "s_mul_i32 s53, s56, 24576\n\t"
      "s_add_u32 s55, s53, 4096\n\t"
      "s_waitcnt lgkmcnt(2)\n\t"
      "v_mfma_f32_16x16x32_bf16 v[52:55], v[60:63], a[12:15], 0\n\t"
      "v_mov_b32 v5, s56\n\t"
      "v_xor_b32 v13, 65536, v1\n\t"
      "v_mfma_f32_16x16x32_bf16 v[24:27], v[64:67], a[16:19], v[24:27]\n\t"
      "ds_read_b128 v[80:83], v1 offset:3072\n\t"
      "v_cvt_pk_bf16_f32 v88, v192, v193\n\t"
      "v_cvt_pk_bf16_f32 v89, v194, v195\n\t"
      "v_mfma_f32_16x16x32_bf16 v[32:35], v[64:67], a[20:23], v[32:35]\n\t"
      "ds_read_b128 v[84:87], v1 offset:23552\n\t"
      "v_cvt_pk_bf16_f32 v90, v196, v197\n\t"
      "v_cvt_pk_bf16_f32 v91, v198, v199\n\t"
      "v_mfma_f32_16x16x32_bf16 v[40:43], v[64:67], a[24:27], v[40:43]\n\t"
      "buffer_store_dwordx4 v[88:91], v2, %[rs], s53 offen offset:0 sc1\n\t"
      "v_mfma_f32_16x16x32_bf16 v[48:51], v[64:67], a[28:31], v[48:51]\n\t"
      "v_mfma_f32_16x16x32_bf16 v[28:31], v[68:71], a[16:19], v[28:31]\n\t"
      "v_mfma_f32_16x16x32_bf16 v[36:39], v[68:71], a[20:23], v[36:39]\n\t"
      "s_add_u32 m0, s54, 0\n\t"
      "s_add_u32 s43, s52, 0\n\t"
      "buffer_load_dwordx4 v0, %[rs], s43 offen sc1 lds\n\t"
      "v_mfma_f32_16x16x32_bf16 v[44:47], v[68:71], a[24:27], v[44:47]\n\t"
      "s_waitcnt lgkmcnt(2)\n\t"
      "v_mfma_f32_16x16x32_bf16 v[52:55], v[68:71], a[28:31], v[52:55]\n\t"
      "v_mfma_f32_16x16x32_bf16 v[24:27], v[72:75], a[32:35], v[24:27]\n\t"
      "ds_read_b128 v[56:59], v1 offset:4096\n\t"
      "v_cvt_pk_bf16_f32 v92, v200, v201\n\t"
      "v_cvt_pk_bf16_f32 v93, v202, v203\n\t"
      "v_mfma_f32_16x16x32_bf16 v[32:35], v[72:75], a[36:39], v[32:35]\n\t"
      "ds_read_b128 v[60:63], v1 offset:24576\n\t"
      "v_cvt_pk_bf16_f32 v94, v204, v205\n\t"
      "v_cvt_pk_bf16_f32 v95, v206, v207\n\t"
      "v_mfma_f32_16x16x32_bf16 v[40:43], v[72:75], a[40:43], v[40:43]\n\t"
      "buffer_store_dwordx4 v[92:95], v2, %[rs], s53 offen offset:1024 sc1\n\t"
      "v_mfma_f32_16x16x32_bf16 v[48:51], v[72:75], a[44:47], v[48:51]\n\t"
      "v_mfma_f32_16x16x32_bf16 v[28:31], v[76:79], a[32:35], v[28:31]\n\t"
      "v_mfma_f32_16x16x32_bf16 v[36:39], v[76:79], a[36:39], v[36:39]\n\t"
      "s_add_u32 m0, s54, 4096\n\t"
      "s_add_u32 s43, s52, 4096\n\t"
      "buffer_load_dwordx4 v0, %[rs], s43 offen sc1 lds\n\t"
      "v_mfma_f32_16x16x32_bf16 v[44:47], v[76:79], a[40:43], v[44:47]\n\t"
      "s_waitcnt lgkmcnt(2)\n\t"
      "v_mfma_f32_16x16x32_bf16 v[52:55], v[76:79], a[44:47], v[52:55]\n\t"
      "v_mfma_f32_16x16x32_bf16 v[24:27], v[80:83], a[48:51], v[24:27]\n\t"
      "ds_read_b128 v[64:67], v1 offset:5120\n\t"
      "v_cvt_pk_bf16_f32 v96, v208, v209\n\t"
      "v_cvt_pk_bf16_f32 v97, v210, v211\n\t"
      "v_mfma_f32_16x16x32_bf16 v[32:35], v[80:83], a[52:55], v[32:35]\n\t"
      "ds_read_b128 v[68:71], v1 offset:25600\n\t"
      "v_cvt_pk_bf16_f32 v98, v212, v213\n\t"
      "v_cvt_pk_bf16_f32 v99, v214, v215\n\t"
      "v_mfma_f32_16x16x32_bf16 v[40:43], v[80:83], a[56:59], v[40:43]\n\t"
      "buffer_store_dwordx4 v[96:99], v2, %[rs], s53 offen offset:2048 sc1\n\t"
      "v_mfma_f32_16x16x32_bf16 v[48:51], v[80:83], a[60:63], v[48:51]\n\t"
      "v_mfma_f32_16x16x32_bf16 v[28:31], v[84:87], a[48:51], v[28:31]\n\t"
      "v_mfma_f32_16x16x32_bf16 v[36:39], v[84:87], a[52:55], v[36:39]\n\t"
      "s_add_u32 m0, s54, 8192\n\t"
      "s_add_u32 s43, s52, 8192\n\t"
      "buffer_load_dwordx4 v0, %[rs], s43 offen sc1 lds\n\t"
      "v_mfma_f32_16x16x32_bf16 v[44:47], v[84:87], a[56:59], v[44:47]\n\t"
      "s_waitcnt lgkmcnt(2)\n\t"
      "v_mfma_f32_16x16x32_bf16 v[52:55], v[84:87], a[60:63], v[52:55]\n\t"
      "v_mfma_f32_16x16x32_bf16 v[24:27], v[56:59], a[64:67], v[24:27]\n\t"
      "ds_read_b128 v[72:75], v1 offset:6144\n\t"
      "v_cvt_pk_bf16_f32 v100, v216, v217\n\t"
      "v_cvt_pk_bf16_f32 v101, v218, v219\n\t"
      "v_mfma_f32_16x16x32_bf16 v[32:35], v[56:59], a[68:71], v[32:35]\n\t"
      "ds_read_b128 v[76:79], v1 offset:26624\n\t"
      "v_cvt_pk_bf16_f32 v102, v220, v221\n\t"
      "v_cvt_pk_bf16_f32 v103, v222, v223\n\t"
      "v_mfma_f32_16x16x32_bf16 v[40:43], v[56:59], a[72:75], v[40:43]\n\t"
      "buffer_store_dwordx4 v[100:103], v2, %[rs], s53 offen offset:3072 sc1\n\t"
      "v_mfma_f32_16x16x32_bf16 v[48:51], v[56:59], a[76:79], v[48:51]\n\t"
      "v_mfma_f32_16x16x32_bf16 v[28:31], v[60:63], a[64:67], v[28:31]\n\t"
      "v_mfma_f32_16x16x32_bf16 v[36:39], v[60:63], a[68:71], v[36:39]\n\t"
      "s_add_u32 m0, s54, 12288\n\t"
      "s_add_u32 s43, s52, 12288\n\t"
      "buffer_load_dwordx4 v0, %[rs], s43 offen sc1 lds\n\t"
      "v_mfma_f32_16x16x32_bf16 v[44:47], v[60:63], a[72:75], v[44:47]\n\t"
      "s_waitcnt lgkmcnt(2)\n\t"
      "v_mfma_f32_16x16x32_bf16 v[52:55], v[60:63], a[76:79], v[52:55]\n\t"
      "v_mfma_f32_16x16x32_bf16 v[24:27], v[64:67], a[80:83], v[24:27]\n\t"
      "ds_read_b128 v[80:83], v1 offset:7168\n\t"
      "v_mfma_f32_16x16x32_bf16 v[32:35], v[64:67], a[84:87], v[32:35]\n\t"
      "ds_read_b128 v[84:87], v1 offset:27648\n\t"
      "v_mfma_f32_16x16x32_bf16 v[40:43], v[64:67], a[88:91], v[40:43]\n\t"
      "v_mfma_f32_16x16x32_bf16 v[48:51], v[64:67], a[92:95], v[48:51]\n\t"
      "v_mfma_f32_16x16x32_bf16 v[28:31], v[68:71], a[80:83], v[28:31]\n\t"
      "v_mfma_f32_16x16x32_bf16 v[36:39], v[68:71], a[84:87], v[36:39]\n\t"
      "s_add_u32 m0, s54, 16384\n\t"
      "s_add_u32 s43, s52, 16384\n\t"
      "buffer_load_dwordx4 v0, %[rs], s43 offen sc1 lds\n\t"
      "v_mfma_f32_16x16x32_bf16 v[44:47], v[68:71], a[88:91], v[44:47]\n\t"
      "s_waitcnt lgkmcnt(2)\n\t"
      "v_mfma_f32_16x16x32_bf16 v[52:55], v[68:71], a[92:95], v[52:55]\n\t"
      "v_mfma_f32_16x16x32_bf16 v[24:27], v[72:75], a[96:99], v[24:27]\n\t"
      "ds_read_b128 v[56:59], v1 offset:8192\n\t"
      "v_mfma_f32_16x16x32_bf16 v[32:35], v[72:75], a[100:103], v[32:35]\n\t"
      "ds_read_b128 v[60:63], v1 offset:28672\n\t"
      "v_mfma_f32_16x16x32_bf16 v[40:43], v[72:75], a[104:107], v[40:43]\n\t"
      "v_mfma_f32_16x16x32_bf16 v[48:51], v[72:75], a[108:111], v[48:51]\n\t"
      "v_mfma_f32_16x16x32_bf16 v[28:31], v[76:79], a[96:99], v[28:31]\n\t"
      "v_mfma_f32_16x16x32_bf16 v[36:39], v[76:79], a[100:103], v[36:39]\n\t"
      "s_add_u32 m0, s54, 20480\n\t"
      "s_add_u32 s43, s52, 20480\n\t"
      "buffer_load_dwordx4 v0, %[rs], s43 offen sc1 lds\n\t"
      "v_mfma_f32_16x16x32_bf16 v[44:47], v[76:79], a[104:107], v[44:47]\n\t"
      "s_waitcnt lgkmcnt(2)\n\t"
      "v_mfma_f32_16x16x32_bf16 v[52:55], v[76:79], a[108:111], v[52:55]\n\t"
      "v_mfma_f32_16x16x32_bf16 v[24:27], v[80:83], a[112:115], v[24:27]\n\t"
      "ds_read_b128 v[64:67], v1 offset:9216\n\t"
      "v_mfma_f32_16x16x32_bf16 v[32:35], v[80:83], a[116:119], v[32:35]\n\t"
      "ds_read_b128 v[68:71], v1 offset:29696\n\t"
      "v_mfma_f32_16x16x32_bf16 v[40:43], v[80:83], a[120:123], v[40:43]\n\t"
      "v_mfma_f32_16x16x32_bf16 v[48:51], v[80:83], a[124:127], v[48:51]\n\t"
      "v_mfma_f32_16x16x32_bf16 v[28:31], v[84:87], a[112:115], v[28:31]\n\t"
      "v_mfma_f32_16x16x32_bf16 v[36:39], v[84:87], a[116:119], v[36:39]\n\t"
      "s_add_u32 m0, s54, 24576\n\t"
      "s_add_u32 s43, s52, 24576\n\t"
      "buffer_load_dwordx4 v0, %[rs], s43 offen sc1 lds\n\t"
      "v_mfma_f32_16x16x32_bf16 v[44:47], v[84:87], a[120:123], v[44:47]\n\t"
      "s_waitcnt lgkmcnt(2)\n\t"
      "v_mfma_f32_16x16x32_bf16 v[52:55], v[84:87], a[124:127], v[52:55]\n\t"
      "v_mfma_f32_16x16x32_bf16 v[24:27], v[56:59], a[128:131], v[24:27]\n\t"
      "ds_read_b128 v[72:75], v1 offset:10240\n\t"
      "v_mfma_f32_16x16x32_bf16 v[32:35], v[56:59], a[132:135], v[32:35]\n\t"
      "ds_read_b128 v[76:79], v1 offset:30720\n\t"
      "v_mfma_f32_16x16x32_bf16 v[40:43], v[56:59], a[136:139], v[40:43]\n\t"
      "v_mfma_f32_16x16x32_bf16 v[48:51], v[56:59], a[140:143], v[48:51]\n\t"
      "v_mfma_f32_16x16x32_bf16 v[28:31], v[60:63], a[128:131], v[28:31]\n\t"
      "v_mfma_f32_16x16x32_bf16 v[36:39], v[60:63], a[132:135], v[36:39]\n\t"
      "s_add_u32 m0, s54, 28672\n\t"
      "s_add_u32 s43, s52, 28672\n\t"
      "buffer_load_dwordx4 v0, %[rs], s43 offen sc1 lds\n\t"
      "v_mfma_f32_16x16x32_bf16 v[44:47], v[60:63], a[136:139], v[44:47]\n\t"
      "s_waitcnt lgkmcnt(2)\n\t"
      "v_mfma_f32_16x16x32_bf16 v[52:55], v[60:63], a[140:143], v[52:55]\n\t"
      "v_mfma_f32_16x16x32_bf16 v[24:27], v[64:67], a[144:147], v[24:27]\n\t"
      "ds_read_b128 v[80:83], v1 offset:11264\n\t"
      "v_mfma_f32_16x16x32_bf16 v[32:35], v[64:67], a[148:151], v[32:35]\n\t"
      "ds_read_b128 v[84:87], v1 offset:31744\n\t"
      "v_mfma_f32_16x16x32_bf16 v[40:43], v[64:67], a[152:155], v[40:43]\n\t"
      "v_mfma_f32_16x16x32_bf16 v[48:51], v[64:67], a[156:159], v[48:51]\n\t"
      "v_mfma_f32_16x16x32_bf16 v[28:31], v[68:71], a[144:147], v[28:31]\n\t"
      "v_mfma_f32_16x16x32_bf16 v[36:39], v[68:71], a[148:151], v[36:39]\n\t"
      "s_add_u32 m0, s54, 32768\n\t"
      "s_add_u32 s43, s52, 32768\n\t"
      "buffer_load_dwordx4 v0, %[rs], s43 offen sc1 lds\n\t"
      "v_mfma_f32_16x16x32_bf16 v[44:47], v[68:71], a[152:155], v[44:47]\n\t"
      "s_waitcnt lgkmcnt(2)\n\t"
      "v_mfma_f32_16x16x32_bf16 v[52:55], v[68:71], a[156:159], v[52:55]\n\t"
      "v_mfma_f32_16x16x32_bf16 v[24:27], v[72:75], a[160:163], v[24:27]\n\t"
      "ds_read_b128 v[56:59], v1 offset:12288\n\t"
      "v_mfma_f32_16x16x32_bf16 v[32:35], v[72:75], a[164:167], v[32:35]\n\t"
      "ds_read_b128 v[60:63], v1 offset:32768\n\t"
      "v_mfma_f32_16x16x32_bf16 v[40:43], v[72:75], a[168:171], v[40:43]\n\t"
      "v_mfma_f32_16x16x32_bf16 v[48:51], v[72:75], a[172:175], v[48:51]\n\t"
      "v_mfma_f32_16x16x32_bf16 v[28:31], v[76:79], a[160:163], v[28:31]\n\t"
      "v_mfma_f32_16x16x32_bf16 v[36:39], v[76:79], a[164:167], v[36:39]\n\t"
      "s_add_u32 m0, s54, 36864\n\t"
      "s_add_u32 s43, s52, 36864\n\t"
      "buffer_load_dwordx4 v0, %[rs], s43 offen sc1 lds\n\t"
      "v_mfma_f32_16x16x32_bf16 v[44:47], v[76:79], a[168:171], v[44:47]\n\t"
      "s_waitcnt lgkmcnt(2)\n\t"
      "v_mfma_f32_16x16x32_bf16 v[52:55], v[76:79], a[172:175], v[52:55]\n\t"
      "v_mfma_f32_16x16x32_bf16 v[24:27], v[80:83], a[176:179], v[24:27]\n\t"
      "ds_read_b128 v[64:67], v1 offset:13312\n\t"
      "v_mfma_f32_16x16x32_bf16 v[32:35], v[80:83], a[180:183], v[32:35]\n\t"
      "ds_read_b128 v[68:71], v1 offset:33792\n\t"
      "v_mfma_f32_16x16x32_bf16 v[40:43], v[80:83], a[184:187], v[40:43]\n\t"
      "v_mfma_f32_16x16x32_bf16 v[48:51], v[80:83], a[188:191], v[48:51]\n\t"
      "s_waitcnt vmcnt(7)\n\t"
      "buffer_store_dword v5, v4, %[rs], %[flo] offen sc1\n\t"
      "v_mfma_f32_16x16x32_bf16 v[28:31], v[84:87], a[176:179], v[28:31]\n\t"
      "v_mfma_f32_16x16x32_bf16 v[36:39], v[84:87], a[180:183], v[36:39]\n\t"
      "v_mfma_f32_16x16x32_bf16 v[44:47], v[84:87], a[184:187], v[44:47]\n\t"
      "s_waitcnt lgkmcnt(2)\n\t"
      "v_mfma_f32_16x16x32_bf16 v[52:55], v[84:87], a[188:191], v[52:55]\n\t"
      "v_mfma_f32_16x16x32_bf16 v[24:27], v[56:59], a[192:195], v[24:27]\n\t"
      "ds_read_b128 v[72:75], v1 offset:14336\n\t"
      "v_mfma_f32_16x16x32_bf16 v[32:35], v[56:59], a[196:199], v[32:35]\n\t"
      "ds_read_b128 v[76:79], v1 offset:34816\n\t"
      "v_mfma_f32_16x16x32_bf16 v[40:43], v[56:59], a[200:203], v[40:43]\n\t"
      "v_mfma_f32_16x16x32_bf16 v[48:51], v[56:59], a[204:207], v[48:51]\n\t"
      "v_mfma_f32_16x16x32_bf16 v[28:31], v[60:63], a[192:195], v[28:31]\n\t"
      "v_mfma_f32_16x16x32_bf16 v[36:39], v[60:63], a[196:199], v[36:39]\n\t"
      "v_mfma_f32_16x16x32_bf16 v[44:47], v[60:63], a[200:203], v[44:47]\n\t"
      "s_waitcnt lgkmcnt(2)\n\t"
      "v_mfma_f32_16x16x32_bf16 v[52:55], v[60:63], a[204:207], v[52:55]\n\t"
      "v_mfma_f32_16x16x32_bf16 v[24:27], v[64:67], a[208:211], v[24:27]\n\t"
      "ds_read_b128 v[80:83], v1 offset:15360\n\t"
      "v_mfma_f32_16x16x32_bf16 v[32:35], v[64:67], a[212:215], v[32:35]\n\t"
      "ds_read_b128 v[84:87], v1 offset:35840\n\t"
      "v_mfma_f32_16x16x32_bf16 v[40:43], v[64:67], a[216:219], v[40:43]\n\t"
      "v_mfma_f32_16x16x32_bf16 v[48:51], v[64:67], a[220:223], v[48:51]\n\t"
      "buffer_store_dwordx4 v[8:11], v3, %[rs], s57 offen sc1\n\t"
      "v_mfma_f32_16x16x32_bf16 v[28:31], v[68:71], a[208:211], v[28:31]\n\t"
      "v_mfma_f32_16x16x32_bf16 v[36:39], v[68:71], a[212:215], v[36:39]\n\t"
      "v_mfma_f32_16x16x32_bf16 v[44:47], v[68:71], a[216:219], v[44:47]\n\t"
      "s_waitcnt lgkmcnt(2)\n\t"
      "v_mfma_f32_16x16x32_bf16 v[52:55], v[68:71], a[220:223], v[52:55]\n\t"
      "v_mfma_f32_16x16x32_bf16 v[24:27], v[72:75], a[224:227], v[24:27]\n\t"
      "ds_read_b128 v[56:59], v1 offset:16384\n\t"
      "v_mfma_f32_16x16x32_bf16 v[32:35], v[72:75], a[228:231], v[32:35]\n\t"
      "ds_read_b128 v[60:63], v1 offset:36864\n\t"
      "v_mfma_f32_16x16x32_bf16 v[40:43], v[72:75], a[232:235], v[40:43]\n\t"
      "v_mfma_f32_16x16x32_bf16 v[48:51], v[72:75], a[236:239], v[48:51]\n\t"
      "buffer_store_dwordx4 v[8:11], v6, %[rs], s58 offen sc1\n\t"
      "v_mfma_f32_16x16x32_bf16 v[28:31], v[76:79], a[224:227], v[28:31]\n\t"
      "v_mfma_f32_16x16x32_bf16 v[36:39], v[76:79], a[228:231], v[36:39]\n\t"
      "v_mfma_f32_16x16x32_bf16 v[44:47], v[76:79], a[232:235], v[44:47]\n\t"
      "s_waitcnt lgkmcnt(2)\n\t"
      "v_mfma_f32_16x16x32_bf16 v[52:55], v[76:79], a[236:239], v[52:55]\n\t"
      "v_mfma_f32_16x16x32_bf16 v[24:27], v[80:83], a[240:243], v[24:27]\n\t"
      "ds_read_b128 v[64:67], v1 offset:17408\n\t"
      "v_mfma_f32_16x16x32_bf16 v[32:35], v[80:83], a[244:247], v[32:35]\n\t"
      "ds_read_b128 v[68:71], v1 offset:37888\n\t"
      "v_mfma_f32_16x16x32_bf16 v[40:43], v[80:83], a[248:251], v[40:43]\n\t"
      "v_mfma_f32_16x16x32_bf16 v[48:51], v[80:83], a[252:255], v[48:51]\n\t"
      "buffer_store_dwordx4 v[8:11], v7, %[rs], s58 offen sc1\n\t"
      "v_mfma_f32_16x16x32_bf16 v[28:31], v[84:87], a[240:243], v[28:31]\n\t"
      "v_mfma_f32_16x16x32_bf16 v[36:39], v[84:87], a[244:247], v[36:39]\n\t"
      "v_mfma_f32_16x16x32_bf16 v[44:47], v[84:87], a[248:251], v[44:47]\n\t"
      "s_waitcnt lgkmcnt(2)\n\t"
      "v_mfma_f32_16x16x32_bf16 v[52:55], v[84:87], a[252:255], v[52:55]\n\t"
      "v_mfma_f32_16x16x32_bf16 v[24:27], v[56:59], v[128:131], v[24:27]\n\t"
      "ds_read_b128 v[72:75], v1 offset:18432\n\t"
      "v_mfma_f32_16x16x32_bf16 v[32:35], v[56:59], v[132:135], v[32:35]\n\t"
      "ds_read_b128 v[76:79], v1 offset:38912\n\t"
      "v_mfma_f32_16x16x32_bf16 v[40:43], v[56:59], v[136:139], v[40:43]\n\t"
      "v_mfma_f32_16x16x32_bf16 v[48:51], v[56:59], v[140:143], v[48:51]\n\t"
      "v_mfma_f32_16x16x32_bf16 v[28:31], v[60:63], v[128:131], v[28:31]\n\t"
      "v_mfma_f32_16x16x32_bf16 v[36:39], v[60:63], v[132:135], v[36:39]\n\t"
      "v_mfma_f32_16x16x32_bf16 v[44:47], v[60:63], v[136:139], v[44:47]\n\t"
      "s_waitcnt lgkmcnt(2)\n\t"
      "v_mfma_f32_16x16x32_bf16 v[52:55], v[60:63], v[140:143], v[52:55]\n\t"
      "v_mfma_f32_16x16x32_bf16 v[24:27], v[64:67], v[144:147], v[24:27]\n\t"
      "ds_read_b128 v[80:83], v1 offset:19456\n\t"
      "v_mfma_f32_16x16x32_bf16 v[32:35], v[64:67], v[148:151], v[32:35]\n\t"
      "ds_read_b128 v[84:87], v1 offset:39936\n\t"
      "v_mfma_f32_16x16x32_bf16 v[40:43], v[64:67], v[152:155], v[40:43]\n\t"
      "v_mfma_f32_16x16x32_bf16 v[48:51], v[64:67], v[156:159], v[48:51]\n\t"
      "v_mfma_f32_16x16x32_bf16 v[28:31], v[68:71], v[144:147], v[28:31]\n\t"
      "v_mfma_f32_16x16x32_bf16 v[36:39], v[68:71], v[148:151], v[36:39]\n\t"
      "v_mfma_f32_16x16x32_bf16 v[44:47], v[68:71], v[152:155], v[44:47]\n\t"
      "s_waitcnt lgkmcnt(2)\n\t"
      "v_mfma_f32_16x16x32_bf16 v[52:55], v[68:71], v[156:159], v[52:55]\n\t"
      "s_waitcnt vmcnt(4) lgkmcnt(0)\n\t"
      "s_barrier\n\t"
      "v_mfma_f32_16x16x32_bf16 v[24:27], v[72:75], v[160:163], v[24:27]\n\t"
      "ds_read_b128 v[56:59], v13 offset:0\n\t"
      "v_mfma_f32_16x16x32_bf16 v[32:35], v[72:75], v[164:167], v[32:35]\n\t"
      "ds_read_b128 v[60:63], v13 offset:20480\n\t"
      "v_mfma_f32_16x16x32_bf16 v[40:43], v[72:75], v[168:171], v[40:43]\n\t"
      "v_mfma_f32_16x16x32_bf16 v[48:51], v[72:75], v[172:175], v[48:51]\n\t"
      "v_mfma_f32_16x16x32_bf16 v[28:31], v[76:79], v[160:163], v[28:31]\n\t"
      "v_mfma_f32_16x16x32_bf16 v[36:39], v[76:79], v[164:167], v[36:39]\n\t"
      "v_mfma_f32_16x16x32_bf16 v[44:47], v[76:79], v[168:171], v[44:47]\n\t"
      "v_mfma_f32_16x16x32_bf16 v[52:55], v[76:79], v[172:175], v[52:55]\n\t"
      "v_mfma_f32_16x16x32_bf16 v[24:27], v[80:83], v[176:179], v[24:27]\n\t"
      "ds_read_b128 v[64:67], v13 offset:1024\n\t"
      "v_mfma_f32_16x16x32_bf16 v[32:35], v[80:83], v[180:183], v[32:35]\n\t"
      "ds_read_b128 v[68:71], v13 offset:21504\n\t"
      "v_mfma_f32_16x16x32_bf16 v[40:43], v[80:83], v[184:187], v[40:43]\n\t"
      "v_mfma_f32_16x16x32_bf16 v[48:51], v[80:83], v[188:191], v[48:51]\n\t"
      "v_mov_b32 v1, v13\n\t"
      "v_mfma_f32_16x16x32_bf16 v[28:31], v[84:87], v[176:179], v[28:31]\n\t"
      "v_mfma_f32_16x16x32_bf16 v[36:39], v[84:87], v[180:183], v[36:39]\n\t"
      "s_add_u32 s40, s40, 1\n\t"
      "s_cmp_lt_u32 s40, 63\n\t"
      "v_mfma_f32_16x16x32_bf16 v[44:47], v[84:87], v[184:187], v[44:47]\n\t"
      "s_waitcnt lgkmcnt(2)\n\t"
      "v_mfma_f32_16x16x32_bf16 v[52:55], v[84:87], v[188:191], v[52:55]\n\t"
      "s_branch LOOP%=\n\t"
      "LLAST%=:\n\t"
      "s_mov_b32 s53, 1548288\n\t"
      "s_mov_b32 s55, 1552384\n\t"
      "v_mov_b32 v5, 63\n\t"
      "s_nop 4\n\t"
      "s_waitcnt lgkmcnt(0)\n\t"
      "v_cvt_pk_bf16_f32 v88, v192, v193\n\t"
      "v_cvt_pk_bf16_f32 v89, v194, v195\n\t"
      "v_cvt_pk_bf16_f32 v90, v196, v197\n\t"
      "v_cvt_pk_bf16_f32 v91, v198, v199\n\t"
      "buffer_store_dwordx4 v[88:91], v2, %[rs], s53 offen offset:0 sc1\n\t"
      "v_cvt_pk_bf16_f32 v92, v200, v201\n\t"
      "v_cvt_pk_bf16_f32 v93, v202, v203\n\t"
      "v_cvt_pk_bf16_f32 v94, v204, v205\n\t"
      "v_cvt_pk_bf16_f32 v95, v206, v207\n\t"
      "buffer_store_dwordx4 v[92:95], v2, %[rs], s53 offen offset:1024 sc1\n\t"
      "v_cvt_pk_bf16_f32 v96, v208, v209\n\t"
      "v_cvt_pk_bf16_f32 v97, v210, v211\n\t"
      "v_cvt_pk_bf16_f32 v98, v212, v213\n\t"
      "v_cvt_pk_bf16_f32 v99, v214, v215\n\t"
      "buffer_store_dwordx4 v[96:99], v2, %[rs], s53 offen offset:2048 sc1\n\t"
      "v_cvt_pk_bf16_f32 v100, v216, v217\n\t"
      "v_cvt_pk_bf16_f32 v101, v218, v219\n\t"
      "v_cvt_pk_bf16_f32 v102, v220, v221\n\t"
      "v_cvt_pk_bf16_f32 v103, v222, v223\n\t"
      "buffer_store_dwordx4 v[100:103], v2, %[rs], s53 offen offset:3072 sc1\n\t"
      "s_waitcnt vmcnt(0)\n\t"
      "buffer_store_dword v5, v4, %[rs], %[flo] offen sc1\n\t"
      "s_branch LEND%=\n\t"
      "LFAIL%=:\n\t"
      "v_mov_b32 v12, 1\n\t"
      "s_mov_b32 s43, 688128\n\t"
      "buffer_store_dword v12, off, %[rs], s43 sc1\n\t"
      "LEND%=:\n\t"
      "s_waitcnt vmcnt(0) lgkmcnt(0)\n\t"
      "s_mov_b64 exec, -1\n\t"
      "s_mov_b32 m0, s39\n\t"
      : "+{v[0:7]}"(ad), "+{v[16:23]}"(ld)
      : [rs] "s"(rs), [rw] "s"(rw), [w] "s"(w), [st] "s"(uni(stage)), [cb] "s"(cb), [t0] "s"(tag.x & 0x7FFFFFFFu), [flo] "s"(flo), [ms] "i"(46080)
      : "memory", "vcc", "scc", "v8", "v9", "v10", "v11", "v12", "v13", "v14", "v15", "v24", "v25", "v26", "v27", "v28", "v29", "v30", "v31", "v32", "v33",
        "v34", "v35", "v36", "v37", "v38", "v39", "v40", "v41", "v42", "v43", "v44", "v45", "v46", "v47", "v48", "v49", "v50", "v51", "v52", "v53", "v54",
        "v55", "v56", "v57", "v58", "v59", "v60", "v61", "v62", "v63", "v64", "v65", "v66", "v67", "v68", "v69", "v70", "v71", "v72", "v73", "v74", "v75",
        "v76", "v77", "v78", "v79", "v80", "v81", "v82", "v83", "v84", "v85", "v86", "v87", "v88", "v89", "v90", "v91", "v92", "v93", "v94", "v95", "v96",
        "v97", "v98", "v99", "v100", "v101", "v102", "v103", "v104", "v105", "v106", "v107", "v108", "v109", "v110", "v111", "v112", "v113", "v114", "v115",
        "v116", "v117", "v118", "v119", "v120", "v121", "v122", "v123", "v124", "v125", "v126", "v127", "v128", "v129", "v130", "v131", "v132", "v133",
        "v134", "v135", "v136", "v137", "v138", "v139", "v140", "v141", "v142", "v143", "v144", "v145", "v146", "v147", "v148", "v149", "v150", "v151",
        "v152", "v153", "v154", "v155", "v156", "v157", "v158", "v159", "v160", "v161", "v162", "v163", "v164", "v165", "v166", "v167", "v168", "v169",
        "v170", "v171", "v172", "v173", "v174", "v175", "v176", "v177", "v178", "v179", "v180", "v181", "v182", "v183", "v184", "v185", "v186", "v187",
        "v188", "v189", "v190", "v191", "v192", "v193", "v194", "v195", "v196", "v197", "v198", "v199", "v200", "v201", "v202", "v203", "v204", "v205",
        "v206", "v207", "v208", "v209", "v210", "v211", "v212", "v213", "v214", "v215", "v216", "v217", "v218", "v219", "v220", "v221", "v222", "v223", "a0",
        "a1", "a2", "a3", "a4", "a5", "a6", "a7", "a8", "a9", "a10", "a11", "a12", "a13", "a14", "a15", "a16", "a17", "a18", "a19", "a20", "a21", "a22",
        "a23", "a24", "a25", "a26", "a27", "a28", "a29", "a30", "a31", "a32", "a33", "a34", "a35", "a36", "a37", "a38", "a39", "a40", "a41", "a42", "a43",
        "a44", "a45", "a46", "a47", "a48", "a49", "a50", "a51", "a52", "a53", "a54", "a55", "a56", "a57", "a58", "a59", "a60", "a61", "a62", "a63", "a64",
        "a65", "a66", "a67", "a68", "a69", "a70", "a71", "a72", "a73", "a74", "a75", "a76", "a77", "a78", "a79", "a80", "a81", "a82", "a83", "a84", "a85",
        "a86", "a87", "a88", "a89", "a90", "a91", "a92", "a93", "a94", "a95", "a96", "a97", "a98", "a99", "a100", "a101", "a102", "a103", "a104", "a105",
        "a106", "a107", "a108", "a109", "a110", "a111", "a112", "a113", "a114", "a115", "a116", "a117", "a118", "a119", "a120", "a121", "a122", "a123",
        "a124", "a125", "a126", "a127", "a128", "a129", "a130", "a131", "a132", "a133", "a134", "a135", "a136", "a137", "a138", "a139", "a140", "a141",
        "a142", "a143", "a144", "a145", "a146", "a147", "a148", "a149", "a150", "a151", "a152", "a153", "a154", "a155", "a156", "a157", "a158", "a159",
        "a160", "a161", "a162", "a163", "a164", "a165", "a166", "a167", "a168", "a169", "a170", "a171", "a172", "a173", "a174", "a175", "a176", "a177",
        "a178", "a179", "a180", "a181", "a182", "a183", "a184", "a185", "a186", "a187", "a188", "a189", "a190", "a191", "a192", "a193", "a194", "a195",
        "a196", "a197", "a198", "a199", "a200", "a201", "a202", "a203", "a204", "a205", "a206", "a207", "a208", "a209", "a210", "a211", "a212", "a213",
        "a214", "a215", "a216", "a217", "a218", "a219", "a220", "a221", "a222", "a223", "a224", "a225", "a226", "a227", "a228", "a229", "a230", "a231",
        "a232", "a233", "a234", "a235", "a236", "a237", "a238", "a239", "a240", "a241", "a242", "a243", "a244", "a245", "a246", "a247", "a248", "a249",
        "a250", "a251", "a252", "a253", "a254", "a255", "s33", "s34", "s35", "s36", "s37", "s38", "s39", "s40", "s41", "s42", "s43", "s44", "s45", "s46",
        "s47", "s48", "s49", "s50", "s51", "s52", "s53", "s54", "s55", "s56", "s57", "s58", "s59", "s60", "s61", "s62", "s63", "s64", "s65", "s66", "s67"
    );
  } else {
    ad[3] = WS_OX + (h >> 3) * (2 * OX_MT) + ((h & 7) * 4 + w) * 1024 + lane * 16;
    ad[6] = ad[3] + OX_MT;
    ad[7] = 0;
    asm volatile(
      "s_lshl_b32 s54, %[w], 10\n\t"
      "s_mov_b32 s52, 88080384\n\t"
      "s_nop 4\n\t"
      "s_mov_b32 s39, m0\n\t"
      "s_cmp_eq_u32 %[w], 0\n\t"
      "s_cselect_b64 s[66:67], 1, 0\n\t"
      "s_add_u32 m0, %[st], 0\n\t"
      "s_add_u32 s43, %[cb], 0\n\t"
      "buffer_load_dwordx4 v16, %[rw], s43 offen lds\n\t"
      "buffer_load_dwordx4 v17, %[rw], s43 offen offset:1024 lds\n\t"
      "s_add_u32 m0, %[st], 2048\n\t"
      "s_add_u32 s43, %[cb], 64\n\t"
      "buffer_load_dwordx4 v16, %[rw], s43 offen lds\n\t"
      "buffer_load_dwordx4 v17, %[rw], s43 offen offset:1024 lds\n\t"
      "s_add_u32 m0, %[st], 4096\n\t"
      "s_add_u32 s43, %[cb], 262144\n\t"
      "buffer_load_dwordx4 v16, %[rw], s43 offen lds\n\t"
      "buffer_load_dwordx4 v17, %[rw], s43 offen offset:1024 lds\n\t"
      "s_add_u32 m0, %[st], 6144\n\t"
      "s_add_u32 s43, %[cb], 262208\n\t"
      "buffer_load_dwordx4 v16, %[rw], s43 offen lds\n\t"
      "buffer_load_dwordx4 v17, %[rw], s43 offen offset:1024 lds\n\t"
      "s_add_u32 m0, %[st], 8192\n\t"
      "s_add_u32 s43, %[cb], 524288\n\t"
      "buffer_load_dwordx4 v16, %[rw], s43 offen lds\n\t"
      "buffer_load_dwordx4 v17, %[rw], s43 offen offset:1024 lds\n\t"
      "s_add_u32 m0, %[st], 10240\n\t"
      "s_add_u32 s43, %[cb], 524352\n\t"
      "buffer_load_dwordx4 v16, %[rw], s43 offen lds\n\t"
      "buffer_load_dwordx4 v17, %[rw], s43 offen offset:1024 lds\n\t"
      "s_add_u32 m0, %[st], 12288\n\t"
      "s_add_u32 s43, %[cb], 786432\n\t"
      "buffer_load_dwordx4 v16, %[rw], s43 offen lds\n\t"
      "buffer_load_dwordx4 v17, %[rw], s43 offen offset:1024 lds\n\t"
      "s_add_u32 m0, %[st], 14336\n\t"
      "s_add_u32 s43, %[cb], 786496\n\t"
      "buffer_load_dwordx4 v16, %[rw], s43 offen lds\n\t"
      "buffer_load_dwordx4 v17, %[rw], s43 offen offset:1024 lds\n\t"
      "s_waitcnt vmcnt(12)\n\t"
      "ds_read_b64_tr_b16 a[0:1], v18 offset:0\n\t"
      "ds_read_b64_tr_b16 a[2:3], v18 offset:1024\n\t"
      "ds_read_b64_tr_b16 a[4:5], v19 offset:0\n\t"
      "ds_read_b64_tr_b16 a[6:7], v19 offset:1024\n\t"
      "ds_read_b64_tr_b16 a[8:9], v18 offset:2048\n\t"
      "ds_read_b64_tr_b16 a[10:11], v18 offset:3072\n\t"
      "ds_read_b64_tr_b16 a[12:13], v19 offset:2048\n\t"
      "ds_read_b64_tr_b16 a[14:15], v19 offset:3072\n\t"
      "s_waitcnt lgkmcnt(0)\n\t"
      "s_add_u32 m0, %[st], 0\n\t"
      "s_add_u32 s43, %[cb], 1048576\n\t"
      "buffer_load_dwordx4 v16, %[rw], s43 offen lds\n\t"
      "buffer_load_dwordx4 v17, %[rw], s43 offen offset:1024 lds\n\t"
      "s_add_u32 m0, %[st], 2048\n\t"
      "s_add_u32 s43, %[cb], 1048640\n\t"
      "buffer_load_dwordx4 v16, %[rw], s43 offen lds\n\t"
      "buffer_load_dwordx4 v17, %[rw], s43 offen offset:1024 lds\n\t"
      "s_waitcnt vmcnt(12)\n\t"
      "ds_read_b64_tr_b16 a[16:17], v18 offset:4096\n\t"
      "ds_read_b64_tr_b16 a[18:19], v18 offset:5120\n\t"
      "ds_read_b64_tr_b16 a[20:21], v19 offset:4096\n\t"
      "ds_read_b64_tr_b16 a[22:23], v19 offset:5120\n\t"
      "ds_read_b64_tr_b16 a[24:25], v18 offset:6144\n\t"
      "ds_read_b64_tr_b16 a[26:27], v18 offset:7168\n\t"
      "ds_read_b64_tr_b16 a[28:29], v19 offset:6144\n\t"
      "ds_read_b64_tr_b16 a[30:31], v19 offset:7168\n\t"
      "s_waitcnt lgkmcnt(0)\n\t"
      "s_add_u32 m0, %[st], 4096\n\t"
      "s_add_u32 s43, %[cb], 1310720\n\t"
      "buffer_load_dwordx4 v16, %[rw], s43 offen lds\n\t"
      "buffer_load_dwordx4 v17, %[rw], s43 offen offset:1024 lds\n\t"
      "s_add_u32 m0, %[st], 6144\n\t"
      "s_add_u32 s43, %[cb], 1310784\n\t"
      "buffer_load_dwordx4 v16, %[rw], s43 offen lds\n\t"
      "buffer_load_dwordx4 v17, %[rw], s43 offen offset:1024 lds\n\t"
      "s_waitcnt vmcnt(12)\n\t"
      "ds_read_b64_tr_b16 a[32:33], v18 offset:8192\n\t"
      "ds_read_b64_tr_b16 a[34:35], v18 offset:9216\n\t"
      "ds_read_b64_tr_b16 a[36:37], v19 offset:8192\n\t"
      "ds_read_b64_tr_b16 a[38:39], v19 offset:9216\n\t"
      "ds_read_b64_tr_b16 a[40:41], v18 offset:10240\n\t"
      "ds_read_b64_tr_b16 a[42:43], v18 offset:11264\n\t"
      "ds_read_b64_tr_b16 a[44:45], v19 offset:10240\n\t"
      "ds_read_b64_tr_b16 a[46:47], v19 offset:11264\n\t"
      "s_waitcnt lgkmcnt(0)\n\t"
      "s_add_u32 m0, %[st], 8192\n\t"
      "s_add_u32 s43, %[cb], 1572864\n\t"
      "buffer_load_dwordx4 v16, %[rw], s43 offen lds\n\t"
      "buffer_load_dwordx4 v17, %[rw], s43 offen offset:1024 lds\n\t"
      "s_add_u32 m0, %[st], 10240\n\t"
      "s_add_u32 s43, %[cb], 1572928\n\t"
      "buffer_load_dwordx4 v16, %[rw], s43 offen lds\n\t"
      "buffer_load_dwordx4 v17, %[rw], s43 offen offset:1024 lds\n\t"
      "s_waitcnt vmcnt(12)\n\t"
      "ds_read_b64_tr_b16 a[48:49], v18 offset:12288\n\t"
      "ds_read_b64_tr_b16 a[50:51], v18 offset:13312\n\t"
      "ds_read_b64_tr_b16 a[52:53], v19 offset:12288\n\t"
      "ds_read_b64_tr_b16 a[54:55], v19 offset:13312\n\t"
      "ds_read_b64_tr_b16 a[56:57], v18 offset:14336\n\t"
      "ds_read_b64_tr_b16 a[58:59], v18 offset:15360\n\t"
      "ds_read_b64_tr_b16 a[60:61], v19 offset:14336\n\t"
      "ds_read_b64_tr_b16 a[62:63], v19 offset:15360\n\t"
      "s_waitcnt lgkmcnt(0)\n\t"
      "s_add_u32 m0, %[st], 12288\n\t"
      "s_add_u32 s43, %[cb], 1835008\n\t"
      "buffer_load_dwordx4 v16, %[rw], s43 offen lds\n\t"
      "buffer_load_dwordx4 v17, %[rw], s43 offen offset:1024 lds\n\t"
      "s_add_u32 m0, %[st], 14336\n\t"
      "s_add_u32 s43, %[cb], 1835072\n\t"
      "buffer_load_dwordx4 v16, %[rw], s43 offen lds\n\t"
      "buffer_load_dwordx4 v17, %[rw], s43 offen offset:1024 lds\n\t"
      "s_waitcnt vmcnt(12)\n\t"
      "ds_read_b64_tr_b16 a[64:65], v18 offset:0\n\t"
      "ds_read_b64_tr_b16 a[66:67], v18 offset:1024\n\t"
      "ds_read_b64_tr_b16 a[68:69], v19 offset:0\n\t"
      "ds_read_b64_tr_b16 a[70:71], v19 offset:1024\n\t"
      "ds_read_b64_tr_b16 a[72:73], v18 offset:2048\n\t"
      "ds_read_b64_tr_b16 a[74:75], v18 offset:3072\n\t"
      "ds_read_b64_tr_b16 a[76:77], v19 offset:2048\n\t"
      "ds_read_b64_tr_b16 a[78:79], v19 offset:3072\n\t"
      "s_waitcnt lgkmcnt(0)\n\t"
      "s_add_u32 m0, %[st], 0\n\t"
      "s_add_u32 s43, %[cb], 2097152\n\t"
      "buffer_load_dwordx4 v16, %[rw], s43 offen lds\n\t"
      "buffer_load_dwordx4 v17, %[rw], s43 offen offset:1024 lds\n\t"
      "s_add_u32 m0, %[st], 2048\n\t"
      "s_add_u32 s43, %[cb], 2097216\n\t"
      "buffer_load_dwordx4 v16, %[rw], s43 offen lds\n\t"
      "buffer_load_dwordx4 v17, %[rw], s43 offen offset:1024 lds\n\t"
      "s_waitcnt vmcnt(12)\n\t"
      "ds_read_b64_tr_b16 a[80:81], v18 offset:4096\n\t"
      "ds_read_b64_tr_b16 a[82:83], v18 offset:5120\n\t"
      "ds_read_b64_tr_b16 a[84:85], v19 offset:4096\n\t"
      "ds_read_b64_tr_b16 a[86:87], v19 offset:5120\n\t"
      "ds_read_b64_tr_b16 a[88:89], v18 offset:6144\n\t"
      "ds_read_b64_tr_b16 a[90:91], v18 offset:7168\n\t"
      "ds_read_b64_tr_b16 a[92:93], v19 offset:6144\n\t"
      "ds_read_b64_tr_b16 a[94:95], v19 offset:7168\n\t"
      "s_waitcnt lgkmcnt(0)\n\t"
      "s_add_u32 m0, %[st], 4096\n\t"
      "s_add_u32 s43, %[cb], 2359296\n\t"
      "buffer_load_dwordx4 v16, %[rw], s43 offen lds\n\t"
      "buffer_load_dwordx4 v17, %[rw], s43 offen offset:1024 lds\n\t"
      "s_add_u32 m0, %[st], 6144\n\t"
      "s_add_u32 s43, %[cb], 2359360\n\t"
      "buffer_load_dwordx4 v16, %[rw], s43 offen lds\n\t"
      "buffer_load_dwordx4 v17, %[rw], s43 offen offset:1024 lds\n\t"
      "s_waitcnt vmcnt(12)\n\t"
      "ds_read_b64_tr_b16 a[96:97], v18 offset:8192\n\t"
      "ds_read_b64_tr_b16 a[98:99], v18 offset:9216\n\t"
      "ds_read_b64_tr_b16 a[100:101], v19 offset:8192\n\t"
      "ds_read_b64_tr_b16 a[102:103], v19 offset:9216\n\t"
      "ds_read_b64_tr_b16 a[104:105], v18 offset:10240\n\t"
      "ds_read_b64_tr_b16 a[106:107], v18 offset:11264\n\t"
      "ds_read_b64_tr_b16 a[108:109], v19 offset:10240\n\t"
      "ds_read_b64_tr_b16 a[110:111], v19 offset:11264\n\t"
      "s_waitcnt lgkmcnt(0)\n\t"
      "s_add_u32 m0, %[st], 8192\n\t"
      "s_add_u32 s43, %[cb], 2621440\n\t"
      "buffer_load_dwordx4 v16, %[rw], s43 offen lds\n\t"
      "buffer_load_dwordx4 v17, %[rw], s43 offen offset:1024 lds\n\t"
      "s_add_u32 m0, %[st], 10240\n\t"
      "s_add_u32 s43, %[cb], 2621504\n\t"
      "buffer_load_dwordx4 v16, %[rw], s43 offen lds\n\t"
      "buffer_load_dwordx4 v17, %[rw], s43 offen offset:1024 lds\n\t"
      "s_waitcnt vmcnt(12)\n\t"
      "ds_read_b64_tr_b16 a[112:113], v18 offset:12288\n\t"
      "ds_read_b64_tr_b16 a[114:115], v18 offset:13312\n\t"
      "ds_read_b64_tr_b16 a[116:117], v19 offset:12288\n\t"
      "ds_read_b64_tr_b16 a[118:119], v19 offset:13312\n\t"
      "ds_read_b64_tr_b16 a[120:121], v18 offset:14336\n\t"
      "ds_read_b64_tr_b16 a[122:123], v18 offset:15360\n\t"
      "ds_read_b64_tr_b16 a[124:125], v19 offset:14336\n\t"
      "ds_read_b64_tr_b16 a[126:127], v19 offset:15360\n\t"
      "s_waitcnt lgkmcnt(0)\n\t"
      "s_add_u32 m0, %[st], 12288\n\t"
      "s_add_u32 s43, %[cb], 2883584\n\t"
      "buffer_load_dwordx4 v16, %[rw], s43 offen lds\n\t"
      "buffer_load_dwordx4 v17, %[rw], s43 offen offset:1024 lds\n\t"
      "s_add_u32 m0, %[st], 14336\n\t"
      "s_add_u32 s43, %[cb], 2883648\n\t"
      "buffer_load_dwordx4 v16, %[rw], s43 offen lds\n\t"
      "buffer_load_dwordx4 v17, %[rw], s43 offen offset:1024 lds\n\t"
      "s_waitcnt vmcnt(12)\n\t"
      "ds_read_b64_tr_b16 a[128:129], v18 offset:0\n\t"
      "ds_read_b64_tr_b16 a[130:131], v18 offset:1024\n\t"
      "ds_read_b64_tr_b16 a[132:133], v19 offset:0\n\t"
      "ds_read_b64_tr_b16 a[134:135], v19 offset:1024\n\t"
      "ds_read_b64_tr_b16 a[136:137], v18 offset:2048\n\t"
      "ds_read_b64_tr_b16 a[138:139], v18 offset:3072\n\t"
      "ds_read_b64_tr_b16 a[140:141], v19 offset:2048\n\t"
      "ds_read_b64_tr_b16 a[142:143], v19 offset:3072\n\t"
      "s_waitcnt lgkmcnt(0)\n\t"
      "s_add_u32 m0, %[st], 0\n\t"
      "s_add_u32 s43, %[cb], 3145728\n\t"
      "buffer_load_dwordx4 v16, %[rw], s43 offen lds\n\t"
      "buffer_load_dwordx4 v17, %[rw], s43 offen offset:1024 lds\n\t"
      "s_add_u32 m0, %[st], 2048\n\t"
      "s_add_u32 s43, %[cb], 3145792\n\t"
      "buffer_load_dwordx4 v16, %[rw], s43 offen lds\n\t"
      "buffer_load_dwordx4 v17, %[rw], s43 offen offset:1024 lds\n\t"
      "s_waitcnt vmcnt(12)\n\t"
      "ds_read_b64_tr_b16 a[144:145], v18 offset:4096\n\t"
      "ds_read_b64_tr_b16 a[146:147], v18 offset:5120\n\t"
      "ds_read_b64_tr_b16 a[148:149], v19 offset:4096\n\t"
      "ds_read_b64_tr_b16 a[150:151], v19 offset:5120\n\t"
      "ds_read_b64_tr_b16 a[152:153], v18 offset:6144\n\t"
      "ds_read_b64_tr_b16 a[154:155], v18 offset:7168\n\t"
      "ds_read_b64_tr_b16 a[156:157], v19 offset:6144\n\t"
      "ds_read_b64_tr_b16 a[158:159], v19 offset:7168\n\t"
      "s_waitcnt lgkmcnt(0)\n\t"
      "s_add_u32 m0, %[st], 4096\n\t"
      "s_add_u32 s43, %[cb], 3407872\n\t"
      "buffer_load_dwordx4 v16, %[rw], s43 offen lds\n\t"
      "buffer_load_dwordx4 v17, %[rw], s43 offen offset:1024 lds\n\t"
      "s_add_u32 m0, %[st], 6144\n\t"
      "s_add_u32 s43, %[cb], 3407936\n\t"
      "buffer_load_dwordx4 v16, %[rw], s43 offen lds\n\t"
      "buffer_load_dwordx4 v17, %[rw], s43 offen offset:1024 lds\n\t"
      "s_waitcnt vmcnt(12)\n\t"
      "ds_read_b64_tr_b16 a[160:161], v18 offset:8192\n\t"
      "ds_read_b64_tr_b16 a[162:163], v18 offset:9216\n\t"
      "ds_read_b64_tr_b16 a[164:165], v19 offset:8192\n\t"
      "ds_read_b64_tr_b16 a[166:167], v19 offset:9216\n\t"
      "ds_read_b64_tr_b16 a[168:169], v18 offset:10240\n\t"
      "ds_read_b64_tr_b16 a[170:171], v18 offset:11264\n\t"
      "ds_read_b64_tr_b16 a[172:173], v19 offset:10240\n\t"
      "ds_read_b64_tr_b16 a[174:175], v19 offset:11264\n\t"
      "s_waitcnt lgkmcnt(0)\n\t"
      "s_add_u32 m0, %[st], 8192\n\t"
      "s_add_u32 s43, %[cb], 3670016\n\t"
      "buffer_load_dwordx4 v16, %[rw], s43 offen lds\n\t"
      "buffer_load_dwordx4 v17, %[rw], s43 offen offset:1024 lds\n\t"
      "s_add_u32 m0, %[st], 10240\n\t"
      "s_add_u32 s43, %[cb], 3670080\n\t"
      "buffer_load_dwordx4 v16, %[rw], s43 offen lds\n\t"
      "buffer_load_dwordx4 v17, %[rw], s43 offen offset:1024 lds\n\t"
      "buffer_load_dword v26, v21, %[rs], 0 offen sc1\n\t"
      "s_waitcnt vmcnt(13)\n\t"
      "ds_read_b64_tr_b16 a[176:177], v18 offset:12288\n\t"
      "ds_read_b64_tr_b16 a[178:179], v18 offset:13312\n\t"
      "ds_read_b64_tr_b16 a[180:181], v19 offset:12288\n\t"
      "ds_read_b64_tr_b16 a[182:183], v19 offset:13312\n\t"
      "ds_read_b64_tr_b16 a[184:185], v18 offset:14336\n\t"
      "ds_read_b64_tr_b16 a[186:187], v18 offset:15360\n\t"
      "ds_read_b64_tr_b16 a[188:189], v19 offset:14336\n\t"
      "ds_read_b64_tr_b16 a[190:191], v19 offset:15360\n\t"
      "s_waitcnt lgkmcnt(0)\n\t"
      "s_add_u32 m0, %[st], 12288\n\t"
      "s_add_u32 s43, %[cb], 3932160\n\t"
      "buffer_load_dwordx4 v16, %[rw], s43 offen lds\n\t"
      "buffer_load_dwordx4 v17, %[rw], s43 offen offset:1024 lds\n\t"
      "s_add_u32 m0, %[st], 14336\n\t"
      "s_add_u32 s43, %[cb], 3932224\n\t"
      "buffer_load_dwordx4 v16, %[rw], s43 offen lds\n\t"
      "buffer_load_dwordx4 v17, %[rw], s43 offen offset:1024 lds\n\t"
      "s_waitcnt vmcnt(13)\n\t"
      "ds_read_b64_tr_b16 a[192:193], v18 offset:0\n\t"
      "ds_read_b64_tr_b16 a[194:195], v18 offset:1024\n\t"
      "ds_read_b64_tr_b16 a[196:197], v19 offset:0\n\t"
      "ds_read_b64_tr_b16 a[198:199], v19 offset:1024\n\t"
      "ds_read_b64_tr_b16 a[200:201], v18 offset:2048\n\t"
      "ds_read_b64_tr_b16 a[202:203], v18 offset:3072\n\t"
      "ds_read_b64_tr_b16 a[204:205], v19 offset:2048\n\t"
      "ds_read_b64_tr_b16 a[206:207], v19 offset:3072\n\t"
      "s_waitcnt lgkmcnt(0)\n\t"
      "s_add_u32 m0, %[st], 0\n\t"
      "s_add_u32 s43, %[cb], 4194304\n\t"
      "buffer_load_dwordx4 v16, %[rw], s43 offen lds\n\t"
      "buffer_load_dwordx4 v17, %[rw], s43 offen offset:1024 lds\n\t"
      "s_add_u32 m0, %[st], 2048\n\t"
      "s_add_u32 s43, %[cb], 4194368\n\t"
      "buffer_load_dwordx4 v16, %[rw], s43 offen lds\n\t"
      "buffer_load_dwordx4 v17, %[rw], s43 offen offset:1024 lds\n\t"
      "s_waitcnt vmcnt(13)\n\t"
      "ds_read_b64_tr_b16 a[208:209], v18 offset:4096\n\t"
      "ds_read_b64_tr_b16 a[210:211], v18 offset:5120\n\t"
      "ds_read_b64_tr_b16 a[212:213], v19 offset:4096\n\t"
      "ds_read_b64_tr_b16 a[214:215], v19 offset:5120\n\t"
      "ds_read_b64_tr_b16 a[216:217], v18 offset:6144\n\t"
      "ds_read_b64_tr_b16 a[218:219], v18 offset:7168\n\t"
      "ds_read_b64_tr_b16 a[220:221], v19 offset:6144\n\t"
      "ds_read_b64_tr_b16 a[222:223], v19 offset:7168\n\t"
      "s_waitcnt lgkmcnt(0)\n\t"
      "s_add_u32 m0, %[st], 4096\n\t"
      "s_add_u32 s43, %[cb], 4456448\n\t"
      "buffer_load_dwordx4 v16, %[rw], s43 offen lds\n\t"
      "buffer_load_dwordx4 v17, %[rw], s43 offen offset:1024 lds\n\t"
      "s_add_u32 m0, %[st], 6144\n\t"
      "s_add_u32 s43, %[cb], 4456512\n\t"
      "buffer_load_dwordx4 v16, %[rw], s43 offen lds\n\t"
      "buffer_load_dwordx4 v17, %[rw], s43 offen offset:1024 lds\n\t"
      "s_waitcnt vmcnt(13)\n\t"
      "ds_read_b64_tr_b16 a[224:225], v18 offset:8192\n\t"
      "ds_read_b64_tr_b16 a[226:227], v18 offset:9216\n\t"
      "ds_read_b64_tr_b16 a[228:229], v19 offset:8192\n\t"
      "ds_read_b64_tr_b16 a[230:231], v19 offset:9216\n\t"
      "ds_read_b64_tr_b16 a[232:233], v18 offset:10240\n\t"
      "ds_read_b64_tr_b16 a[234:235], v18 offset:11264\n\t"
      "ds_read_b64_tr_b16 a[236:237], v19 offset:10240\n\t"
      "ds_read_b64_tr_b16 a[238:239], v19 offset:11264\n\t"
      "s_waitcnt lgkmcnt(0)\n\t"
      "s_add_u32 m0, %[st], 8192\n\t"
      "s_add_u32 s43, %[cb], 4718592\n\t"
      "buffer_load_dwordx4 v16, %[rw], s43 offen lds\n\t"
      "buffer_load_dwordx4 v17, %[rw], s43 offen offset:1024 lds\n\t"
      "s_add_u32 m0, %[st], 10240\n\t"
      "s_add_u32 s43, %[cb], 4718656\n\t"
      "buffer_load_dwordx4 v16, %[rw], s43 offen lds\n\t"
      "buffer_load_dwordx4 v17, %[rw], s43 offen offset:1024 lds\n\t"
      "s_waitcnt vmcnt(12)\n\t"
      "ds_read_b64_tr_b16 a[240:241], v18 offset:12288\n\t"
      "ds_read_b64_tr_b16 a[242:243], v18 offset:13312\n\t"
      "ds_read_b64_tr_b16 a[244:245], v19 offset:12288\n\t"
      "ds_read_b64_tr_b16 a[246:247], v19 offset:13312\n\t"
      "ds_read_b64_tr_b16 a[248:249], v18 offset:14336\n\t"
      "ds_read_b64_tr_b16 a[250:251], v18 offset:15360\n\t"
      "ds_read_b64_tr_b16 a[252:253], v19 offset:14336\n\t"
      "ds_read_b64_tr_b16 a[254:255], v19 offset:15360\n\t"
      "s_waitcnt lgkmcnt(0)\n\t"
      "s_add_u32 m0, %[st], 12288\n\t"
      "s_add_u32 s43, %[cb], 4980736\n\t"
      "buffer_load_dwordx4 v16, %[rw], s43 offen lds\n\t"
      "buffer_load_dwordx4 v17, %[rw], s43 offen offset:1024 lds\n\t"
      "s_add_u32 m0, %[st], 14336\n\t"
      "s_add_u32 s43, %[cb], 4980800\n\t"
      "buffer_load_dwordx4 v16, %[rw], s43 offen lds\n\t"
      "buffer_load_dwordx4 v17, %[rw], s43 offen offset:1024 lds\n\t"
      "v_cmp_ne_u32 vcc, %[t0], v26\n\t"
      "s_add_u32 m0, s54, 0\n\t"
      "s_add_u32 s43, s52, 0\n\t"
      "buffer_load_dwordx4 v0, %[rs], s43 offen sc1 lds\n\t"
      "s_add_u32 m0, s54, 4096\n\t"
      "s_add_u32 s43, s52, 4096\n\t"
      "buffer_load_dwordx4 v0, %[rs], s43 offen sc1 lds\n\t"
      "s_add_u32 m0, s54, 8192\n\t"
      "s_add_u32 s43, s52, 8192\n\t"
      "buffer_load_dwordx4 v0, %[rs], s43 offen sc1 lds\n\t"
      "s_add_u32 m0, s54, 12288\n\t"
      "s_add_u32 s43, s52, 12288\n\t"
      "buffer_load_dwordx4 v0, %[rs], s43 offen sc1 lds\n\t"
      "s_add_u32 m0, s54, 16384\n\t"
      "s_add_u32 s43, s52, 16384\n\t"
      "buffer_load_dwordx4 v0, %[rs], s43 offen sc1 lds\n\t"
      "s_add_u32 m0, s54, 20480\n\t"
      "s_add_u32 s43, s52, 20480\n\t"
      "buffer_load_dwordx4 v0, %[rs], s43 offen sc1 lds\n\t"
      "s_add_u32 m0, s54, 24576\n\t"
      "s_add_u32 s43, s52, 24576\n\t"
      "buffer_load_dwordx4 v0, %[rs], s43 offen sc1 lds\n\t"
      "s_add_u32 m0, s54, 28672\n\t"
      "s_add_u32 s43, s52, 28672\n\t"
      "buffer_load_dwordx4 v0, %[rs], s43 offen sc1 lds\n\t"
      "s_add_u32 m0, s54, 32768\n\t"
      "s_add_u32 s43, s52, 32768\n\t"
      "buffer_load_dwordx4 v0, %[rs], s43 offen sc1 lds\n\t"
      "s_add_u32 m0, s54, 36864\n\t"
      "s_add_u32 s43, s52, 36864\n\t"
      "buffer_load_dwordx4 v0, %[rs], s43 offen sc1 lds\n\t"
      "s_waitcnt vmcnt(22)\n\t"
      "ds_read_b64_tr_b16 v[128:129], v18 offset:0\n\t"
      "ds_read_b64_tr_b16 v[130:131], v18 offset:1024\n\t"
      "ds_read_b64_tr_b16 v[132:133], v19 offset:0\n\t"
      "ds_read_b64_tr_b16 v[134:135], v19 offset:1024\n\t"
      "ds_read_b64_tr_b16 v[136:137], v18 offset:2048\n\t"
      "ds_read_b64_tr_b16 v[138:139], v18 offset:3072\n\t"
      "ds_read_b64_tr_b16 v[140:141], v19 offset:2048\n\t"
      "ds_read_b64_tr_b16 v[142:143], v19 offset:3072\n\t"
      "s_waitcnt lgkmcnt(0)\n\t"
      "s_waitcnt vmcnt(18)\n\t"
      "ds_read_b64_tr_b16 v[144:145], v18 offset:4096\n\t"
      "ds_read_b64_tr_b16 v[146:147], v18 offset:5120\n\t"
      "ds_read_b64_tr_b16 v[148:149], v19 offset:4096\n\t"
      "ds_read_b64_tr_b16 v[150:151], v19 offset:5120\n\t"
      "ds_read_b64_tr_b16 v[152:153], v18 offset:6144\n\t"
      "ds_read_b64_tr_b16 v[154:155], v18 offset:7168\n\t"
      "ds_read_b64_tr_b16 v[156:157], v19 offset:6144\n\t"
      "ds_read_b64_tr_b16 v[158:159], v19 offset:7168\n\t"
      "s_waitcnt lgkmcnt(0)\n\t"
      "s_waitcnt vmcnt(14)\n\t"
      "ds_read_b64_tr_b16 v[160:161], v18 offset:8192\n\t"
      "ds_read_b64_tr_b16 v[162:163], v18 offset:9216\n\t"
      "ds_read_b64_tr_b16 v[164:165], v19 offset:8192\n\t"
      "ds_read_b64_tr_b16 v[166:167], v19 offset:9216\n\t"
      "ds_read_b64_tr_b16 v[168:169], v18 offset:10240\n\t"
      "ds_read_b64_tr_b16 v[170:171], v18 offset:11264\n\t"
      "ds_read_b64_tr_b16 v[172:173], v19 offset:10240\n\t"
      "ds_read_b64_tr_b16 v[174:175], v19 offset:11264\n\t"
      "s_waitcnt lgkmcnt(0)\n\t"
      "s_waitcnt vmcnt(10)\n\t"
      "ds_read_b64_tr_b16 v[176:177], v18 offset:12288\n\t"
      "ds_read_b64_tr_b16 v[178:179], v18 offset:13312\n\t"
      "ds_read_b64_tr_b16 v[180:181], v19 offset:12288\n\t"
      "ds_read_b64_tr_b16 v[182:183], v19 offset:13312\n\t"
      "ds_read_b64_tr_b16 v[184:185], v18 offset:14336\n\t"
      "ds_read_b64_tr_b16 v[186:187], v18 offset:15360\n\t"
      "ds_read_b64_tr_b16 v[188:189], v19 offset:14336\n\t"
      "ds_read_b64_tr_b16 v[190:191], v19 offset:15360\n\t"
      "s_waitcnt lgkmcnt(0)\n\t"
      "s_waitcnt lgkmcnt(0)\n\t"
      "s_barrier\n\t"
      "s_cbranch_vccz LOK%=\n\t"
      "s_mov_b32 s42, 0\n\t"
      "LLN%=:\n\t"
      "buffer_load_dword v24, v21, %[rs], 0 offen sc1\n\t"
      "s_add_u32 s42, s42, 1\n\t"
      "s_cmp_gt_u32 s42, 400000\n\t"
      "s_cbranch_scc1 LFAIL%=\n\t"
      "s_waitcnt vmcnt(0)\n\t"
      "v_cmp_ne_u32 vcc, %[t0], v24\n\t"
      "s_cbranch_vccnz LLN%=\n\t"
      "s_add_u32 m0, s54, 0\n\t"
      "s_add_u32 s43, s52, 0\n\t"
      "buffer_load_dwordx4 v0, %[rs], s43 offen sc1 lds\n\t"
      "s_add_u32 m0, s54, 4096\n\t"
      "s_add_u32 s43, s52, 4096\n\t"
      "buffer_load_dwordx4 v0, %[rs], s43 offen sc1 lds\n\t"
      "s_add_u32 m0, s54, 8192\n\t"
      "s_add_u32 s43, s52, 8192\n\t"
      "buffer_load_dwordx4 v0, %[rs], s43 offen sc1 lds\n\t"
      "s_add_u32 m0, s54, 12288\n\t"
      "s_add_u32 s43, s52, 12288\n\t"
      "buffer_load_dwordx4 v0, %[rs], s43 offen sc1 lds\n\t"
      "s_add_u32 m0, s54, 16384\n\t"
      "s_add_u32 s43, s52, 16384\n\t"
      "buffer_load_dwordx4 v0, %[rs], s43 offen sc1 lds\n\t"
      "s_add_u32 m0, s54, 20480\n\t"
      "s_add_u32 s43, s52, 20480\n\t"
      "buffer_load_dwordx4 v0, %[rs], s43 offen sc1 lds\n\t"
      "s_add_u32 m0, s54, 24576\n\t"
      "s_add_u32 s43, s52, 24576\n\t"
      "buffer_load_dwordx4 v0, %[rs], s43 offen sc1 lds\n\t"
      "s_add_u32 m0, s54, 28672\n\t"
      "s_add_u32 s43, s52, 28672\n\t"
      "buffer_load_dwordx4 v0, %[rs], s43 offen sc1 lds\n\t"
      "s_add_u32 m0, s54, 32768\n\t"
      "s_add_u32 s43, s52, 32768\n\t"
      "buffer_load_dwordx4 v0, %[rs], s43 offen sc1 lds\n\t"
      "s_add_u32 m0, s54, 36864\n\t"
      "s_add_u32 s43, s52, 36864\n\t"
      "buffer_load_dwordx4 v0, %[rs], s43 offen sc1 lds\n\t"
      "LOK%=:\n\t"
      "s_mov_b32 s40, 0\n\t"
      "s_mov_b32 s53, 0\n\t"
      "v_mov_b64 v[8:9], -1\n\t"
      "v_mov_b64 v[10:11], -1\n\t"
      "s_waitcnt vmcnt(0)\n\t"
      "s_barrier\n\t"
      "ds_read_b128 v[56:59], v1 offset:0\n\t"
      "ds_read_b128 v[60:63], v1 offset:20480\n\t"
      "ds_read_b128 v[64:67], v1 offset:1024\n\t"
      "ds_read_b128 v[68:71], v1 offset:21504\n\t"
      "s_waitcnt lgkmcnt(3)\n\t"
      "v_mfma_f32_16x16x32_bf16 v[24:27], a[0:3], v[56:59], 0\n\t"
      "ds_read_b128 v[72:75], v1 offset:2048\n\t"
      "v_mfma_f32_16x16x32_bf16 v[32:35], a[4:7], v[56:59], 0\n\t"
      "ds_read_b128 v[76:79], v1 offset:22528\n\t"
      "v_mfma_f32_16x16x32_bf16 v[40:43], a[8:11], v[56:59], 0\n\t"
      "s_xor_b32 s54, s54, 65536\n\t"
      "s_add_u32 s43, s40, 1\n\t"
      "v_mfma_f32_16x16x32_bf16 v[48:51], a[12:15], v[56:59], 0\n\t"
      "s_min_u32 s43, s43, 63\n\t"
      "s_mul_i32 s43, s43, 40960\n\t"
      "s_waitcnt lgkmcnt(4)\n\t"
      "v_mfma_f32_16x16x32_bf16 v[28:31], a[0:3], v[60:63], 0\n\t"
      "s_add_u32 s52, s43, 88080384\n\t"
      "s_mul_i32 s57, s40, %[ms]\n\t"
      "v_mfma_f32_16x16x32_bf16 v[36:39], a[4:7], v[60:63], 0\n\t"
      "s_mul_i32 s58, s40, 110592\n\t"
      "s_sub_u32 s56, s40, 1\n\t"
      "v_mfma_f32_16x16x32_bf16 v[44:47], a[8:11], v[60:63], 0\n\t"
      "s_mul_i32 s53, s56, 24576\n\t"
      "s_add_u32 s55, s53, 4096\n\t"
      "s_waitcnt lgkmcnt(2)\n\t"
      "v_mfma_f32_16x16x32_bf16 v[52:55], a[12:15], v[60:63], 0\n\t"
      "v_mov_b32 v5, s56\n\t"
      "v_xor_b32 v13, 65536, v1\n\t"
      "v_mfma_f32_16x16x32_bf16 v[24:27], a[16:19], v[64:67], v[24:27]\n\t"
      "ds_read_b128 v[80:83], v1 offset:3072\n\t"
      "v_mfma_f32_16x16x32_bf16 v[32:35], a[20:23], v[64:67], v[32:35]\n\t"
      "ds_read_b128 v[84:87], v1 offset:23552\n\t"
      "v_mfma_f32_16x16x32_bf16 v[40:43], a[24:27], v[64:67], v[40:43]\n\t"
      "v_mfma_f32_16x16x32_bf16 v[48:51], a[28:31], v[64:67], v[48:51]\n\t"
      "v_mfma_f32_16x16x32_bf16 v[28:31], a[16:19], v[68:71], v[28:31]\n\t"
      "v_mfma_f32_16x16x32_bf16 v[36:39], a[20:23], v[68:71], v[36:39]\n\t"
      "s_add_u32 m0, s54, 0\n\t"
      "s_add_u32 s43, s52, 0\n\t"
      "buffer_load_dwordx4 v0, %[rs], s43 offen sc1 lds\n\t"
      "v_mfma_f32_16x16x32_bf16 v[44:47], a[24:27], v[68:71], v[44:47]\n\t"
      "s_waitcnt lgkmcnt(2)\n\t"
      "v_mfma_f32_16x16x32_bf16 v[52:55], a[28:31], v[68:71], v[52:55]\n\t"
      "v_mfma_f32_16x16x32_bf16 v[24:27], a[32:35], v[72:75], v[24:27]\n\t"
      "ds_read_b128 v[56:59], v1 offset:4096\n\t"
      "v_mfma_f32_16x16x32_bf16 v[32:35], a[36:39], v[72:75], v[32:35]\n\t"
      "ds_read_b128 v[60:63], v1 offset:24576\n\t"
      "v_mfma_f32_16x16x32_bf16 v[40:43], a[40:43], v[72:75], v[40:43]\n\t"
      "v_mfma_f32_16x16x32_bf16 v[48:51], a[44:47], v[72:75], v[48:51]\n\t"
      "v_mfma_f32_16x16x32_bf16 v[28:31], a[32:35], v[76:79], v[28:31]\n\t"
      "v_mfma_f32_16x16x32_bf16 v[36:39], a[36:39], v[76:79], v[36:39]\n\t"
      "s_add_u32 m0, s54, 4096\n\t"
      "s_add_u32 s43, s52, 4096\n\t"
      "buffer_load_dwordx4 v0, %[rs], s43 offen sc1 lds\n\t"
      "v_mfma_f32_16x16x32_bf16 v[44:47], a[40:43], v[76:79], v[44:47]\n\t"
      "s_waitcnt lgkmcnt(2)\n\t"
      "v_mfma_f32_16x16x32_bf16 v[52:55], a[44:47], v[76:79], v[52:55]\n\t"
      "v_mfma_f32_16x16x32_bf16 v[24:27], a[48:51], v[80:83], v[24:27]\n\t"
      "ds_read_b128 v[64:67], v1 offset:5120\n\t"
      "v_mfma_f32_16x16x32_bf16 v[32:35], a[52:55], v[80:83], v[32:35]\n\t"
      "ds_read_b128 v[68:71], v1 offset:25600\n\t"
      "v_mfma_f32_16x16x32_bf16 v[40:43], a[56:59], v[80:83], v[40:43]\n\t"
      "v_mfma_f32_16x16x32_bf16 v[48:51], a[60:63], v[80:83], v[48:51]\n\t"
      "v_mfma_f32_16x16x32_bf16 v[28:31], a[48:51], v[84:87], v[28:31]\n\t"
      "v_mfma_f32_16x16x32_bf16 v[36:39], a[52:55], v[84:87], v[36:39]\n\t"
      "s_add_u32 m0, s54, 8192\n\t"
      "s_add_u32 s43, s52, 8192\n\t"
      "buffer_load_dwordx4 v0, %[rs], s43 offen sc1 lds\n\t"
      "v_mfma_f32_16x16x32_bf16 v[44:47], a[56:59], v[84:87], v[44:47]\n\t"
      "s_waitcnt lgkmcnt(2)\n\t"
      "v_mfma_f32_16x16x32_bf16 v[52:55], a[60:63], v[84:87], v[52:55]\n\t"
      "v_mfma_f32_16x16x32_bf16 v[24:27], a[64:67], v[56:59], v[24:27]\n\t"
      "ds_read_b128 v[72:75], v1 offset:6144\n\t"
      "v_mfma_f32_16x16x32_bf16 v[32:35], a[68:71], v[56:59], v[32:35]\n\t"
      "ds_read_b128 v[76:79], v1 offset:26624\n\t"
      "v_mfma_f32_16x16x32_bf16 v[40:43], a[72:75], v[56:59], v[40:43]\n\t"
      "v_mfma_f32_16x16x32_bf16 v[48:51], a[76:79], v[56:59], v[48:51]\n\t"
      "v_mfma_f32_16x16x32_bf16 v[28:31], a[64:67], v[60:63], v[28:31]\n\t"
      "v_mfma_f32_16x16x32_bf16 v[36:39], a[68:71], v[60:63], v[36:39]\n\t"
      "s_add_u32 m0, s54, 12288\n\t"
      "s_add_u32 s43, s52, 12288\n\t"
      "buffer_load_dwordx4 v0, %[rs], s43 offen sc1 lds\n\t"
      "v_mfma_f32_16x16x32_bf16 v[44:47], a[72:75], v[60:63], v[44:47]\n\t"
      "s_waitcnt lgkmcnt(2)\n\t"
      "v_mfma_f32_16x16x32_bf16 v[52:55], a[76:79], v[60:63], v[52:55]\n\t"
      "v_mfma_f32_16x16x32_bf16 v[24:27], a[80:83], v[64:67], v[24:27]\n\t"
      "ds_read_b128 v[80:83], v1 offset:7168\n\t"
      "v_mfma_f32_16x16x32_bf16 v[32:35], a[84:87], v[64:67], v[32:35]\n\t"
      "ds_read_b128 v[84:87], v1 offset:27648\n\t"
      "v_mfma_f32_16x16x32_bf16 v[40:43], a[88:91], v[64:67], v[40:43]\n\t"
      "v_mfma_f32_16x16x32_bf16 v[48:51], a[92:95], v[64:67], v[48:51]\n\t"
      "v_mfma_f32_16x16x32_bf16 v[28:31], a[80:83], v[68:71], v[28:31]\n\t"
      "v_mfma_f32_16x16x32_bf16 v[36:39], a[84:87], v[68:71], v[36:39]\n\t"
      "s_add_u32 m0, s54, 16384\n\t"
      "s_add_u32 s43, s52, 16384\n\t"
      "buffer_load_dwordx4 v0, %[rs], s43 offen sc1 lds\n\t"
      "v_mfma_f32_16x16x32_bf16 v[44:47], a[88:91], v[68:71], v[44:47]\n\t"
      "s_waitcnt lgkmcnt(2)\n\t"
      "v_mfma_f32_16x16x32_bf16 v[52:55], a[92:95], v[68:71], v[52:55]\n\t"
      "v_mfma_f32_16x16x32_bf16 v[24:27], a[96:99], v[72:75], v[24:27]\n\t"
      "ds_read_b128 v[56:59], v1 offset:8192\n\t"
      "v_mfma_f32_16x16x32_bf16 v[32:35], a[100:103], v[72:75], v[32:35]\n\t"
      "ds_read_b128 v[60:63], v1 offset:28672\n\t"
      "v_mfma_f32_16x16x32_bf16 v[40:43], a[104:107], v[72:75], v[40:43]\n\t"
      "v_mfma_f32_16x16x32_bf16 v[48:51], a[108:111], v[72:75], v[48:51]\n\t"
      "v_mfma_f32_16x16x32_bf16 v[28:31], a[96:99], v[76:79], v[28:31]\n\t"
      "v_mfma_f32_16x16x32_bf16 v[36:39], a[100:103], v[76:79], v[36:39]\n\t"
      "s_add_u32 m0, s54, 20480\n\t"
      "s_add_u32 s43, s52, 20480\n\t"
      "buffer_load_dwordx4 v0, %[rs], s43 offen sc1 lds\n\t"
      "v_mfma_f32_16x16x32_bf16 v[44:47], a[104:107], v[76:79], v[44:47]\n\t"
      "s_waitcnt lgkmcnt(2)\n\t"
      "v_mfma_f32_16x16x32_bf16 v[52:55], a[108:111], v[76:79], v[52:55]\n\t"
      "v_mfma_f32_16x16x32_bf16 v[24:27], a[112:115], v[80:83], v[24:27]\n\t"
      "ds_read_b128 v[64:67], v1 offset:9216\n\t"
      "v_mfma_f32_16x16x32_bf16 v[32:35], a[116:119], v[80:83], v[32:35]\n\t"
      "ds_read_b128 v[68:71], v1 offset:29696\n\t"
      "v_mfma_f32_16x16x32_bf16 v[40:43], a[120:123], v[80:83], v[40:43]\n\t"
      "v_mfma_f32_16x16x32_bf16 v[48:51], a[124:127], v[80:83], v[48:51]\n\t"
      "v_mfma_f32_16x16x32_bf16 v[28:31], a[112:115], v[84:87], v[28:31]\n\t"
      "v_mfma_f32_16x16x32_bf16 v[36:39], a[116:119], v[84:87], v[36:39]\n\t"
      "s_add_u32 m0, s54, 24576\n\t"
      "s_add_u32 s43, s52, 24576\n\t"
      "buffer_load_dwordx4 v0, %[rs], s43 offen sc1 lds\n\t"
      "v_mfma_f32_16x16x32_bf16 v[44:47], a[120:123], v[84:87], v[44:47]\n\t"
      "s_waitcnt lgkmcnt(2)\n\t"
      "v_mfma_f32_16x16x32_bf16 v[52:55], a[124:127], v[84:87], v[52:55]\n\t"
      "v_mfma_f32_16x16x32_bf16 v[24:27], a[128:131], v[56:59], v[24:27]\n\t"
      "ds_read_b128 v[72:75], v1 offset:10240\n\t"
      "v_mfma_f32_16x16x32_bf16 v[32:35], a[132:135], v[56:59], v[32:35]\n\t"
      "ds_read_b128 v[76:79], v1 offset:30720\n\t"
      "v_mfma_f32_16x16x32_bf16 v[40:43], a[136:139], v[56:59], v[40:43]\n\t"
      "v_mfma_f32_16x16x32_bf16 v[48:51], a[140:143], v[56:59], v[48:51]\n\t"
      "v_mfma_f32_16x16x32_bf16 v[28:31], a[128:131], v[60:63], v[28:31]\n\t"
      "v_mfma_f32_16x16x32_bf16 v[36:39], a[132:135], v[60:63], v[36:39]\n\t"
      "s_add_u32 m0, s54, 28672\n\t"
      "s_add_u32 s43, s52, 28672\n\t"
      "buffer_load_dwordx4 v0, %[rs], s43 offen sc1 lds\n\t"
      "v_mfma_f32_16x16x32_bf16 v[44:47], a[136:139], v[60:63], v[44:47]\n\t"
      "s_waitcnt lgkmcnt(2)\n\t"
      "v_mfma_f32_16x16x32_bf16 v[52:55], a[140:143], v[60:63], v[52:55]\n\t"
      "v_mfma_f32_16x16x32_bf16 v[24:27], a[144:147], v[64:67], v[24:27]\n\t"
      "ds_read_b128 v[80:83], v1 offset:11264\n\t"
      "v_mfma_f32_16x16x32_bf16 v[32:35], a[148:151], v[64:67], v[32:35]\n\t"
      "ds_read_b128 v[84:87], v1 offset:31744\n\t"
      "v_mfma_f32_16x16x32_bf16 v[40:43], a[152:155], v[64:67], v[40:43]\n\t"
      "v_mfma_f32_16x16x32_bf16 v[48:51], a[156:159], v[64:67], v[48:51]\n\t"
      "v_mfma_f32_16x16x32_bf16 v[28:31], a[144:147], v[68:71], v[28:31]\n\t"
      "v_mfma_f32_16x16x32_bf16 v[36:39], a[148:151], v[68:71], v[36:39]\n\t"
      "s_add_u32 m0, s54, 32768\n\t"
      "s_add_u32 s43, s52, 32768\n\t"
      "buffer_load_dwordx4 v0, %[rs], s43 offen sc1 lds\n\t"
      "v_mfma_f32_16x16x32_bf16 v[44:47], a[152:155], v[68:71], v[44:47]\n\t"
      "s_waitcnt lgkmcnt(2)\n\t"
      "v_mfma_f32_16x16x32_bf16 v[52:55], a[156:159], v[68:71], v[52:55]\n\t"
      "v_mfma_f32_16x16x32_bf16 v[24:27], a[160:163], v[72:75], v[24:27]\n\t"
      "ds_read_b128 v[56:59], v1 offset:12288\n\t"
      "v_mfma_f32_16x16x32_bf16 v[32:35], a[164:167], v[72:75], v[32:35]\n\t"
      "ds_read_b128 v[60:63], v1 offset:32768\n\t"
      "v_mfma_f32_16x16x32_bf16 v[40:43], a[168:171], v[72:75], v[40:43]\n\t"
      "v_mfma_f32_16x16x32_bf16 v[48:51], a[172:175], v[72:75], v[48:51]\n\t"
      "v_mfma_f32_16x16x32_bf16 v[28:31], a[160:163], v[76:79], v[28:31]\n\t"
      "v_mfma_f32_16x16x32_bf16 v[36:39], a[164:167], v[76:79], v[36:39]\n\t"
      "s_add_u32 m0, s54, 36864\n\t"
      "s_add_u32 s43, s52, 36864\n\t"
      "buffer_load_dwordx4 v0, %[rs], s43 offen sc1 lds\n\t"
      "v_mfma_f32_16x16x32_bf16 v[44:47], a[168:171], v[76:79], v[44:47]\n\t"
      "s_waitcnt lgkmcnt(2)\n\t"
      "v_mfma_f32_16x16x32_bf16 v[52:55], a[172:175], v[76:79], v[52:55]\n\t"
      "v_mfma_f32_16x16x32_bf16 v[24:27], a[176:179], v[80:83], v[24:27]\n\t"
      "ds_read_b128 v[64:67], v1 offset:13312\n\t"
      "v_mfma_f32_16x16x32_bf16 v[32:35], a[180:183], v[80:83], v[32:35]\n\t"
      "ds_read_b128 v[68:71], v1 offset:33792\n\t"
      "v_mfma_f32_16x16x32_bf16 v[40:43], a[184:187], v[80:83], v[40:43]\n\t"
      "v_mfma_f32_16x16x32_bf16 v[48:51], a[188:191], v[80:83], v[48:51]\n\t"
      "v_mfma_f32_16x16x32_bf16 v[28:31], a[176:179], v[84:87], v[28:31]\n\t"
      "v_mfma_f32_16x16x32_bf16 v[36:39], a[180:183], v[84:87], v[36:39]\n\t"
      "v_mfma_f32_16x16x32_bf16 v[44:47], a[184:187], v[84:87], v[44:47]\n\t"
      "s_waitcnt lgkmcnt(2)\n\t"
      "v_mfma_f32_16x16x32_bf16 v[52:55], a[188:191], v[84:87], v[52:55]\n\t"
      "v_mfma_f32_16x16x32_bf16 v[24:27], a[192:195], v[56:59], v[24:27]\n\t"
      "ds_read_b128 v[72:75], v1 offset:14336\n\t"
      "v_mfma_f32_16x16x32_bf16 v[32:35], a[196:199], v[56:59], v[32:35]\n\t"
      "ds_read_b128 v[76:79], v1 offset:34816\n\t"
      "v_mfma_f32_16x16x32_bf16 v[40:43], a[200:203], v[56:59], v[40:43]\n\t"
      "v_mfma_f32_16x16x32_bf16 v[48:51], a[204:207], v[56:59], v[48:51]\n\t"
      "v_mfma_f32_16x16x32_bf16 v[28:31], a[192:195], v[60:63], v[28:31]\n\t"
      "v_mfma_f32_16x16x32_bf16 v[36:39], a[196:199], v[60:63], v[36:39]\n\t"
      "v_mfma_f32_16x16x32_bf16 v[44:47], a[200:203], v[60:63], v[44:47]\n\t"
      "s_waitcnt lgkmcnt(2)\n\t"
      "v_mfma_f32_16x16x32_bf16 v[52:55], a[204:207], v[60:63], v[52:55]\n\t"
      "v_mfma_f32_16x16x32_bf16 v[24:27], a[208:211], v[64:67], v[24:27]\n\t"
      "ds_read_b128 v[80:83], v1 offset:15360\n\t"
      "v_mfma_f32_16x16x32_bf16 v[32:35], a[212:215], v[64:67], v[32:35]\n\t"
      "ds_read_b128 v[84:87], v1 offset:35840\n\t"
      "v_mfma_f32_16x16x32_bf16 v[40:43], a[216:219], v[64:67], v[40:43]\n\t"
      "v_mfma_f32_16x16x32_bf16 v[48:51], a[220:223], v[64:67], v[48:51]\n\t"
      "buffer_store_dwordx4 v[8:11], v3, %[rs], s57 offen sc1\n\t"
      "v_mfma_f32_16x16x32_bf16 v[28:31], a[208:211], v[68:71], v[28:31]\n\t"
      "v_mfma_f32_16x16x32_bf16 v[36:39], a[212:215], v[68:71], v[36:39]\n\t"
      "v_mfma_f32_16x16x32_bf16 v[44:47], a[216:219], v[68:71], v[44:47]\n\t"
      "s_waitcnt lgkmcnt(2)\n\t"
      "v_mfma_f32_16x16x32_bf16 v[52:55], a[220:223], v[68:71], v[52:55]\n\t"
      "v_mfma_f32_16x16x32_bf16 v[24:27], a[224:227], v[72:75], v[24:27]\n\t"
      "ds_read_b128 v[56:59], v1 offset:16384\n\t"
      "v_mfma_f32_16x16x32_bf16 v[32:35], a[228:231], v[72:75], v[32:35]\n\t"
      "ds_read_b128 v[60:63], v1 offset:36864\n\t"
      "v_mfma_f32_16x16x32_bf16 v[40:43], a[232:235], v[72:75], v[40:43]\n\t"
      "v_mfma_f32_16x16x32_bf16 v[48:51], a[236:239], v[72:75], v[48:51]\n\t"
      "buffer_store_dwordx4 v[8:11], v6, %[rs], s57 offen sc1\n\t"
      "v_mfma_f32_16x16x32_bf16 v[28:31], a[224:227], v[76:79], v[28:31]\n\t"
      "v_mfma_f32_16x16x32_bf16 v[36:39], a[228:231], v[76:79], v[36:39]\n\t"
      "v_mfma_f32_16x16x32_bf16 v[44:47], a[232:235], v[76:79], v[44:47]\n\t"
      "s_waitcnt lgkmcnt(2)\n\t"
      "v_mfma_f32_16x16x32_bf16 v[52:55], a[236:239], v[76:79], v[52:55]\n\t"
      "v_mfma_f32_16x16x32_bf16 v[24:27], a[240:243], v[80:83], v[24:27]\n\t"
      "ds_read_b128 v[64:67], v1 offset:17408\n\t"
      "v_mfma_f32_16x16x32_bf16 v[32:35], a[244:247], v[80:83], v[32:35]\n\t"
      "ds_read_b128 v[68:71], v1 offset:37888\n\t"
      "v_mfma_f32_16x16x32_bf16 v[40:43], a[248:251], v[80:83], v[40:43]\n\t"
      "v_mfma_f32_16x16x32_bf16 v[48:51], a[252:255], v[80:83], v[48:51]\n\t"
      "v_mfma_f32_16x16x32_bf16 v[28:31], a[240:243], v[84:87], v[28:31]\n\t"
      "v_mfma_f32_16x16x32_bf16 v[36:39], a[244:247], v[84:87], v[36:39]\n\t"
      "v_mfma_f32_16x16x32_bf16 v[44:47], a[248:251], v[84:87], v[44:47]\n\t"
      "s_waitcnt lgkmcnt(2)\n\t"
      "v_mfma_f32_16x16x32_bf16 v[52:55], a[252:255], v[84:87], v[52:55]\n\t"
      "v_mfma_f32_16x16x32_bf16 v[24:27], v[128:131], v[56:59], v[24:27]\n\t"
      "ds_read_b128 v[72:75], v1 offset:18432\n\t"
      "v_mfma_f32_16x16x32_bf16 v[32:35], v[132:135], v[56:59], v[32:35]\n\t"
      "ds_read_b128 v[76:79], v1 offset:38912\n\t"
      "v_mfma_f32_16x16x32_bf16 v[40:43], v[136:139], v[56:59], v[40:43]\n\t"
      "v_mfma_f32_16x16x32_bf16 v[48:51], v[140:143], v[56:59], v[48:51]\n\t"
      "v_mfma_f32_16x16x32_bf16 v[28:31], v[128:131], v[60:63], v[28:31]\n\t"
      "v_mfma_f32_16x16x32_bf16 v[36:39], v[132:135], v[60:63], v[36:39]\n\t"
      "v_mfma_f32_16x16x32_bf16 v[44:47], v[136:139], v[60:63], v[44:47]\n\t"
      "s_waitcnt lgkmcnt(2)\n\t"
      "v_mfma_f32_16x16x32_bf16 v[52:55], v[140:143], v[60:63], v[52:55]\n\t"
      "v_mfma_f32_16x16x32_bf16 v[24:27], v[144:147], v[64:67], v[24:27]\n\t"
      "ds_read_b128 v[80:83], v1 offset:19456\n\t"
      "v_mfma_f32_16x16x32_bf16 v[32:35], v[148:151], v[64:67], v[32:35]\n\t"
      "ds_read_b128 v[84:87], v1 offset:39936\n\t"
      "v_mfma_f32_16x16x32_bf16 v[40:43], v[152:155], v[64:67], v[40:43]\n\t"
      "v_mfma_f32_16x16x32_bf16 v[48:51], v[156:159], v[64:67], v[48:51]\n\t"
      "v_mfma_f32_16x16x32_bf16 v[28:31], v[144:147], v[68:71], v[28:31]\n\t"
      "v_mfma_f32_16x16x32_bf16 v[36:39], v[148:151], v[68:71], v[36:39]\n\t"
      "v_mfma_f32_16x16x32_bf16 v[44:47], v[152:155], v[68:71], v[44:47]\n\t"
      "s_waitcnt lgkmcnt(2)\n\t"
      "v_mfma_f32_16x16x32_bf16 v[52:55], v[156:159], v[68:71], v[52:55]\n\t"
      "s_waitcnt vmcnt(2) lgkmcnt(0)\n\t"
      "s_barrier\n\t"
      "v_mfma_f32_16x16x32_bf16 v[24:27], v[160:163], v[72:75], v[24:27]\n\t"
      "ds_read_b128 v[56:59], v13 offset:0\n\t"
      "v_mfma_f32_16x16x32_bf16 v[32:35], v[164:167], v[72:75], v[32:35]\n\t"
      "ds_read_b128 v[60:63], v13 offset:20480\n\t"
      "v_mfma_f32_16x16x32_bf16 v[40:43], v[168:171], v[72:75], v[40:43]\n\t"
      "v_mfma_f32_16x16x32_bf16 v[48:51], v[172:175], v[72:75], v[48:51]\n\t"
      "v_mfma_f32_16x16x32_bf16 v[28:31], v[160:163], v[76:79], v[28:31]\n\t"
      "v_mfma_f32_16x16x32_bf16 v[36:39], v[164:167], v[76:79], v[36:39]\n\t"
      "v_mfma_f32_16x16x32_bf16 v[44:47], v[168:171], v[76:79], v[44:47]\n\t"
      "v_mfma_f32_16x16x32_bf16 v[52:55], v[172:175], v[76:79], v[52:55]\n\t"
      "v_mfma_f32_16x16x32_bf16 v[24:27], v[176:179], v[80:83], v[24:27]\n\t"
      "ds_read_b128 v[64:67], v13 offset:1024\n\t"
      "v_mfma_f32_16x16x32_bf16 v[32:35], v[180:183], v[80:83], v[32:35]\n\t"
      "ds_read_b128 v[68:71], v13 offset:21504\n\t"
      "v_mfma_f32_16x16x32_bf16 v[40:43], v[184:187], v[80:83], v[40:43]\n\t"
      "v_mfma_f32_16x16x32_bf16 v[48:51], v[188:191], v[80:83], v[48:51]\n\t"
      "v_mov_b32 v1, v13\n\t"
      "v_mfma_f32_16x16x32_bf16 v[28:31], v[176:179], v[84:87], v[28:31]\n\t"
      "v_mfma_f32_16x16x32_bf16 v[36:39], v[180:183], v[84:87], v[36:39]\n\t"
      "s_add_u32 s40, s40, 1\n\t"
      "s_cmp_lt_u32 s40, 63\n\t"
      "v_mfma_f32_16x16x32_bf16 v[44:47], v[184:187], v[84:87], v[44:47]\n\t"
      "s_waitcnt lgkmcnt(2)\n\t"
      "v_mfma_f32_16x16x32_bf16 v[52:55], v[188:191], v[84:87], v[52:55]\n\t"
      "LOOP%=:\n\t"
      "v_mfma_f32_16x16x32_bf16 v[192:195], a[0:3], v[56:59], 0\n\t"
      "ds_read_b128 v[72:75], v1 offset:2048\n\t"
      "v_mfma_f32_16x16x32_bf16 v[200:203], a[4:7], v[56:59], 0\n\t"
      "ds_read_b128 v[76:79], v1 offset:22528\n\t"
      "v_mfma_f32_16x16x32_bf16 v[208:211], a[8:11], v[56:59], 0\n\t"
      "s_xor_b32 s54, s54, 65536\n\t"
      "s_add_u32 s43, s40, 1\n\t"
      "v_mfma_f32_16x16x32_bf16 v[216:219], a[12:15], v[56:59], 0\n\t"
      "s_min_u32 s43, s43, 63\n\t"
      "s_mul_i32 s43, s43, 40960\n\t"
      "v_mfma_f32_16x16x32_bf16 v[196:199], a[0:3], v[60:63], 0\n\t"
      "s_add_u32 s52, s43, 88080384\n\t"
      "s_mul_i32 s57, s40, %[ms]\n\t"
      "v_mfma_f32_16x16x32_bf16 v[204:207], a[4:7], v[60:63], 0\n\t"
      "s_mul_i32 s58, s40, 110592\n\t"
      "s_sub_u32 s56, s40, 1\n\t"
      "v_mfma_f32_16x16x32_bf16 v[212:215], a[8:11], v[60:63], 0\n\t"
      "s_mul_i32 s53, s56, 24576\n\t"
      "s_add_u32 s55, s53, 4096\n\t"
      "s_waitcnt lgkmcnt(2)\n\t"
      "v_mfma_f32_16x16x32_bf16 v[220:223], a[12:15], v[60:63], 0\n\t"
      "v_mov_b32 v5, s56\n\t"
      "v_xor_b32 v13, 65536, v1\n\t"
      "v_mfma_f32_16x16x32_bf16 v[192:195], a[16:19], v[64:67], v[192:195]\n\t"
      "ds_read_b128 v[80:83], v1 offset:3072\n\t"
      "v_cvt_pk_bf16_f32 v88, v24, v32\n\t"
      "v_cvt_pk_bf16_f32 v89, v25, v33\n\t"
      "v_mfma_f32_16x16x32_bf16 v[200:203], a[20:23], v[64:67], v[200:203]\n\t"
      "ds_read_b128 v[84:87], v1 offset:23552\n\t"
      "v_cvt_pk_bf16_f32 v90, v26, v34\n\t"
      "v_cvt_pk_bf16_f32 v91, v27, v35\n\t"
      "v_mfma_f32_16x16x32_bf16 v[208:211], a[24:27], v[64:67], v[208:211]\n\t"
      "buffer_store_dwordx4 v[88:91], v2, %[rs], s53 offen offset:0 sc1\n\t"
      "v_mfma_f32_16x16x32_bf16 v[216:219], a[28:31], v[64:67], v[216:219]\n\t"
      "v_mfma_f32_16x16x32_bf16 v[196:199], a[16:19], v[68:71], v[196:199]\n\t"
      "v_mfma_f32_16x16x32_bf16 v[204:207], a[20:23], v[68:71], v[204:207]\n\t"
      "s_add_u32 m0, s54, 0\n\t"
      "s_add_u32 s43, s52, 0\n\t"
      "buffer_load_dwordx4 v0, %[rs], s43 offen sc1 lds\n\t"
      "v_mfma_f32_16x16x32_bf16 v[212:215], a[24:27], v[68:71], v[212:215]\n\t"
      "s_waitcnt lgkmcnt(2)\n\t"
      "v_mfma_f32_16x16x32_bf16 v[220:223], a[28:31], v[68:71], v[220:223]\n\t"
      "v_mfma_f32_16x16x32_bf16 v[192:195], a[32:35], v[72:75], v[192:195]\n\t"
      "ds_read_b128 v[56:59], v1 offset:4096\n\t"
      "v_cvt_pk_bf16_f32 v92, v40, v48\n\t"
      "v_cvt_pk_bf16_f32 v93, v41, v49\n\t"
      "v_mfma_f32_16x16x32_bf16 v[200:203], a[36:39], v[72:75], v[200:203]\n\t"
      "ds_read_b128 v[60:63], v1 offset:24576\n\t"
      "v_cvt_pk_bf16_f32 v94, v42, v50\n\t"
      "v_cvt_pk_bf16_f32 v95, v43, v51\n\t"
      "v_mfma_f32_16x16x32_bf16 v[208:211], a[40:43], v[72:75], v[208:211]\n\t"
      "buffer_store_dwordx4 v[92:95], v2, %[rs], s53 offen offset:1024 sc1\n\t"
      "v_mfma_f32_16x16x32_bf16 v[216:219], a[44:47], v[72:75], v[216:219]\n\t"
      "v_mfma_f32_16x16x32_bf16 v[196:199], a[32:35], v[76:79], v[196:199]\n\t"
      "v_mfma_f32_16x16x32_bf16 v[204:207], a[36:39], v[76:79], v[204:207]\n\t"
      "s_add_u32 m0, s54, 4096\n\t"
      "s_add_u32 s43, s52, 4096\n\t"
      "buffer_load_dwordx4 v0, %[rs], s43 offen sc1 lds\n\t"
      "v_mfma_f32_16x16x32_bf16 v[212:215], a[40:43], v[76:79], v[212:215]\n\t"
      "s_waitcnt lgkmcnt(2)\n\t"
      "v_mfma_f32_16x16x32_bf16 v[220:223], a[44:47], v[76:79], v[220:223]\n\t"
      "v_mfma_f32_16x16x32_bf16 v[192:195], a[48:51], v[80:83], v[192:195]\n\t"
      "ds_read_b128 v[64:67], v1 offset:5120\n\t"
      "v_cvt_pk_bf16_f32 v96, v28, v36\n\t"
      "v_cvt_pk_bf16_f32 v97, v29, v37\n\t"
      "v_mfma_f32_16x16x32_bf16 v[200:203], a[52:55], v[80:83], v[200:203]\n\t"
      "ds_read_b128 v[68:71], v1 offset:25600\n\t"
      "v_cvt_pk_bf16_f32 v98, v30, v38\n\t"
      "v_cvt_pk_bf16_f32 v99, v31, v39\n\t"
      "v_mfma_f32_16x16x32_bf16 v[208:211], a[56:59], v[80:83], v[208:211]\n\t"
      "buffer_store_dwordx4 v[96:99], v2, %[rs], s55 offen offset:0 sc1\n\t"
      "v_mfma_f32_16x16x32_bf16 v[216:219], a[60:63], v[80:83], v[216:219]\n\t"
      "v_mfma_f32_16x16x32_bf16 v[196:199], a[48:51], v[84:87], v[196:199]\n\t"
      "v_mfma_f32_16x16x32_bf16 v[204:207], a[52:55], v[84:87], v[204:207]\n\t"
      "s_add_u32 m0, s54, 8192\n\t"
      "s_add_u32 s43, s52, 8192\n\t"
      "buffer_load_dwordx4 v0, %[rs], s43 offen sc1 lds\n\t"
      "v_mfma_f32_16x16x32_bf16 v[212:215], a[56:59], v[84:87], v[212:215]\n\t"
      "s_waitcnt lgkmcnt(2)\n\t"
      "v_mfma_f32_16x16x32_bf16 v[220:223], a[60:63], v[84:87], v[220:223]\n\t"
      "v_mfma_f32_16x16x32_bf16 v[192:195], a[64:67], v[56:59], v[192:195]\n\t"
      "ds_read_b128 v[72:75], v1 offset:6144\n\t"
      "v_cvt_pk_bf16_f32 v100, v44, v52\n\t"
      "v_cvt_pk_bf16_f32 v101, v45, v53\n\t"
      "v_mfma_f32_16x16x32_bf16 v[200:203], a[68:71], v[56:59], v[200:203]\n\t"
      "ds_read_b128 v[76:79], v1 offset:26624\n\t"
      "v_cvt_pk_bf16_f32 v102, v46, v54\n\t"
      "v_cvt_pk_bf16_f32 v103, v47, v55\n\t"
      "v_mfma_f32_16x16x32_bf16 v[208:211], a[72:75], v[56:59], v[208:211]\n\t"
      "buffer_store_dwordx4 v[100:103], v2, %[rs], s55 offen offset:1024 sc1\n\t"
      "v_mfma_f32_16x16x32_bf16 v[216:219], a[76:79], v[56:59], v[216:219]\n\t"
      "v_mfma_f32_16x16x32_bf16 v[196:199], a[64:67], v[60:63], v[196:199]\n\t"
      "v_mfma_f32_16x16x32_bf16 v[204:207], a[68:71], v[60:63], v[204:207]\n\t"
      "s_add_u32 m0, s54, 12288\n\t"
      "s_add_u32 s43, s52, 12288\n\t"
      "buffer_load_dwordx4 v0, %[rs], s43 offen sc1 lds\n\t"
      "v_mfma_f32_16x16x32_bf16 v[212:215], a[72:75], v[60:63], v[212:215]\n\t"
      "s_waitcnt lgkmcnt(2)\n\t"
      "v_mfma_f32_16x16x32_bf16 v[220:223], a[76:79], v[60:63], v[220:223]\n\t"
      "v_mfma_f32_16x16x32_bf16 v[192:195], a[80:83], v[64:67], v[192:195]\n\t"
      "ds_read_b128 v[80:83], v1 offset:7168\n\t"
      "v_mfma_f32_16x16x32_bf16 v[200:203], a[84:87], v[64:67], v[200:203]\n\t"
      "ds_read_b128 v[84:87], v1 offset:27648\n\t"
      "v_mfma_f32_16x16x32_bf16 v[208:211], a[88:91], v[64:67], v[208:211]\n\t"
      "v_mfma_f32_16x16x32_bf16 v[216:219], a[92:95], v[64:67], v[216:219]\n\t"
      "v_mfma_f32_16x16x32_bf16 v[196:199], a[80:83], v[68:71], v[196:199]\n\t"
      "v_mfma_f32_16x16x32_bf16 v[204:207], a[84:87], v[68:71], v[204:207]\n\t"
      "s_add_u32 m0, s54, 16384\n\t"
      "s_add_u32 s43, s52, 16384\n\t"
      "buffer_load_dwordx4 v0, %[rs], s43 offen sc1 lds\n\t"
      "v_mfma_f32_16x16x32_bf16 v[212:215], a[88:91], v[68:71], v[212:215]\n\t"
      "s_waitcnt lgkmcnt(2)\n\t"
      "v_mfma_f32_16x16x32_bf16 v[220:223], a[92:95], v[68:71], v[220:223]\n\t"
      "v_mfma_f32_16x16x32_bf16 v[192:195], a[96:99], v[72:75], v[192:195]\n\t"
      "ds_read_b128 v[56:59], v1 offset:8192\n\t"
      "v_mfma_f32_16x16x32_bf16 v[200:203], a[100:103], v[72:75], v[200:203]\n\t"
      "ds_read_b128 v[60:63], v1 offset:28672\n\t"
      "v_mfma_f32_16x16x32_bf16 v[208:211], a[104:107], v[72:75], v[208:211]\n\t"
      "v_mfma_f32_16x16x32_bf16 v[216:219], a[108:111], v[72:75], v[216:219]\n\t"
      "v_mfma_f32_16x16x32_bf16 v[196:199], a[96:99], v[76:79], v[196:199]\n\t"
      "v_mfma_f32_16x16x32_bf16 v[204:207], a[100:103], v[76:79], v[204:207]\n\t"
      "s_add_u32 m0, s54, 20480\n\t"
      "s_add_u32 s43, s52, 20480\n\t"
      "buffer_load_dwordx4 v0, %[rs], s43 offen sc1 lds\n\t"
      "v_mfma_f32_16x16x32_bf16 v[212:215], a[104:107], v[76:79], v[212:215]\n\t"
      "s_waitcnt lgkmcnt(2)\n\t"
      "v_mfma_f32_16x16x32_bf16 v[220:223], a[108:111], v[76:79], v[220:223]\n\t"
      "v_mfma_f32_16x16x32_bf16 v[192:195], a[112:115], v[80:83], v[192:195]\n\t"
      "ds_read_b128 v[64:67], v1 offset:9216\n\t"
      "v_mfma_f32_16x16x32_bf16 v[200:203], a[116:119], v[80:83], v[200:203]\n\t"
      "ds_read_b128 v[68:71], v1 offset:29696\n\t"
      "v_mfma_f32_16x16x32_bf16 v[208:211], a[120:123], v[80:83], v[208:211]\n\t"
      "v_mfma_f32_16x16x32_bf16 v[216:219], a[124:127], v[80:83], v[216:219]\n\t"
      "v_mfma_f32_16x16x32_bf16 v[196:199], a[112:115], v[84:87], v[196:199]\n\t"
      "v_mfma_f32_16x16x32_bf16 v[204:207], a[116:119], v[84:87], v[204:207]\n\t"
      "s_add_u32 m0, s54, 24576\n\t"
      "s_add_u32 s43, s52, 24576\n\t"
      "buffer_load_dwordx4 v0, %[rs], s43 offen sc1 lds\n\t"
      "v_mfma_f32_16x16x32_bf16 v[212:215], a[120:123], v[84:87], v[212:215]\n\t"
      "s_waitcnt lgkmcnt(2)\n\t"
      "v_mfma_f32_16x16x32_bf16 v[220:223], a[124:127], v[84:87], v[220:223]\n\t"
      "v_mfma_f32_16x16x32_bf16 v[192:195], a[128:131], v[56:59], v[192:195]\n\t"
      "ds_read_b128 v[72:75], v1 offset:10240\n\t"
      "v_mfma_f32_16x16x32_bf16 v[200:203], a[132:135], v[56:59], v[200:203]\n\t"
      "ds_read_b128 v[76:79], v1 offset:30720\n\t"
      "v_mfma_f32_16x16x32_bf16 v[208:211], a[136:139], v[56:59], v[208:211]\n\t"
      "v_mfma_f32_16x16x32_bf16 v[216:219], a[140:143], v[56:59], v[216:219]\n\t"
      "v_mfma_f32_16x16x32_bf16 v[196:199], a[128:131], v[60:63], v[196:199]\n\t"
      "v_mfma_f32_16x16x32_bf16 v[204:207], a[132:135], v[60:63], v[204:207]\n\t"
      "s_add_u32 m0, s54, 28672\n\t"
      "s_add_u32 s43, s52, 28672\n\t"
      "buffer_load_dwordx4 v0, %[rs], s43 offen sc1 lds\n\t"
      "v_mfma_f32_16x16x32_bf16 v[212:215], a[136:139], v[60:63], v[212:215]\n\t"
      "s_waitcnt lgkmcnt(2)\n\t"
      "v_mfma_f32_16x16x32_bf16 v[220:223], a[140:143], v[60:63], v[220:223]\n\t"
      "v_mfma_f32_16x16x32_bf16 v[192:195], a[144:147], v[64:67], v[192:195]\n\t"
      "ds_read_b128 v[80:83], v1 offset:11264\n\t"
      "v_mfma_f32_16x16x32_bf16 v[200:203], a[148:151], v[64:67], v[200:203]\n\t"
      "ds_read_b128 v[84:87], v1 offset:31744\n\t"
      "v_mfma_f32_16x16x32_bf16 v[208:211], a[152:155], v[64:67], v[208:211]\n\t"
      "v_mfma_f32_16x16x32_bf16 v[216:219], a[156:159], v[64:67], v[216:219]\n\t"
      "v_mfma_f32_16x16x32_bf16 v[196:199], a[144:147], v[68:71], v[196:199]\n\t"
      "v_mfma_f32_16x16x32_bf16 v[204:207], a[148:151], v[68:71], v[204:207]\n\t"
      "s_add_u32 m0, s54, 32768\n\t"
      "s_add_u32 s43, s52, 32768\n\t"
      "buffer_load_dwordx4 v0, %[rs], s43 offen sc1 lds\n\t"
      "v_mfma_f32_16x16x32_bf16 v[212:215], a[152:155], v[68:71], v[212:215]\n\t"
      "s_waitcnt lgkmcnt(2)\n\t"
      "v_mfma_f32_16x16x32_bf16 v[220:223], a[156:159], v[68:71], v[220:223]\n\t"
      "v_mfma_f32_16x16x32_bf16 v[192:195], a[160:163], v[72:75], v[192:195]\n\t"
      "ds_read_b128 v[56:59], v1 offset:12288\n\t"
      "v_mfma_f32_16x16x32_bf16 v[200:203], a[164:167], v[72:75], v[200:203]\n\t"
      "ds_read_b128 v[60:63], v1 offset:32768\n\t"
      "v_mfma_f32_16x16x32_bf16 v[208:211], a[168:171], v[72:75], v[208:211]\n\t"
      "v_mfma_f32_16x16x32_bf16 v[216:219], a[172:175], v[72:75], v[216:219]\n\t"
      "v_mfma_f32_16x16x32_bf16 v[196:199], a[160:163], v[76:79], v[196:199]\n\t"
      "v_mfma_f32_16x16x32_bf16 v[204:207], a[164:167], v[76:79], v[204:207]\n\t"
      "s_add_u32 m0, s54, 36864\n\t"
      "s_add_u32 s43, s52, 36864\n\t"
      "buffer_load_dwordx4 v0, %[rs], s43 offen sc1 lds\n\t"
      "v_mfma_f32_16x16x32_bf16 v[212:215], a[168:171], v[76:79], v[212:215]\n\t"
      "s_waitcnt lgkmcnt(2)\n\t"
      "v_mfma_f32_16x16x32_bf16 v[220:223], a[172:175], v[76:79], v[220:223]\n\t"
      "v_mfma_f32_16x16x32_bf16 v[192:195], a[176:179], v[80:83], v[192:195]\n\t"
      "ds_read_b128 v[64:67], v1 offset:13312\n\t"
      "v_mfma_f32_16x16x32_bf16 v[200:203], a[180:183], v[80:83], v[200:203]\n\t"
      "ds_read_b128 v[68:71], v1 offset:33792\n\t"
      "v_mfma_f32_16x16x32_bf16 v[208:211], a[184:187], v[80:83], v[208:211]\n\t"
      "v_mfma_f32_16x16x32_bf16 v[216:219], a[188:191], v[80:83], v[216:219]\n\t"
      "s_waitcnt vmcnt(7)\n\t"
      "buffer_store_dword v5, v4, %[rs], %[flo] offen sc1\n\t"
      "v_mfma_f32_16x16x32_bf16 v[196:199], a[176:179], v[84:87], v[196:199]\n\t"
      "v_mfma_f32_16x16x32_bf16 v[204:207], a[180:183], v[84:87], v[204:207]\n\t"
      "v_mfma_f32_16x16x32_bf16 v[212:215], a[184:187], v[84:87], v[212:215]\n\t"
      "s_waitcnt lgkmcnt(2)\n\t"
      "v_mfma_f32_16x16x32_bf16 v[220:223], a[188:191], v[84:87], v[220:223]\n\t"
      "v_mfma_f32_16x16x32_bf16 v[192:195], a[192:195], v[56:59], v[192:195]\n\t"
      "ds_read_b128 v[72:75], v1 offset:14336\n\t"
      "v_mfma_f32_16x16x32_bf16 v[200:203], a[196:199], v[56:59], v[200:203]\n\t"
      "ds_read_b128 v[76:79], v1 offset:34816\n\t"
      "v_mfma_f32_16x16x32_bf16 v[208:211], a[200:203], v[56:59], v[208:211]\n\t"
      "v_mfma_f32_16x16x32_bf16 v[216:219], a[204:207], v[56:59], v[216:219]\n\t"
      "v_mfma_f32_16x16x32_bf16 v[196:199], a[192:195], v[60:63], v[196:199]\n\t"
      "v_mfma_f32_16x16x32_bf16 v[204:207], a[196:199], v[60:63], v[204:207]\n\t"
      "v_mfma_f32_16x16x32_bf16 v[212:215], a[200:203], v[60:63], v[212:215]\n\t"
      "s_waitcnt lgkmcnt(2)\n\t"
      "v_mfma_f32_16x16x32_bf16 v[220:223], a[204:207], v[60:63], v[220:223]\n\t"
      "v_mfma_f32_16x16x32_bf16 v[192:195], a[208:211], v[64:67], v[192:195]\n\t"
      "ds_read_b128 v[80:83], v1 offset:15360\n\t"
      "v_mfma_f32_16x16x32_bf16 v[200:203], a[212:215], v[64:67], v[200:203]\n\t"
      "ds_read_b128 v[84:87], v1 offset:35840\n\t"
      "v_mfma_f32_16x16x32_bf16 v[208:211], a[216:219], v[64:67], v[208:211]\n\t"
      "v_mfma_f32_16x16x32_bf16 v[216:219], a[220:223], v[64:67], v[216:219]\n\t"
      "buffer_store_dwordx4 v[8:11], v3, %[rs], s57 offen sc1\n\t"
      "v_mfma_f32_16x16x32_bf16 v[196:199], a[208:211], v[68:71], v[196:199]\n\t"
      "v_mfma_f32_16x16x32_bf16 v[204:207], a[212:215], v[68:71], v[204:207]\n\t"
      "v_mfma_f32_16x16x32_bf16 v[212:215], a[216:219], v[68:71], v[212:215]\n\t"
      "s_waitcnt lgkmcnt(2)\n\t"
      "v_mfma_f32_16x16x32_bf16 v[220:223], a[220:223], v[68:71], v[220:223]\n\t"
      "v_mfma_f32_16x16x32_bf16 v[192:195], a[224:227], v[72:75], v[192:195]\n\t"
      "ds_read_b128 v[56:59], v1 offset:16384\n\t"
      "v_mfma_f32_16x16x32_bf16 v[200:203], a[228:231], v[72:75], v[200:203]\n\t"
      "ds_read_b128 v[60:63], v1 offset:36864\n\t"
      "v_mfma_f32_16x16x32_bf16 v[208:211], a[232:235], v[72:75], v[208:211]\n\t"
      "v_mfma_f32_16x16x32_bf16 v[216:219], a[236:239], v[72:75], v[216:219]\n\t"
      "buffer_store_dwordx4 v[8:11], v6, %[rs], s57 offen sc1\n\t"
      "v_mfma_f32_16x16x32_bf16 v[196:199], a[224:227], v[76:79], v[196:199]\n\t"
      "v_mfma_f32_16x16x32_bf16 v[204:207], a[228:231], v[76:79], v[204:207]\n\t"
      "v_mfma_f32_16x16x32_bf16 v[212:215], a[232:235], v[76:79], v[212:215]\n\t"
      "s_waitcnt lgkmcnt(2)\n\t"
      "v_mfma_f32_16x16x32_bf16 v[220:223], a[236:239], v[76:79], v[220:223]\n\t"
      "v_mfma_f32_16x16x32_bf16 v[192:195], a[240:243], v[80:83], v[192:195]\n\t"
      "ds_read_b128 v[64:67], v1 offset:17408\n\t"
      "v_mfma_f32_16x16x32_bf16 v[200:203], a[244:247], v[80:83], v[200:203]\n\t"
      "ds_read_b128 v[68:71], v1 offset:37888\n\t"
      "v_mfma_f32_16x16x32_bf16 v[208:211], a[248:251], v[80:83], v[208:211]\n\t"
      "v_mfma_f32_16x16x32_bf16 v[216:219], a[252:255], v[80:83], v[216:219]\n\t"
      "v_mfma_f32_16x16x32_bf16 v[196:199], a[240:243], v[84:87], v[196:199]\n\t"
      "v_mfma_f32_16x16x32_bf16 v[204:207], a[244:247], v[84:87], v[204:207]\n\t"
      "v_mfma_f32_16x16x32_bf16 v[212:215], a[248:251], v[84:87], v[212:215]\n\t"
      "s_waitcnt lgkmcnt(2)\n\t"
      "v_mfma_f32_16x16x32_bf16 v[220:223], a[252:255], v[84:87], v[220:223]\n\t"
      "v_mfma_f32_16x16x32_bf16 v[192:195], v[128:131], v[56:59], v[192:195]\n\t"
      "ds_read_b128 v[72:75], v1 offset:18432\n\t"
      "v_mfma_f32_16x16x32_bf16 v[200:203], v[132:135], v[56:59], v[200:203]\n\t"
      "ds_read_b128 v[76:79], v1 offset:38912\n\t"
      "v_mfma_f32_16x16x32_bf16 v[208:211], v[136:139], v[56:59], v[208:211]\n\t"
      "v_mfma_f32_16x16x32_bf16 v[216:219], v[140:143], v[56:59], v[216:219]\n\t"
      "v_mfma_f32_16x16x32_bf16 v[196:199], v[128:131], v[60:63], v[196:199]\n\t"
      "v_mfma_f32_16x16x32_bf16 v[204:207], v[132:135], v[60:63], v[204:207]\n\t"
      "v_mfma_f32_16x16x32_bf16 v[212:215], v[136:139], v[60:63], v[212:215]\n\t"
      "s_waitcnt lgkmcnt(2)\n\t"
      "v_mfma_f32_16x16x32_bf16 v[220:223], v[140:143], v[60:63], v[220:223]\n\t"
      "v_mfma_f32_16x16x32_bf16 v[192:195], v[144:147], v[64:67], v[192:195]\n\t"
      "ds_read_b128 v[80:83], v1 offset:19456\n\t"
      "v_mfma_f32_16x16x32_bf16 v[200:203], v[148:151], v[64:67], v[200:203]\n\t"
      "ds_read_b128 v[84:87], v1 offset:39936\n\t"
      "v_mfma_f32_16x16x32_bf16 v[208:211], v[152:155], v[64:67], v[208:211]\n\t"
      "v_mfma_f32_16x16x32_bf16 v[216:219], v[156:159], v[64:67], v[216:219]\n\t"
      "v_mfma_f32_16x16x32_bf16 v[196:199], v[144:147], v[68:71], v[196:199]\n\t"
      "v_mfma_f32_16x16x32_bf16 v[204:207], v[148:151], v[68:71], v[204:207]\n\t"
      "v_mfma_f32_16x16x32_bf16 v[212:215], v[152:155], v[68:71], v[212:215]\n\t"
      "s_waitcnt lgkmcnt(2)\n\t"
      "v_mfma_f32_16x16x32_bf16 v[220:223], v[156:159], v[68:71], v[220:223]\n\t"
      "s_waitcnt vmcnt(3) lgkmcnt(0)\n\t"
      "s_barrier\n\t"
      "v_mfma_f32_16x16x32_bf16 v[192:195], v[160:163], v[72:75], v[192:195]\n\t"
      "ds_read_b128 v[56:59], v13 offset:0\n\t"
      "v_mfma_f32_16x16x32_bf16 v[200:203], v[164:167], v[72:75], v[200:203]\n\t"
      "ds_read_b128 v[60:63], v13 offset:20480\n\t"
      "v_mfma_f32_16x16x32_bf16 v[208:211], v[168:171], v[72:75], v[208:211]\n\t"
      "v_mfma_f32_16x16x32_bf16 v[216:219], v[172:175], v[72:75], v[216:219]\n\t"
      "v_mfma_f32_16x16x32_bf16 v[196:199], v[160:163], v[76:79], v[196:199]\n\t"
      "v_mfma_f32_16x16x32_bf16 v[204:207], v[164:167], v[76:79], v[204:207]\n\t"
      "v_mfma_f32_16x16x32_bf16 v[212:215], v[168:171], v[76:79], v[212:215]\n\t"
      "v_mfma_f32_16x16x32_bf16 v[220:223], v[172:175], v[76:79], v[220:223]\n\t"
      "v_mfma_f32_16x16x32_bf16 v[192:195], v[176:179], v[80:83], v[192:195]\n\t"
      "ds_read_b128 v[64:67], v13 offset:1024\n\t"
      "v_mfma_f32_16x16x32_bf16 v[200:203], v[180:183], v[80:83], v[200:203]\n\t"
      "ds_read_b128 v[68:71], v13 offset:21504\n\t"
      "v_mfma_f32_16x16x32_bf16 v[208:211], v[184:187], v[80:83], v[208:211]\n\t"
      "v_mfma_f32_16x16x32_bf16 v[216:219], v[188:191], v[80:83], v[216:219]\n\t"
      "v_mov_b32 v1, v13\n\t"
      "v_mfma_f32_16x16x32_bf16 v[196:199], v[176:179], v[84:87], v[196:199]\n\t"
      "v_mfma_f32_16x16x32_bf16 v[204:207], v[180:183], v[84:87], v[204:207]\n\t"
      "s_add_u32 s40, s40, 1\n\t"
      "s_cmp_lt_u32 s40, 63\n\t"
      "v_mfma_f32_16x16x32_bf16 v[212:215], v[184:187], v[84:87], v[212:215]\n\t"
      "s_waitcnt lgkmcnt(2)\n\t"
      "v_mfma_f32_16x16x32_bf16 v[220:223], v[188:191], v[84:87], v[220:223]\n\t"
      "s_cbranch_scc0 LLAST%=\n\t"
      "v_mfma_f32_16x16x32_bf16 v[24:27], a[0:3], v[56:59], 0\n\t"
      "ds_read_b128 v[72:75], v1 offset:2048\n\t"
      "v_mfma_f32_16x16x32_bf16 v[32:35], a[4:7], v[56:59], 0\n\t"
      "ds_read_b128 v[76:79], v1 offset:22528\n\t"
      "v_mfma_f32_16x16x32_bf16 v[40:43], a[8:11], v[56:59], 0\n\t"
      "s_xor_b32 s54, s54, 65536\n\t"
      "s_add_u32 s43, s40, 1\n\t"
      "v_mfma_f32_16x16x32_bf16 v[48:51], a[12:15], v[56:59], 0\n\t"
      "s_min_u32 s43, s43, 63\n\t"
      "s_mul_i32 s43, s43, 40960\n\t"
      "v_mfma_f32_16x16x32_bf16 v[28:31], a[0:3], v[60:63], 0\n\t"
      "s_add_u32 s52, s43, 88080384\n\t"
      "s_mul_i32 s57, s40, %[ms]\n\t"
      "v_mfma_f32_16x16x32_bf16 v[36:39], a[4:7], v[60:63], 0\n\t"
      "s_mul_i32 s58, s40, 110592\n\t"
      "s_sub_u32 s56, s40, 1\n\t"
      "v_mfma_f32_16x16x32_bf16 v[44:47], a[8:11], v[60:63], 0\n\t"
      "s_mul_i32 s53, s56, 24576\n\t"
      "s_add_u32 s55, s53, 4096\n\t"
      "s_waitcnt lgkmcnt(2)\n\t"
      "v_mfma_f32_16x16x32_bf16 v[52:55], a[12:15], v[60:63], 0\n\t"
      "v_mov_b32 v5, s56\n\t"
      "v_xor_b32 v13, 65536, v1\n\t"
      "v_mfma_f32_16x16x32_bf16 v[24:27], a[16:19], v[64:67], v[24:27]\n\t"
      "ds_read_b128 v[80:83], v1 offset:3072\n\t"
      "v_cvt_pk_bf16_f32 v88, v192, v200\n\t"
      "v_cvt_pk_bf16_f32 v89, v193, v201\n\t"
      "v_mfma_f32_16x16x32_bf16 v[32:35], a[20:23], v[64:67], v[32:35]\n\t"
      "ds_read_b128 v[84:87], v1 offset:23552\n\t"
      "v_cvt_pk_bf16_f32 v90, v194, v202\n\t"
      "v_cvt_pk_bf16_f32 v91, v195, v203\n\t"
      "v_mfma_f32_16x16x32_bf16 v[40:43], a[24:27], v[64:67], v[40:43]\n\t"
      "buffer_store_dwordx4 v[88:91], v2, %[rs], s53 offen offset:0 sc1\n\t"
      "v_mfma_f32_16x16x32_bf16 v[48:51], a[28:31], v[64:67], v[48:51]\n\t"
      "v_mfma_f32_16x16x32_bf16 v[28:31], a[16:19], v[68:71], v[28:31]\n\t"
      "v_mfma_f32_16x16x32_bf16 v[36:39], a[20:23], v[68:71], v[36:39]\n\t"
      "s_add_u32 m0, s54, 0\n\t"
      "s_add_u32 s43, s52, 0\n\t"
      "buffer_load_dwordx4 v0, %[rs], s43 offen sc1 lds\n\t"
      "v_mfma_f32_16x16x32_bf16 v[44:47], a[24:27], v[68:71], v[44:47]\n\t"
      "s_waitcnt lgkmcnt(2)\n\t"
      "v_mfma_f32_16x16x32_bf16 v[52:55], a[28:31], v[68:71], v[52:55]\n\t"
      "v_mfma_f32_16x16x32_bf16 v[24:27], a[32:35], v[72:75], v[24:27]\n\t"
      "ds_read_b128 v[56:59], v1 offset:4096\n\t"
      "v_cvt_pk_bf16_f32 v92, v208, v216\n\t"
      "v_cvt_pk_bf16_f32 v93, v209, v217\n\t"
      "v_mfma_f32_16x16x32_bf16 v[32:35], a[36:39], v[72:75], v[32:35]\n\t"
      "ds_read_b128 v[60:63], v1 offset:24576\n\t"
      "v_cvt_pk_bf16_f32 v94, v210, v218\n\t"
      "v_cvt_pk_bf16_f32 v95, v211, v219\n\t"
      "v_mfma_f32_16x16x32_bf16 v[40:43], a[40:43], v[72:75], v[40:43]\n\t"
      "buffer_store_dwordx4 v[92:95], v2, %[rs], s53 offen offset:1024 sc1\n\t"
      "v_mfma_f32_16x16x32_bf16 v[48:51], a[44:47], v[72:75], v[48:51]\n\t"
      "v_mfma_f32_16x16x32_bf16 v[28:31], a[32:35], v[76:79], v[28:31]\n\t"
      "v_mfma_f32_16x16x32_bf16 v[36:39], a[36:39], v[76:79], v[36:39]\n\t"
      "s_add_u32 m0, s54, 4096\n\t"
      "s_add_u32 s43, s52, 4096\n\t"
      "buffer_load_dwordx4 v0, %[rs], s43 offen sc1 lds\n\t"
      "v_mfma_f32_16x16x32_bf16 v[44:47], a[40:43], v[76:79], v[44:47]\n\t"
      "s_waitcnt lgkmcnt(2)\n\t"
      "v_mfma_f32_16x16x32_bf16 v[52:55], a[44:47], v[76:79], v[52:55]\n\t"
      "v_mfma_f32_16x16x32_bf16 v[24:27], a[48:51], v[80:83], v[24:27]\n\t"
      "ds_read_b128 v[64:67], v1 offset:5120\n\t"
      "v_cvt_pk_bf16_f32 v96, v196, v204\n\t"
      "v_cvt_pk_bf16_f32 v97, v197, v205\n\t"
      "v_mfma_f32_16x16x32_bf16 v[32:35], a[52:55], v[80:83], v[32:35]\n\t"
      "ds_read_b128 v[68:71], v1 offset:25600\n\t"
      "v_cvt_pk_bf16_f32 v98, v198, v206\n\t"
      "v_cvt_pk_bf16_f32 v99, v199, v207\n\t"
      "v_mfma_f32_16x16x32_bf16 v[40:43], a[56:59], v[80:83], v[40:43]\n\t"
      "buffer_store_dwordx4 v[96:99], v2, %[rs], s55 offen offset:0 sc1\n\t"
      "v_mfma_f32_16x16x32_bf16 v[48:51], a[60:63], v[80:83], v[48:51]\n\t"
      "v_mfma_f32_16x16x32_bf16 v[28:31], a[48:51], v[84:87], v[28:31]\n\t"
      "v_mfma_f32_16x16x32_bf16 v[36:39], a[52:55], v[84:87], v[36:39]\n\t"
      "s_add_u32 m0, s54, 8192\n\t"
      "s_add_u32 s43, s52, 8192\n\t"
      "buffer_load_dwordx4 v0, %[rs], s43 offen sc1 lds\n\t"
      "v_mfma_f32_16x16x32_bf16 v[44:47], a[56:59], v[84:87], v[44:47]\n\t"
      "s_waitcnt lgkmcnt(2)\n\t"
      "v_mfma_f32_16x16x32_bf16 v[52:55], a[60:63], v[84:87], v[52:55]\n\t"
      "v_mfma_f32_16x16x32_bf16 v[24:27], a[64:67], v[56:59], v[24:27]\n\t"
      "ds_read_b128 v[72:75], v1 offset:6144\n\t"
      "v_cvt_pk_bf16_f32 v100, v212, v220\n\t"
      "v_cvt_pk_bf16_f32 v101, v213, v221\n\t"
      "v_mfma_f32_16x16x32_bf16 v[32:35], a[68:71], v[56:59], v[32:35]\n\t"
      "ds_read_b128 v[76:79], v1 offset:26624\n\t"
      "v_cvt_pk_bf16_f32 v102, v214, v222\n\t"
      "v_cvt_pk_bf16_f32 v103, v215, v223\n\t"
      "v_mfma_f32_16x16x32_bf16 v[40:43], a[72:75], v[56:59], v[40:43]\n\t"
      "buffer_store_dwordx4 v[100:103], v2, %[rs], s55 offen offset:1024 sc1\n\t"
      "v_mfma_f32_16x16x32_bf16 v[48:51], a[76:79], v[56:59], v[48:51]\n\t"
      "v_mfma_f32_16x16x32_bf16 v[28:31], a[64:67], v[60:63], v[28:31]\n\t"
      "v_mfma_f32_16x16x32_bf16 v[36:39], a[68:71], v[60:63], v[36:39]\n\t"
      "s_add_u32 m0, s54, 12288\n\t"
      "s_add_u32 s43, s52, 12288\n\t"
      "buffer_load_dwordx4 v0, %[rs], s43 offen sc1 lds\n\t"
      "v_mfma_f32_16x16x32_bf16 v[44:47], a[72:75], v[60:63], v[44:47]\n\t"
      "s_waitcnt lgkmcnt(2)\n\t"
      "v_mfma_f32_16x16x32_bf16 v[52:55], a[76:79], v[60:63], v[52:55]\n\t"
      "v_mfma_f32_16x16x32_bf16 v[24:27], a[80:83], v[64:67], v[24:27]\n\t"
      "ds_read_b128 v[80:83], v1 offset:7168\n\t"
      "v_mfma_f32_16x16x32_bf16 v[32:35], a[84:87], v[64:67], v[32:35]\n\t"
      "ds_read_b128 v[84:87], v1 offset:27648\n\t"
      "v_mfma_f32_16x16x32_bf16 v[40:43], a[88:91], v[64:67], v[40:43]\n\t"
      "v_mfma_f32_16x16x32_bf16 v[48:51], a[92:95], v[64:67], v[48:51]\n\t"
      "v_mfma_f32_16x16x32_bf16 v[28:31], a[80:83], v[68:71], v[28:31]\n\t"
      "v_mfma_f32_16x16x32_bf16 v[36:39], a[84:87], v[68:71], v[36:39]\n\t"
      "s_add_u32 m0, s54, 16384\n\t"
      "s_add_u32 s43, s52, 16384\n\t"
      "buffer_load_dwordx4 v0, %[rs], s43 offen sc1 lds\n\t"
      "v_mfma_f32_16x16x32_bf16 v[44:47], a[88:91], v[68:71], v[44:47]\n\t"
      "s_waitcnt lgkmcnt(2)\n\t"
      "v_mfma_f32_16x16x32_bf16 v[52:55], a[92:95], v[68:71], v[52:55]\n\t"
      "v_mfma_f32_16x16x32_bf16 v[24:27], a[96:99], v[72:75], v[24:27]\n\t"
      "ds_read_b128 v[56:59], v1 offset:8192\n\t"
      "v_mfma_f32_16x16x32_bf16 v[32:35], a[100:103], v[72:75], v[32:35]\n\t"
      "ds_read_b128 v[60:63], v1 offset:28672\n\t"
      "v_mfma_f32_16x16x32_bf16 v[40:43], a[104:107], v[72:75], v[40:43]\n\t"
      "v_mfma_f32_16x16x32_bf16 v[48:51], a[108:111], v[72:75], v[48:51]\n\t"
      "v_mfma_f32_16x16x32_bf16 v[28:31], a[96:99], v[76:79], v[28:31]\n\t"
      "v_mfma_f32_16x16x32_bf16 v[36:39], a[100:103], v[76:79], v[36:39]\n\t"
      "s_add_u32 m0, s54, 20480\n\t"
      "s_add_u32 s43, s52, 20480\n\t"
      "buffer_load_dwordx4 v0, %[rs], s43 offen sc1 lds\n\t"
      "v_mfma_f32_16x16x32_bf16 v[44:47], a[104:107], v[76:79], v[44:47]\n\t"
      "s_waitcnt lgkmcnt(2)\n\t"
      "v_mfma_f32_16x16x32_bf16 v[52:55], a[108:111], v[76:79], v[52:55]\n\t"
      "v_mfma_f32_16x16x32_bf16 v[24:27], a[112:115], v[80:83], v[24:27]\n\t"
      "ds_read_b128 v[64:67], v1 offset:9216\n\t"
      "v_mfma_f32_16x16x32_bf16 v[32:35], a[116:119], v[80:83], v[32:35]\n\t"
      "ds_read_b128 v[68:71], v1 offset:29696\n\t"
      "v_mfma_f32_16x16x32_bf16 v[40:43], a[120:123], v[80:83], v[40:43]\n\t"
      "v_mfma_f32_16x16x32_bf16 v[48:51], a[124:127], v[80:83], v[48:51]\n\t"
      "v_mfma_f32_16x16x32_bf16 v[28:31], a[112:115], v[84:87], v[28:31]\n\t"
      "v_mfma_f32_16x16x32_bf16 v[36:39], a[116:119], v[84:87], v[36:39]\n\t"
      "s_add_u32 m0, s54, 24576\n\t"
      "s_add_u32 s43, s52, 24576\n\t"
      "buffer_load_dwordx4 v0, %[rs], s43 offen sc1 lds\n\t"
      "v_mfma_f32_16x16x32_bf16 v[44:47], a[120:123], v[84:87], v[44:47]\n\t"
      "s_waitcnt lgkmcnt(2)\n\t"
      "v_mfma_f32_16x16x32_bf16 v[52:55], a[124:127], v[84:87], v[52:55]\n\t"
      "v_mfma_f32_16x16x32_bf16 v[24:27], a[128:131], v[56:59], v[24:27]\n\t"
      "ds_read_b128 v[72:75], v1 offset:10240\n\t"
      "v_mfma_f32_16x16x32_bf16 v[32:35], a[132:135], v[56:59], v[32:35]\n\t"
      "ds_read_b128 v[76:79], v1 offset:30720\n\t"
      "v_mfma_f32_16x16x32_bf16 v[40:43], a[136:139], v[56:59], v[40:43]\n\t"
      "v_mfma_f32_16x16x32_bf16 v[48:51], a[140:143], v[56:59], v[48:51]\n\t"
      "v_mfma_f32_16x16x32_bf16 v[28:31], a[128:131], v[60:63], v[28:31]\n\t"
      "v_mfma_f32_16x16x32_bf16 v[36:39], a[132:135], v[60:63], v[36:39]\n\t"
      "s_add_u32 m0, s54, 28672\n\t"
      "s_add_u32 s43, s52, 28672\n\t"
      "buffer_load_dwordx4 v0, %[rs], s43 offen sc1 lds\n\t"
      "v_mfma_f32_16x16x32_bf16 v[44:47], a[136:139], v[60:63], v[44:47]\n\t"
      "s_waitcnt lgkmcnt(2)\n\t"
      "v_mfma_f32_16x16x32_bf16 v[52:55], a[140:143], v[60:63], v[52:55]\n\t"
      "v_mfma_f32_16x16x32_bf16 v[24:27], a[144:147], v[64:67], v[24:27]\n\t"
      "ds_read_b128 v[80:83], v1 offset:11264\n\t"
      "v_mfma_f32_16x16x32_bf16 v[32:35], a[148:151], v[64:67], v[32:35]\n\t"
      "ds_read_b128 v[84:87], v1 offset:31744\n\t"
      "v_mfma_f32_16x16x32_bf16 v[40:43], a[152:155], v[64:67], v[40:43]\n\t"
      "v_mfma_f32_16x16x32_bf16 v[48:51], a[156:159], v[64:67], v[48:51]\n\t"
      "v_mfma_f32_16x16x32_bf16 v[28:31], a[144:147], v[68:71], v[28:31]\n\t"
      "v_mfma_f32_16x16x32_bf16 v[36:39], a[148:151], v[68:71], v[36:39]\n\t"
      "s_add_u32 m0, s54, 32768\n\t"
      "s_add_u32 s43, s52, 32768\n\t"
      "buffer_load_dwordx4 v0, %[rs], s43 offen sc1 lds\n\t"
      "v_mfma_f32_16x16x32_bf16 v[44:47], a[152:155], v[68:71], v[44:47]\n\t"
      "s_waitcnt lgkmcnt(2)\n\t"
      "v_mfma_f32_16x16x32_bf16 v[52:55], a[156:159], v[68:71], v[52:55]\n\t"
      "v_mfma_f32_16x16x32_bf16 v[24:27], a[160:163], v[72:75], v[24:27]\n\t"
      "ds_read_b128 v[56:59], v1 offset:12288\n\t"
      "v_mfma_f32_16x16x32_bf16 v[32:35], a[164:167], v[72:75], v[32:35]\n\t"
      "ds_read_b128 v[60:63], v1 offset:32768\n\t"
      "v_mfma_f32_16x16x32_bf16 v[40:43], a[168:171], v[72:75], v[40:43]\n\t"
      "v_mfma_f32_16x16x32_bf16 v[48:51], a[172:175], v[72:75], v[48:51]\n\t"
      "v_mfma_f32_16x16x32_bf16 v[28:31], a[160:163], v[76:79], v[28:31]\n\t"
      "v_mfma_f32_16x16x32_bf16 v[36:39], a[164:167], v[76:79], v[36:39]\n\t"
      "s_add_u32 m0, s54, 36864\n\t"
      "s_add_u32 s43, s52, 36864\n\t"
      "buffer_load_dwordx4 v0, %[rs], s43 offen sc1 lds\n\t"
      "v_mfma_f32_16x16x32_bf16 v[44:47], a[168:171], v[76:79], v[44:47]\n\t"
      "s_waitcnt lgkmcnt(2)\n\t"
      "v_mfma_f32_16x16x32_bf16 v[52:55], a[172:175], v[76:79], v[52:55]\n\t"
      "v_mfma_f32_16x16x32_bf16 v[24:27], a[176:179], v[80:83], v[24:27]\n\t"
      "ds_read_b128 v[64:67], v1 offset:13312\n\t"
      "v_mfma_f32_16x16x32_bf16 v[32:35], a[180:183], v[80:83], v[32:35]\n\t"
      "ds_read_b128 v[68:71], v1 offset:33792\n\t"
      "v_mfma_f32_16x16x32_bf16 v[40:43], a[184:187], v[80:83], v[40:43]\n\t"
      "v_mfma_f32_16x16x32_bf16 v[48:51], a[188:191], v[80:83], v[48:51]\n\t"
      "s_waitcnt vmcnt(7)\n\t"
      "buffer_store_dword v5, v4, %[rs], %[flo] offen sc1\n\t"
      "v_mfma_f32_16x16x32_bf16 v[28:31], a[176:179], v[84:87], v[28:31]\n\t"
      "v_mfma_f32_16x16x32_bf16 v[36:39], a[180:183], v[84:87], v[36:39]\n\t"
      "v_mfma_f32_16x16x32_bf16 v[44:47], a[184:187], v[84:87], v[44:47]\n\t"
      "s_waitcnt lgkmcnt(2)\n\t"
      "v_mfma_f32_16x16x32_bf16 v[52:55], a[188:191], v[84:87], v[52:55]\n\t"
      "v_mfma_f32_16x16x32_bf16 v[24:27], a[192:195], v[56:59], v[24:27]\n\t"
      "ds_read_b128 v[72:75], v1 offset:14336\n\t"
      "v_mfma_f32_16x16x32_bf16 v[32:35], a[196:199], v[56:59], v[32:35]\n\t"
      "ds_read_b128 v[76:79], v1 offset:34816\n\t"
      "v_mfma_f32_16x16x32_bf16 v[40:43], a[200:203], v[56:59], v[40:43]\n\t"
      "v_mfma_f32_16x16x32_bf16 v[48:51], a[204:207], v[56:59], v[48:51]\n\t"
      "v_mfma_f32_16x16x32_bf16 v[28:31], a[192:195], v[60:63], v[28:31]\n\t"
      "v_mfma_f32_16x16x32_bf16 v[36:39], a[196:199], v[60:63], v[36:39]\n\t"
      "v_mfma_f32_16x16x32_bf16 v[44:47], a[200:203], v[60:63], v[44:47]\n\t"
      "s_waitcnt lgkmcnt(2)\n\t"
      "v_mfma_f32_16x16x32_bf16 v[52:55], a[204:207], v[60:63], v[52:55]\n\t"
      "v_mfma_f32_16x16x32_bf16 v[24:27], a[208:211], v[64:67], v[24:27]\n\t"
      "ds_read_b128 v[80:83], v1 offset:15360\n\t"
      "v_mfma_f32_16x16x32_bf16 v[32:35], a[212:215], v[64:67], v[32:35]\n\t"
      "ds_read_b128 v[84:87], v1 offset:35840\n\t"
      "v_mfma_f32_16x16x32_bf16 v[40:43], a[216:219], v[64:67], v[40:43]\n\t"
      "v_mfma_f32_16x16x32_bf16 v[48:51], a[220:223], v[64:67], v[48:51]\n\t"
      "buffer_store_dwordx4 v[8:11], v3, %[rs], s57 offen sc1\n\t"
      "v_mfma_f32_16x16x32_bf16 v[28:31], a[208:211], v[68:71], v[28:31]\n\t"
      "v_mfma_f32_16x16x32_bf16 v[36:39], a[212:215], v[68:71], v[36:39]\n\t"
      "v_mfma_f32_16x16x32_bf16 v[44:47], a[216:219], v[68:71], v[44:47]\n\t"
      "s_waitcnt lgkmcnt(2)\n\t"
      "v_mfma_f32_16x16x32_bf16 v[52:55], a[220:223], v[68:71], v[52:55]\n\t"
      "v_mfma_f32_16x16x32_bf16 v[24:27], a[224:227], v[72:75], v[24:27]\n\t"
      "ds_read_b128 v[56:59], v1 offset:16384\n\t"
      "v_mfma_f32_16x16x32_bf16 v[32:35], a[228:231], v[72:75], v[32:35]\n\t"
      "ds_read_b128 v[60:63], v1 offset:36864\n\t"
      "v_mfma_f32_16x16x32_bf16 v[40:43], a[232:235], v[72:75], v[40:43]\n\t"
      "v_mfma_f32_16x16x32_bf16 v[48:51], a[236:239], v[72:75], v[48:51]\n\t"
      "buffer_store_dwordx4 v[8:11], v6, %[rs], s57 offen sc1\n\t"
      "v_mfma_f32_16x16x32_bf16 v[28:31], a[224:227], v[76:79], v[28:31]\n\t"
      "v_mfma_f32_16x16x32_bf16 v[36:39], a[228:231], v[76:79], v[36:39]\n\t"
      "v_mfma_f32_16x16x32_bf16 v[44:47], a[232:235], v[76:79], v[44:47]\n\t"
      "s_waitcnt lgkmcnt(2)\n\t"
      "v_mfma_f32_16x16x32_bf16 v[52:55], a[236:239], v[76:79], v[52:55]\n\t"
      "v_mfma_f32_16x16x32_bf16 v[24:27], a[240:243], v[80:83], v[24:27]\n\t"
      "ds_read_b128 v[64:67], v1 offset:17408\n\t"
      "v_mfma_f32_16x16x32_bf16 v[32:35], a[244:247], v[80:83], v[32:35]\n\t"
      "ds_read_b128 v[68:71], v1 offset:37888\n\t"
      "v_mfma_f32_16x16x32_bf16 v[40:43], a[248:251], v[80:83], v[40:43]\n\t"
      "v_mfma_f32_16x16x32_bf16 v[48:51], a[252:255], v[80:83], v[48:51]\n\t"
      "v_mfma_f32_16x16x32_bf16 v[28:31], a[240:243], v[84:87], v[28:31]\n\t"
      "v_mfma_f32_16x16x32_bf16 v[36:39], a[244:247], v[84:87], v[36:39]\n\t"
      "v_mfma_f32_16x16x32_bf16 v[44:47], a[248:251], v[84:87], v[44:47]\n\t"
      "s_waitcnt lgkmcnt(2)\n\t"
      "v_mfma_f32_16x16x32_bf16 v[52:55], a[252:255], v[84:87], v[52:55]\n\t"
      "v_mfma_f32_16x16x32_bf16 v[24:27], v[128:131], v[56:59], v[24:27]\n\t"
      "ds_read_b128 v[72:75], v1 offset:18432\n\t"
      "v_mfma_f32_16x16x32_bf16 v[32:35], v[132:135], v[56:59], v[32:35]\n\t"
      "ds_read_b128 v[76:79], v1 offset:38912\n\t"
      "v_mfma_f32_16x16x32_bf16 v[40:43], v[136:139], v[56:59], v[40:43]\n\t"
      "v_mfma_f32_16x16x32_bf16 v[48:51], v[140:143], v[56:59], v[48:51]\n\t"
      "v_mfma_f32_16x16x32_bf16 v[28:31], v[128:131], v[60:63], v[28:31]\n\t"
      "v_mfma_f32_16x16x32_bf16 v[36:39], v[132:135], v[60:63], v[36:39]\n\t"
      "v_mfma_f32_16x16x32_bf16 v[44:47], v[136:139], v[60:63], v[44:47]\n\t"
      "s_waitcnt lgkmcnt(2)\n\t"
      "v_mfma_f32_16x16x32_bf16 v[52:55], v[140:143], v[60:63], v[52:55]\n\t"
      "v_mfma_f32_16x16x32_bf16 v[24:27], v[144:147], v[64:67], v[24:27]\n\t"
      "ds_read_b128 v[80:83], v1 offset:19456\n\t"
      "v_mfma_f32_16x16x32_bf16 v[32:35], v[148:151], v[64:67], v[32:35]\n\t"
      "ds_read_b128 v[84:87], v1 offset:39936\n\t"
      "v_mfma_f32_16x16x32_bf16 v[40:43], v[152:155], v[64:67], v[40:43]\n\t"
      "v_mfma_f32_16x16x32_bf16 v[48:51], v[156:159], v[64:67], v[48:51]\n\t"
      "v_mfma_f32_16x16x32_bf16 v[28:31], v[144:147], v[68:71], v[28:31]\n\t"
      "v_mfma_f32_16x16x32_bf16 v[36:39], v[148:151], v[68:71], v[36:39]\n\t"
      "v_mfma_f32_16x16x32_bf16 v[44:47], v[152:155], v[68:71], v[44:47]\n\t"
      "s_waitcnt lgkmcnt(2)\n\t"
      "v_mfma_f32_16x16x32_bf16 v[52:55], v[156:159], v[68:71], v[52:55]\n\t"
      "s_waitcnt vmcnt(3) lgkmcnt(0)\n\t"
      "s_barrier\n\t"
      "v_mfma_f32_16x16x32_bf16 v[24:27], v[160:163], v[72:75], v[24:27]\n\t"
      "ds_read_b128 v[56:59], v13 offset:0\n\t"
      "v_mfma_f32_16x16x32_bf16 v[32:35], v[164:167], v[72:75], v[32:35]\n\t"
      "ds_read_b128 v[60:63], v13 offset:20480\n\t"
      "v_mfma_f32_16x16x32_bf16 v[40:43], v[168:171], v[72:75], v[40:43]\n\t"
      "v_mfma_f32_16x16x32_bf16 v[48:51], v[172:175], v[72:75], v[48:51]\n\t"
      "v_mfma_f32_16x16x32_bf16 v[28:31], v[160:163], v[76:79], v[28:31]\n\t"
      "v_mfma_f32_16x16x32_bf16 v[36:39], v[164:167], v[76:79], v[36:39]\n\t"
      "v_mfma_f32_16x16x32_bf16 v[44:47], v[168:171], v[76:79], v[44:47]\n\t"
      "v_mfma_f32_16x16x32_bf16 v[52:55], v[172:175], v[76:79], v[52:55]\n\t"
      "v_mfma_f32_16x16x32_bf16 v[24:27], v[176:179], v[80:83], v[24:27]\n\t"
      "ds_read_b128 v[64:67], v13 offset:1024\n\t"
      "v_mfma_f32_16x16x32_bf16 v[32:35], v[180:183], v[80:83], v[32:35]\n\t"
      "ds_read_b128 v[68:71], v13 offset:21504\n\t"
      "v_mfma_f32_16x16x32_bf16 v[40:43], v[184:187], v[80:83], v[40:43]\n\t"
      "v_mfma_f32_16x16x32_bf16 v[48:51], v[188:191], v[80:83], v[48:51]\n\t"
      "v_mov_b32 v1, v13\n\t"
      "v_mfma_f32_16x16x32_bf16 v[28:31], v[176:179], v[84:87], v[28:31]\n\t"
      "v_mfma_f32_16x16x32_bf16 v[36:39], v[180:183], v[84:87], v[36:39]\n\t"
      "s_add_u32 s40, s40, 1\n\t"
      "s_cmp_lt_u32 s40, 63\n\t"
      "v_mfma_f32_16x16x32_bf16 v[44:47], v[184:187], v[84:87], v[44:47]\n\t"
      "s_waitcnt lgkmcnt(2)\n\t"
      "v_mfma_f32_16x16x32_bf16 v[52:55], v[188:191], v[84:87], v[52:55]\n\t"
      "s_branch LOOP%=\n\t"
      "LLAST%=:\n\t"
      "s_mov_b32 s53, 1548288\n\t"
      "s_mov_b32 s55, 1552384\n\t"
      "v_mov_b32 v5, 63\n\t"
      "s_nop 4\n\t"
      "s_waitcnt lgkmcnt(0)\n\t"
      "v_cvt_pk_bf16_f32 v88, v192, v200\n\t"
      "v_cvt_pk_bf16_f32 v89, v193, v201\n\t"
      "v_cvt_pk_bf16_f32 v90, v194, v202\n\t"
      "v_cvt_pk_bf16_f32 v91, v195, v203\n\t"
      "buffer_store_dwordx4 v[88:91], v2, %[rs], s53 offen offset:0 sc1\n\t"
      "v_cvt_pk_bf16_f32 v92, v208, v216\n\t"
      "v_cvt_pk_bf16_f32 v93, v209, v217\n\t"
      "v_cvt_pk_bf16_f32 v94, v210, v218\n\t"
      "v_cvt_pk_bf16_f32 v95, v211, v219\n\t"
      "buffer_store_dwordx4 v[92:95], v2, %[rs], s53 offen offset:1024 sc1\n\t"
      "v_cvt_pk_bf16_f32 v96, v196, v204\n\t"
      "v_cvt_pk_bf16_f32 v97, v197, v205\n\t"
      "v_cvt_pk_bf16_f32 v98, v198, v206\n\t"
      "v_cvt_pk_bf16_f32 v99, v199, v207\n\t"
      "buffer_store_dwordx4 v[96:99], v2, %[rs], s55 offen offset:0 sc1\n\t"
      "v_cvt_pk_bf16_f32 v100, v212, v220\n\t"
      "v_cvt_pk_bf16_f32 v101, v213, v221\n\t"
      "v_cvt_pk_bf16_f32 v102, v214, v222\n\t"
      "v_cvt_pk_bf16_f32 v103, v215, v223\n\t"
      "buffer_store_dwordx4 v[100:103], v2, %[rs], s55 offen offset:1024 sc1\n\t"
      "s_waitcnt vmcnt(0)\n\t"
      "buffer_store_dword v5, v4, %[rs], %[flo] offen sc1\n\t"
      "s_branch LEND%=\n\t"
      "LFAIL%=:\n\t"
      "v_mov_b32 v12, 1\n\t"
      "s_mov_b32 s43, 688128\n\t"
      "buffer_store_dword v12, off, %[rs], s43 sc1\n\t"
      "LEND%=:\n\t"
      "s_waitcnt vmcnt(0) lgkmcnt(0)\n\t"
      "s_mov_b64 exec, -1\n\t"
      "s_mov_b32 m0, s39\n\t"
      : "+{v[0:7]}"(ad), "+{v[16:23]}"(ld)
      : [rs] "s"(rs), [rw] "s"(rw), [w] "s"(w), [st] "s"(uni(stage)), [cb] "s"(cb), [t0] "s"(tag.x & 0x7FFFFFFFu), [flo] "s"(flo), [ms] "i"(32768)
      : "memory", "vcc", "scc", "v8", "v9", "v10", "v11", "v12", "v13", "v14", "v15", "v24", "v25", "v26", "v27", "v28", "v29", "v30", "v31", "v32", "v33",
        "v34", "v35", "v36", "v37", "v38", "v39", "v40", "v41", "v42", "v43", "v44", "v45", "v46", "v47", "v48", "v49", "v50", "v51", "v52", "v53", "v54",
        "v55", "v56", "v57", "v58", "v59", "v60", "v61", "v62", "v63", "v64", "v65", "v66", "v67", "v68", "v69", "v70", "v71", "v72", "v73", "v74", "v75",
        "v76", "v77", "v78", "v79", "v80", "v81", "v82", "v83", "v84", "v85", "v86", "v87", "v88", "v89", "v90", "v91", "v92", "v93", "v94", "v95", "v96",
        "v97", "v98", "v99", "v100", "v101", "v102", "v103", "v104", "v105", "v106", "v107", "v108", "v109", "v110", "v111", "v112", "v113", "v114", "v115",
        "v116", "v117", "v118", "v119", "v120", "v121", "v122", "v123", "v124", "v125", "v126", "v127", "v128", "v129", "v130", "v131", "v132", "v133",
        "v134", "v135", "v136", "v137", "v138", "v139", "v140", "v141", "v142", "v143", "v144", "v145", "v146", "v147", "v148", "v149", "v150", "v151",
        "v152", "v153", "v154", "v155", "v156", "v157", "v158", "v159", "v160", "v161", "v162", "v163", "v164", "v165", "v166", "v167", "v168", "v169",
        "v170", "v171", "v172", "v173", "v174", "v175", "v176", "v177", "v178", "v179", "v180", "v181", "v182", "v183", "v184", "v185", "v186", "v187",
        "v188", "v189", "v190", "v191", "v192", "v193", "v194", "v195", "v196", "v197", "v198", "v199", "v200", "v201", "v202", "v203", "v204", "v205",
        "v206", "v207", "v208", "v209", "v210", "v211", "v212", "v213", "v214", "v215", "v216", "v217", "v218", "v219", "v220", "v221", "v222", "v223", "a0",
        "a1", "a2", "a3", "a4", "a5", "a6", "a7", "a8", "a9", "a10", "a11", "a12", "a13", "a14", "a15", "a16", "a17", "a18", "a19", "a20", "a21", "a22",
        "a23", "a24", "a25", "a26", "a27", "a28", "a29", "a30", "a31", "a32", "a33", "a34", "a35", "a36", "a37", "a38", "a39", "a40", "a41", "a42", "a43",
        "a44", "a45", "a46", "a47", "a48", "a49", "a50", "a51", "a52", "a53", "a54", "a55", "a56", "a57", "a58", "a59", "a60", "a61", "a62", "a63", "a64",
        "a65", "a66", "a67", "a68", "a69", "a70", "a71", "a72", "a73", "a74", "a75", "a76", "a77", "a78", "a79", "a80", "a81", "a82", "a83", "a84", "a85",
        "a86", "a87", "a88", "a89", "a90", "a91", "a92", "a93", "a94", "a95", "a96", "a97", "a98", "a99", "a100", "a101", "a102", "a103", "a104", "a105",
        "a106", "a107", "a108", "a109", "a110", "a111", "a112", "a113", "a114", "a115", "a116", "a117", "a118", "a119", "a120", "a121", "a122", "a123",
        "a124", "a125", "a126", "a127", "a128", "a129", "a130", "a131", "a132", "a133", "a134", "a135", "a136", "a137", "a138", "a139", "a140", "a141",
        "a142", "a143", "a144", "a145", "a146", "a147", "a148", "a149", "a150", "a151", "a152", "a153", "a154", "a155", "a156", "a157", "a158", "a159",
        "a160", "a161", "a162", "a163", "a164", "a165", "a166", "a167", "a168", "a169", "a170", "a171", "a172", "a173", "a174", "a175", "a176", "a177",
        "a178", "a179", "a180", "a181", "a182", "a183", "a184", "a185", "a186", "a187", "a188", "a189", "a190", "a191", "a192", "a193", "a194", "a195",
        "a196", "a197", "a198", "a199", "a200", "a201", "a202", "a203", "a204", "a205", "a206", "a207", "a208", "a209", "a210", "a211", "a212", "a213",
        "a214", "a215", "a216", "a217", "a218", "a219", "a220", "a221", "a222", "a223", "a224", "a225", "a226", "a227", "a228", "a229", "a230", "a231",
        "a232", "a233", "a234", "a235", "a236", "a237", "a238", "a239", "a240", "a241", "a242", "a243", "a244", "a245", "a246", "a247", "a248", "a249",
        "a250", "a251", "a252", "a253", "a254", "a255", "s33", "s34", "s35", "s36", "s37", "s38", "s39", "s40", "s41", "s42", "s43", "s44", "s45", "s46",
        "s47", "s48", "s49", "s50", "s51", "s52", "s53", "s54", "s55", "s56", "s57", "s58", "s59", "s60", "s61", "s62", "s63", "s64", "s65", "s66", "s67"
    );
  }
}

DEV void touch(const char* src, unsigned ldsaddr) {
  unsigned keep;
  asm volatile("s_mov_b32 %0, m0\n\ts_mov_b32 m0, %2\n\ts_nop 0\n\tglobal_load_lds_dwordx4 %1, off\n\ts_mov_b32 m0, %0\n\ts_waitcnt vmcnt(12)"
               : "=&s"(keep) : "v"(src), "s"(ldsaddr) : "memory");
}
DEV void role_help(const Params& p, LAS char* lds, int h) {
  const int lane = threadIdx.x & 63, w = uni(threadIdx.x >> 6);
  const unsigned lb = (unsigned)(size_t)lds + w * 32768;
  const size_t o = (size_t)((h >> 1) * 512 + (lane & 31) * 16) + (size_t)((h & 1) * 8 + w * 2 + (lane >> 5)) * 8192;
  __builtin_amdgcn_s_sleep(0);
  for (int i = 0; i < 13 * 8; ++i) {
    const int gm = (0x266499u >> (2 * (i >> 3))) & 3, gp = (0x4344332221100ull >> (4 * (i >> 3))) & 7;
    touch((gm == 0 ? p.wq : gm == 1 ? p.wk : p.wv) + o + (size_t)(gp * 128 + (i & 7) * 16) * 8192, lb + (i & 15) * 1024);
  }
  drain();
}

DEV void role_gu(const Params& p, LAS char* lds, unsigned flip, int idx, int last) {
  const int lane = threadIdx.x & 63, w = uni(threadIdx.x >> 6), g = lane >> 4, c = lane & 15;
  if (!wait_words<8>(p.ws, CTL_QKV + (6 * lane + 1) * 128, LATE_START)) return;
  const unsigned lb = (unsigned)(size_t)lds, stage = lb + w * 32768;
  u8v ad, ld = loader_regs<RM_NAT>(lane, FF * 2, stage, 1);
  ad[0] = w * 1024 + lane * 16;
  ad[1] = ring_phys(EW_H + ((lane / 5) * 20 + 4 * (lane % 5) + w) * 128, flip ^ 1);
  ad[2] = 40960 + (4 * (lane >> 3) + w) * 128 + (lane & 7) * 16;
  ad[3] = lb + lane * 16;
  ad[4] = lb + 40960 + g * 1280 + c * 8;
  ad[5] = WS_ACT + ((g & 1) * 54 + idx * 2 + (w >> 1)) * 1024 + ((w & 1) * 2 + (g >> 1)) * 256 + c * 16 + (last ? DROP : 0u);
  ad[6] = (lane == 0) ? 0u : DROP;
  ad[7] = 0;
  const u4 rs = mk_rs(p.ws, WS_END), rg = mk_rs(p.wg, DM * FF * 2), ru = mk_rs(p.wu, DM * FF * 2);
  asm volatile(
      "s_mov_b32 s44, 0xcccc3333\n\t"
      "s_mov_b32 s45, 0xcccc3333\n\t"
      "s_not_b64 s[46:47], s[44:45]\n\t"
      "s_nop 4\n\t"
      "s_mov_b32 s39, m0\n\t"
      "s_cmp_eq_u32 %[w], 0\n\t"
      "s_cselect_b64 s[66:67], 1, 0\n\t"
      "s_add_u32 m0, %[st], 0\n\t"
      "s_add_u32 s43, %[cb], 0\n\t"
      "s_mov_b64 exec, s[44:45]\n\t"
      "buffer_load_dwordx4 v16, %[rg], s43 offen lds\n\t"
      "buffer_load_dwordx4 v17, %[rg], s43 offen offset:1024 lds\n\t"
      "s_mov_b64 exec, s[46:47]\n\t"
      "buffer_load_dwordx4 v16, %[ru], s43 offen lds\n\t"
      "buffer_load_dwordx4 v17, %[ru], s43 offen offset:1024 lds\n\t"
      "s_mov_b64 exec, -1\n\t"
      "s_add_u32 m0, %[st], 2048\n\t"
      "s_add_u32 s43, %[cb], 110592\n\t"
      "s_mov_b64 exec, s[44:45]\n\t"
      "buffer_load_dwordx4 v16, %[rg], s43 offen lds\n\t"
      "buffer_load_dwordx4 v17, %[rg], s43 offen offset:1024 lds\n\t"
      "s_mov_b64 exec, s[46:47]\n\t"
      "buffer_load_dwordx4 v16, %[ru], s43 offen lds\n\t"
      "buffer_load_dwordx4 v17, %[ru], s43 offen offset:1024 lds\n\t"
      "s_mov_b64 exec, -1\n\t"
      "s_add_u32 m0, %[st], 4096\n\t"
      "s_add_u32 s43, %[cb], 221184\n\t"
      "s_mov_b64 exec, s[44:45]\n\t"
      "buffer_load_dwordx4 v16, %[rg], s43 offen lds\n\t"
      "buffer_load_dwordx4 v17, %[rg], s43 offen offset:1024 lds\n\t"
      "s_mov_b64 exec, s[46:47]\n\t"
      "buffer_load_dwordx4 v16, %[ru], s43 offen lds\n\t"
      "buffer_load_dwordx4 v17, %[ru], s43 offen offset:1024 lds\n\t"
      "s_mov_b64 exec, -1\n\t"
      "s_add_u32 m0, %[st], 6144\n\t"
      "s_add_u32 s43, %[cb], 331776\n\t"
      "s_mov_b64 exec, s[44:45]\n\t"
      "buffer_load_dwordx4 v16, %[rg], s43 offen lds\n\t"
      "buffer_load_dwordx4 v17, %[rg], s43 offen offset:1024 lds\n\t"
      "s_mov_b64 exec, s[46:47]\n\t"
      "buffer_load_dwordx4 v16, %[ru], s43 offen lds\n\t"
      "buffer_load_dwordx4 v17, %[ru], s43 offen offset:1024 lds\n\t"
      "s_mov_b64 exec, -1\n\t"
      "s_add_u32 m0, %[st], 8192\n\t"
      "s_add_u32 s43, %[cb], 442368\n\t"
      "s_mov_b64 exec, s[44:45]\n\t"
      "buffer_load_dwordx4 v16, %[rg], s43 offen lds\n\t"
      "buffer_load_dwordx4 v17, %[rg], s43 offen offset:1024 lds\n\t"
      "s_mov_b64 exec, s[46:47]\n\t"
      "buffer_load_dwordx4 v16, %[ru], s43 offen lds\n\t"
      "buffer_load_dwordx4 v17, %[ru], s43 offen offset:1024 lds\n\t"
      "s_mov_b64 exec, -1\n\t"
      "s_add_u32 m0, %[st], 10240\n\t"
      "s_add_u32 s43, %[cb], 552960\n\t"
      "s_mov_b64 exec, s[44:45]\n\t"
      "buffer_load_dwordx4 v16, %[rg], s43 offen lds\n\t"
      "buffer_load_dwordx4 v17, %[rg], s43 offen offset:1024 lds\n\t"
      "s_mov_b64 exec, s[46:47]\n\t"
      "buffer_load_dwordx4 v16, %[ru], s43 offen lds\n\t"
      "buffer_load_dwordx4 v17, %[ru], s43 offen offset:1024 lds\n\t"
      "s_mov_b64 exec, -1\n\t"
      "s_add_u32 m0, %[st], 12288\n\t"
      "s_add_u32 s43, %[cb], 663552\n\t"
      "s_mov_b64 exec, s[44:45]\n\t"
      "buffer_load_dwordx4 v16, %[rg], s43 offen lds\n\t"
      "buffer_load_dwordx4 v17, %[rg], s43 offen offset:1024 lds\n\t"
      "s_mov_b64 exec, s[46:47]\n\t"
      "buffer_load_dwordx4 v16, %[ru], s43 offen lds\n\t"
      "buffer_load_dwordx4 v17, %[ru], s43 offen offset:1024 lds\n\t"
      "s_mov_b64 exec, -1\n\t"
      "s_add_u32 m0, %[st], 14336\n\t"
      "s_add_u32 s43, %[cb], 774144\n\t"
      "s_mov_b64 exec, s[44:45]\n\t"
      "buffer_load_dwordx4 v16, %[rg], s43 offen lds\n\t"
      "buffer_load_dwordx4 v17, %[rg], s43 offen offset:1024 lds\n\t"
      "s_mov_b64 exec, s[46:47]\n\t"
      "buffer_load_dwordx4 v16, %[ru], s43 offen lds\n\t"
      "buffer_load_dwordx4 v17, %[ru], s43 offen offset:1024 lds\n\t"
      "s_mov_b64 exec, -1\n\t"
      "s_waitcnt vmcnt(24)\n\t"
      "ds_read_b64_tr_b16 a[0:1], v18 offset:0\n\t"
      "ds_read_b64_tr_b16 a[2:3], v18 offset:1024\n\t"
      "ds_read_b64_tr_b16 a[4:5], v19 offset:0\n\t"
      "ds_read_b64_tr_b16 a[6:7], v19 offset:1024\n\t"
      "ds_read_b64_tr_b16 a[8:9], v18 offset:2048\n\t"
      "ds_read_b64_tr_b16 a[10:11], v18 offset:3072\n\t"
      "ds_read_b64_tr_b16 a[12:13], v19 offset:2048\n\t"
      "ds_read_b64_tr_b16 a[14:15], v19 offset:3072\n\t"
      "s_waitcnt lgkmcnt(0)\n\t"
      "s_add_u32 m0, %[st], 0\n\t"
      "s_add_u32 s43, %[cb], 884736\n\t"
      "s_mov_b64 exec, s[44:45]\n\t"
      "buffer_load_dwordx4 v16, %[rg], s43 offen lds\n\t"
      "buffer_load_dwordx4 v17, %[rg], s43 offen offset:1024 lds\n\t"
      "s_mov_b64 exec, s[46:47]\n\t"
      "buffer_load_dwordx4 v16, %[ru], s43 offen lds\n\t"
      "buffer_load_dwordx4 v17, %[ru], s43 offen offset:1024 lds\n\t"
      "s_mov_b64 exec, -1\n\t"
      "s_add_u32 m0, %[st], 2048\n\t"
      "s_add_u32 s43, %[cb], 995328\n\t"
      "s_mov_b64 exec, s[44:45]\n\t"
      "buffer_load_dwordx4 v16, %[rg], s43 offen lds\n\t"
      "buffer_load_dwordx4 v17, %[rg], s43 offen offset:1024 lds\n\t"
      "s_mov_b64 exec, s[46:47]\n\t"
      "buffer_load_dwordx4 v16, %[ru], s43 offen lds\n\t"
      "buffer_load_dwordx4 v17, %[ru], s43 offen offset:1024 lds\n\t"
      "s_mov_b64 exec, -1\n\t"
      "s_waitcnt vmcnt(24)\n\t"
      "ds_read_b64_tr_b16 a[16:17], v18 offset:4096\n\t"
      "ds_read_b64_tr_b16 a[18:19], v18 offset:5120\n\t"
      "ds_read_b64_tr_b16 a[20:21], v19 offset:4096\n\t"
      "ds_read_b64_tr_b16 a[22:23], v19 offset:5120\n\t"
      "ds_read_b64_tr_b16 a[24:25], v18 offset:6144\n\t"
      "ds_read_b64_tr_b16 a[26:27], v18 offset:7168\n\t"
      "ds_read_b64_tr_b16 a[28:29], v19 offset:6144\n\t"
      "ds_read_b64_tr_b16 a[30:31], v19 offset:7168\n\t"
      "s_waitcnt lgkmcnt(0)\n\t"
      "s_add_u32 m0, %[st], 4096\n\t"
      "s_add_u32 s43, %[cb], 1105920\n\t"
      "s_mov_b64 exec, s[44:45]\n\t"
      "buffer_load_dwordx4 v16, %[rg], s43 offen lds\n\t"
      "buffer_load_dwordx4 v17, %[rg], s43 offen offset:1024 lds\n\t"
      "s_mov_b64 exec, s[46:47]\n\t"
      "buffer_load_dwordx4 v16, %[ru], s43 offen lds\n\t"
      "buffer_load_dwordx4 v17, %[ru], s43 offen offset:1024 lds\n\t"
      "s_mov_b64 exec, -1\n\t"
      "s_add_u32 m0, %[st], 6144\n\t"
      "s_add_u32 s43, %[cb], 1216512\n\t"
      "s_mov_b64 exec, s[44:45]\n\t"
      "buffer_load_dwordx4 v16, %[rg], s43 offen lds\n\t"
      "buffer_load_dwordx4 v17, %[rg], s43 offen offset:1024 lds\n\t"
      "s_mov_b64 exec, s[46:47]\n\t"
      "buffer_load_dwordx4 v16, %[ru], s43 offen lds\n\t"
      "buffer_load_dwordx4 v17, %[ru], s43 offen offset:1024 lds\n\t"
      "s_mov_b64 exec, -1\n\t"
      "s_waitcnt vmcnt(24)\n\t"
      "ds_read_b64_tr_b16 a[32:33], v18 offset:8192\n\t"
      "ds_read_b64_tr_b16 a[34:35], v18 offset:9216\n\t"
      "ds_read_b64_tr_b16 a[36:37], v19 offset:8192\n\t"
      "ds_read_b64_tr_b16 a[38:39], v19 offset:9216\n\t"
      "ds_read_b64_tr_b16 a[40:41], v18 offset:10240\n\t"
      "ds_read_b64_tr_b16 a[42:43], v18 offset:11264\n\t"
      "ds_read_b64_tr_b16 a[44:45], v19 offset:10240\n\t"
      "ds_read_b64_tr_b16 a[46:47], v19 offset:11264\n\t"
      "s_waitcnt lgkmcnt(0)\n\t"
      "s_add_u32 m0, %[st], 8192\n\t"
      "s_add_u32 s43, %[cb], 1327104\n\t"
      "s_mov_b64 exec, s[44:45]\n\t"
      "buffer_load_dwordx4 v16, %[rg], s43 offen lds\n\t"
      "buffer_load_dwordx4 v17, %[rg], s43 offen offset:1024 lds\n\t"
      "s_mov_b64 exec, s[46:47]\n\t"
      "buffer_load_dwordx4 v16, %[ru], s43 offen lds\n\t"
      "buffer_load_dwordx4 v17, %[ru], s43 offen offset:1024 lds\n\t"
      "s_mov_b64 exec, -1\n\t"
      "s_add_u32 m0, %[st], 10240\n\t"
      "s_add_u32 s43, %[cb], 1437696\n\t"
      "s_mov_b64 exec, s[44:45]\n\t"
      "buffer_load_dwordx4 v16, %[rg], s43 offen lds\n\t"
      "buffer_load_dwordx4 v17, %[rg], s43 offen offset:1024 lds\n\t"
      "s_mov_b64 exec, s[46:47]\n\t"
      "buffer_load_dwordx4 v16, %[ru], s43 offen lds\n\t"
      "buffer_load_dwordx4 v17, %[ru], s43 offen offset:1024 lds\n\t"
      "s_mov_b64 exec, -1\n\t"
      "s_waitcnt vmcnt(24)\n\t"
      "ds_read_b64_tr_b16 a[48:49], v18 offset:12288\n\t"
      "ds_read_b64_tr_b16 a[50:51], v18 offset:13312\n\t"
      "ds_read_b64_tr_b16 a[52:53], v19 offset:12288\n\t"
      "ds_read_b64_tr_b16 a[54:55], v19 offset:13312\n\t"
      "ds_read_b64_tr_b16 a[56:57], v18 offset:14336\n\t"
      "ds_read_b64_tr_b16 a[58:59], v18 offset:15360\n\t"
      "ds_read_b64_tr_b16 a[60:61], v19 offset:14336\n\t"
      "ds_read_b64_tr_b16 a[62:63], v19 offset:15360\n\t"
      "s_waitcnt lgkmcnt(0)\n\t"
      "s_add_u32 m0, %[st], 12288\n\t"
      "s_add_u32 s43, %[cb], 1548288\n\t"
      "s_mov_b64 exec, s[44:45]\n\t"
      "buffer_load_dwordx4 v16, %[rg], s43 offen lds\n\t"
      "buffer_load_dwordx4 v17, %[rg], s43 offen offset:1024 lds\n\t"
      "s_mov_b64 exec, s[46:47]\n\t"
      "buffer_load_dwordx4 v16, %[ru], s43 offen lds\n\t"
      "buffer_load_dwordx4 v17, %[ru], s43 offen offset:1024 lds\n\t"
      "s_mov_b64 exec, -1\n\t"
      "s_add_u32 m0, %[st], 14336\n\t"
      "s_add_u32 s43, %[cb], 1658880\n\t"
      "s_mov_b64 exec, s[44:45]\n\t"
      "buffer_load_dwordx4 v16, %[rg], s43 offen lds\n\t"
      "buffer_load_dwordx4 v17, %[rg], s43 offen offset:1024 lds\n\t"
      "s_mov_b64 exec, s[46:47]\n\t"
      "buffer_load_dwordx4 v16, %[ru], s43 offen lds\n\t"
      "buffer_load_dwordx4 v17, %[ru], s43 offen offset:1024 lds\n\t"
      "s_mov_b64 exec, -1\n\t"
      "s_waitcnt vmcnt(24)\n\t"
      "ds_read_b64_tr_b16 a[64:65], v18 offset:0\n\t"
      "ds_read_b64_tr_b16 a[66:67], v18 offset:1024\n\t"
      "ds_read_b64_tr_b16 a[68:69], v19 offset:0\n\t"
      "ds_read_b64_tr_b16 a[70:71], v19 offset:1024\n\t"
      "ds_read_b64_tr_b16 a[72:73], v18 offset:2048\n\t"
      "ds_read_b64_tr_b16 a[74:75], v18 offset:3072\n\t"
      "ds_read_b64_tr_b16 a[76:77], v19 offset:2048\n\t"
      "ds_read_b64_tr_b16 a[78:79], v19 offset:3072\n\t"
      "s_waitcnt lgkmcnt(0)\n\t"
      "s_add_u32 m0, %[st], 0\n\t"
      "s_add_u32 s43, %[cb], 1769472\n\t"
      "s_mov_b64 exec, s[44:45]\n\t"
      "buffer_load_dwordx4 v16, %[rg], s43 offen lds\n\t"
      "buffer_load_dwordx4 v17, %[rg], s43 offen offset:1024 lds\n\t"
      "s_mov_b64 exec, s[46:47]\n\t"
      "buffer_load_dwordx4 v16, %[ru], s43 offen lds\n\t"
      "buffer_load_dwordx4 v17, %[ru], s43 offen offset:1024 lds\n\t"
      "s_mov_b64 exec, -1\n\t"
      "s_add_u32 m0, %[st], 2048\n\t"
      "s_add_u32 s43, %[cb], 1880064\n\t"
      "s_mov_b64 exec, s[44:45]\n\t"
      "buffer_load_dwordx4 v16, %[rg], s43 offen lds\n\t"
      "buffer_load_dwordx4 v17, %[rg], s43 offen offset:1024 lds\n\t"
      "s_mov_b64 exec, s[46:47]\n\t"
      "buffer_load_dwordx4 v16, %[ru], s43 offen lds\n\t"
      "buffer_load_dwordx4 v17, %[ru], s43 offen offset:1024 lds\n\t"
      "s_mov_b64 exec, -1\n\t"
      "s_waitcnt vmcnt(24)\n\t"
      "ds_read_b64_tr_b16 a[80:81], v18 offset:4096\n\t"
      "ds_read_b64_tr_b16 a[82:83], v18 offset:5120\n\t"
      "ds_read_b64_tr_b16 a[84:85], v19 offset:4096\n\t"
      "ds_read_b64_tr_b16 a[86:87], v19 offset:5120\n\t"
      "ds_read_b64_tr_b16 a[88:89], v18 offset:6144\n\t"
      "ds_read_b64_tr_b16 a[90:91], v18 offset:7168\n\t"
      "ds_read_b64_tr_b16 a[92:93], v19 offset:6144\n\t"
      "ds_read_b64_tr_b16 a[94:95], v19 offset:7168\n\t"
      "s_waitcnt lgkmcnt(0)\n\t"
      "s_add_u32 m0, %[st], 4096\n\t"
      "s_add_u32 s43, %[cb], 1990656\n\t"
      "s_mov_b64 exec, s[44:45]\n\t"
      "buffer_load_dwordx4 v16, %[rg], s43 offen lds\n\t"
      "buffer_load_dwordx4 v17, %[rg], s43 offen offset:1024 lds\n\t"
      "s_mov_b64 exec, s[46:47]\n\t"
      "buffer_load_dwordx4 v16, %[ru], s43 offen lds\n\t"
      "buffer_load_dwordx4 v17, %[ru], s43 offen offset:1024 lds\n\t"
      "s_mov_b64 exec, -1\n\t"
      "s_add_u32 m0, %[st], 6144\n\t"
      "s_add_u32 s43, %[cb], 2101248\n\t"
      "s_mov_b64 exec, s[44:45]\n\t"
      "buffer_load_dwordx4 v16, %[rg], s43 offen lds\n\t"
      "buffer_load_dwordx4 v17, %[rg], s43 offen offset:1024 lds\n\t"
      "s_mov_b64 exec, s[46:47]\n\t"
      "buffer_load_dwordx4 v16, %[ru], s43 offen lds\n\t"
      "buffer_load_dwordx4 v17, %[ru], s43 offen offset:1024 lds\n\t"
      "s_mov_b64 exec, -1\n\t"
      "s_waitcnt vmcnt(24)\n\t"
      "ds_read_b64_tr_b16 a[96:97], v18 offset:8192\n\t"
      "ds_read_b64_tr_b16 a[98:99], v18 offset:9216\n\t"
      "ds_read_b64_tr_b16 a[100:101], v19 offset:8192\n\t"
      "ds_read_b64_tr_b16 a[102:103], v19 offset:9216\n\t"
      "ds_read_b64_tr_b16 a[104:105], v18 offset:10240\n\t"
      "ds_read_b64_tr_b16 a[106:107], v18 offset:11264\n\t"
      "ds_read_b64_tr_b16 a[108:109], v19 offset:10240\n\t"
      "ds_read_b64_tr_b16 a[110:111], v19 offset:11264\n\t"
      "s_waitcnt lgkmcnt(0)\n\t"
      "s_waitcnt vmcnt(16)\n\t"
      "ds_read_b64_tr_b16 a[112:113], v18 offset:12288\n\t"
      "ds_read_b64_tr_b16 a[114:115], v18 offset:13312\n\t"
      "ds_read_b64_tr_b16 a[116:117], v19 offset:12288\n\t"
      "ds_read_b64_tr_b16 a[118:119], v19 offset:13312\n\t"
      "ds_read_b64_tr_b16 a[120:121], v18 offset:14336\n\t"
      "ds_read_b64_tr_b16 a[122:123], v18 offset:15360\n\t"
      "ds_read_b64_tr_b16 a[124:125], v19 offset:14336\n\t"
      "ds_read_b64_tr_b16 a[126:127], v19 offset:15360\n\t"
      "s_waitcnt lgkmcnt(0)\n\t"
      "s_waitcnt vmcnt(8)\n\t"
      "ds_read_b64_tr_b16 a[128:129], v18 offset:0\n\t"
      "ds_read_b64_tr_b16 a[130:131], v18 offset:1024\n\t"
      "ds_read_b64_tr_b16 a[132:133], v19 offset:0\n\t"
      "ds_read_b64_tr_b16 a[134:135], v19 offset:1024\n\t"
      "ds_read_b64_tr_b16 a[136:137], v18 offset:2048\n\t"
      "ds_read_b64_tr_b16 a[138:139], v18 offset:3072\n\t"
      "ds_read_b64_tr_b16 a[140:141], v19 offset:2048\n\t"
      "ds_read_b64_tr_b16 a[142:143], v19 offset:3072\n\t"
      "s_waitcnt lgkmcnt(0)\n\t"
      "s_waitcnt vmcnt(0)\n\t"
      "ds_read_b64_tr_b16 a[144:145], v18 offset:4096\n\t"
      "ds_read_b64_tr_b16 a[146:147], v18 offset:5120\n\t"
      "ds_read_b64_tr_b16 a[148:149], v19 offset:4096\n\t"
      "ds_read_b64_tr_b16 a[150:151], v19 offset:5120\n\t"
      "ds_read_b64_tr_b16 a[152:153], v18 offset:6144\n\t"
      "ds_read_b64_tr_b16 a[154:155], v18 offset:7168\n\t"
      "ds_read_b64_tr_b16 a[156:157], v19 offset:6144\n\t"
      "ds_read_b64_tr_b16 a[158:159], v19 offset:7168\n\t"
      "s_waitcnt lgkmcnt(0)\n\t"
      "s_waitcnt lgkmcnt(0)\n\t"
      "s_barrier\n\t"
      "v_mov_b32 v24, 0x3f803f80\n\t"
      "v_mov_b32 v25, 0x3f803f80\n\t"
      "v_mov_b32 v26, 0x3f803f80\n\t"
      "v_mov_b32 v27, 0x3f803f80\n\t"
      "s_nop 1\n\t"
      "v_mfma_f32_16x16x32_bf16 v[8:11], a[0:3], v[24:27], 0\n\t"
      "v_mfma_f32_16x16x32_bf16 v[12:15], a[4:7], v[24:27], 0\n\t"
      "v_mfma_f32_16x16x32_bf16 v[8:11], a[8:11], v[24:27], v[8:11]\n\t"
      "v_mfma_f32_16x16x32_bf16 v[12:15], a[12:15], v[24:27], v[12:15]\n\t"
      "v_mfma_f32_16x16x32_bf16 v[8:11], a[16:19], v[24:27], v[8:11]\n\t"
      "v_mfma_f32_16x16x32_bf16 v[12:15], a[20:23], v[24:27], v[12:15]\n\t"
      "v_mfma_f32_16x16x32_bf16 v[8:11], a[24:27], v[24:27], v[8:11]\n\t"
      "v_mfma_f32_16x16x32_bf16 v[12:15], a[28:31], v[24:27], v[12:15]\n\t"
      "v_mfma_f32_16x16x32_bf16 v[8:11], a[32:35], v[24:27], v[8:11]\n\t"
      "v_mfma_f32_16x16x32_bf16 v[12:15], a[36:39], v[24:27], v[12:15]\n\t"
      "v_mfma_f32_16x16x32_bf16 v[8:11], a[40:43], v[24:27], v[8:11]\n\t"
      "v_mfma_f32_16x16x32_bf16 v[12:15], a[44:47], v[24:27], v[12:15]\n\t"
      "v_mfma_f32_16x16x32_bf16 v[8:11], a[48:51], v[24:27], v[8:11]\n\t"
      "v_mfma_f32_16x16x32_bf16 v[12:15], a[52:55], v[24:27], v[12:15]\n\t"
      "v_mfma_f32_16x16x32_bf16 v[8:11], a[56:59], v[24:27], v[8:11]\n\t"
      "v_mfma_f32_16x16x32_bf16 v[12:15], a[60:63], v[24:27], v[12:15]\n\t"
      "v_mfma_f32_16x16x32_bf16 v[8:11], a[64:67], v[24:27], v[8:11]\n\t"
      "v_mfma_f32_16x16x32_bf16 v[12:15], a[68:71], v[24:27], v[12:15]\n\t"
      "v_mfma_f32_16x16x32_bf16 v[8:11], a[72:75], v[24:27], v[8:11]\n\t"
      "v_mfma_f32_16x16x32_bf16 v[12:15], a[76:79], v[24:27], v[12:15]\n\t"
      "v_mfma_f32_16x16x32_bf16 v[8:11], a[80:83], v[24:27], v[8:11]\n\t"
      "v_mfma_f32_16x16x32_bf16 v[12:15], a[84:87], v[24:27], v[12:15]\n\t"
      "v_mfma_f32_16x16x32_bf16 v[8:11], a[88:91], v[24:27], v[8:11]\n\t"
      "v_mfma_f32_16x16x32_bf16 v[12:15], a[92:95], v[24:27], v[12:15]\n\t"
      "v_mfma_f32_16x16x32_bf16 v[8:11], a[96:99], v[24:27], v[8:11]\n\t"
      "v_mfma_f32_16x16x32_bf16 v[12:15], a[100:103], v[24:27], v[12:15]\n\t"
      "v_mfma_f32_16x16x32_bf16 v[8:11], a[104:107], v[24:27], v[8:11]\n\t"
      "v_mfma_f32_16x16x32_bf16 v[12:15], a[108:111], v[24:27], v[12:15]\n\t"
      "v_mfma_f32_16x16x32_bf16 v[8:11], a[112:115], v[24:27], v[8:11]\n\t"
      "v_mfma_f32_16x16x32_bf16 v[12:15], a[116:119], v[24:27], v[12:15]\n\t"
      "v_mfma_f32_16x16x32_bf16 v[8:11], a[120:123], v[24:27], v[8:11]\n\t"
      "v_mfma_f32_16x16x32_bf16 v[12:15], a[124:127], v[24:27], v[12:15]\n\t"
      "v_mfma_f32_16x16x32_bf16 v[8:11], a[128:131], v[24:27], v[8:11]\n\t"
      "v_mfma_f32_16x16x32_bf16 v[12:15], a[132:135], v[24:27], v[12:15]\n\t"
      "v_mfma_f32_16x16x32_bf16 v[8:11], a[136:139], v[24:27], v[8:11]\n\t"
      "v_mfma_f32_16x16x32_bf16 v[12:15], a[140:143], v[24:27], v[12:15]\n\t"
      "v_mfma_f32_16x16x32_bf16 v[8:11], a[144:147], v[24:27], v[8:11]\n\t"
      "v_mfma_f32_16x16x32_bf16 v[12:15], a[148:151], v[24:27], v[12:15]\n\t"
      "v_mfma_f32_16x16x32_bf16 v[8:11], a[152:155], v[24:27], v[8:11]\n\t"
      "v_mfma_f32_16x16x32_bf16 v[12:15], a[156:159], v[24:27], v[12:15]\n\t"
      "s_mov_b32 s40, 0\n\t"
      "s_mov_b32 s52, 70254592\n\t"
      "s_mov_b32 s53, 0\n\t"
      "s_lshl_b32 s54, %[w], 10\n\t"
      "s_mul_i32 s55, %[w], 1280\n\t"
      "s_add_u32 s55, s55, 40960\n\t"
      "s_mov_b32 s57, 0x3acccccd\n\t"
      "s_mov_b32 s58, 0xbfb8aa3b\n\t"
      "s_mov_b32 s41, 0\n\t"
      "s_mul_i32 s36, %[step], 46080\n\t"
      "s_mul_i32 s37, %[step], 110592\n\t"
      "LOOP%=:\n\t"
      "s_cmp_lg_u32 s40, 63\n\t"
      "s_cbranch_scc1 LQW%=\n\t"
      "s_cmp_lg_u32 %[w], 0\n\t"
      "s_cbranch_scc1 LQB%=\n\t"
      "s_mov_b32 s42, 0\n\t"
      "s_mov_b64 exec, 1\n\t"
      "LQL%=:\n\t"
      "s_sleep 32\n\t"
      "buffer_load_dword v96, off, %[rs], %[qw] sc1\n\t"
      "s_add_u32 s42, s42, 1\n\t"
      "s_cmp_gt_u32 s42, 50000\n\t"
      "s_cbranch_scc1 LFAIL%=\n\t"
      "s_waitcnt vmcnt(0)\n\t"
      "v_cmp_gt_i32 vcc, 61, v96\n\t"
      "s_cbranch_vccnz LQL%=\n\t"
      "s_mov_b64 exec, -1\n\t"
      "LQB%=:\n\t"
      "s_barrier\n\t"
      "LQW%=:\n\t"
      "v_mov_b32 v7, s40\n\t"
      "s_mov_b32 s42, 0\n\t"
      "v_mov_b32 v104, 0\n\t"
      "s_add_u32 s44, s52, s36\n\t"
      "s_xor_b32 s45, s54, 65536\n\t"
      "s_xor_b32 s46, s55, 65536\n\t"
      "s_add_u32 s47, s40, %[step]\n\t"
      "s_cmp_eq_u32 s41, 0\n\t"
      "s_cbranch_scc1 LWAIT%=\n\t"
      "s_waitcnt vmcnt(8)\n\t"
      "s_branch LA%=\n\t"
      "LWAIT%=:\n\t"
      "s_mov_b64 exec, 0x3ff\n\t"
      "buffer_load_dword v96, v1, %[rs], 0 offen sc1\n\t"
      "s_nop 11\n\t"
      "buffer_load_dword v97, v1, %[rs], 0 offen sc1\n\t"
      "s_nop 11\n\t"
      "buffer_load_dword v98, v1, %[rs], 0 offen sc1\n\t"
      "s_nop 11\n\t"
      "buffer_load_dword v99, v1, %[rs], 0 offen sc1\n\t"
      "s_nop 11\n\t"
      "LEW%=:\n\t"
      "s_waitcnt vmcnt(3)\n\t"
      "v_cmp_gt_i32 vcc, s40, v96\n\t"
      "s_cbranch_vccz LEWD%=\n\t"
      "buffer_load_dword v96, v1, %[rs], 0 offen sc1\n\t"
      "s_nop 11\n\t"
      "s_waitcnt vmcnt(3)\n\t"
      "v_cmp_gt_i32 vcc, s40, v97\n\t"
      "s_cbranch_vccz LEWD%=\n\t"
      "buffer_load_dword v97, v1, %[rs], 0 offen sc1\n\t"
      "s_nop 11\n\t"
      "s_waitcnt vmcnt(3)\n\t"
      "v_cmp_gt_i32 vcc, s40, v98\n\t"
      "s_cbranch_vccz LEWD%=\n\t"
      "buffer_load_dword v98, v1, %[rs], 0 offen sc1\n\t"
      "s_nop 11\n\t"
      "s_waitcnt vmcnt(3)\n\t"
      "v_cmp_gt_i32 vcc, s40, v99\n\t"
      "s_cbranch_vccz LEWD%=\n\t"
      "buffer_load_dword v99, v1, %[rs], 0 offen sc1\n\t"
      "s_nop 11\n\t"
      "s_add_u32 s42, s42, 1\n\t"
      "s_cmp_lt_u32 s42, 400000\n\t"
      "s_cbranch_scc1 LEW%=\n\t"
      "s_branch LFAIL%=\n\t"
      "LEWD%=:\n\t"
      "s_mov_b64 exec, -1\n\t"
      "s_mov_b32 s42, 0\n\t"
      "LPOLL%=:\n\t"
      "s_mov_b32 exec_hi, 0xff\n\t"
      "s_add_u32 m0, s55, 0\n\t"
      "s_add_u32 s43, s52, 0\n\t"
      "buffer_load_dwordx4 v2, %[rs], s43 offen sc1 lds\n\t"
      "s_mov_b32 exec_hi, -1\n\t"
      "s_add_u32 m0, s54, 0\n\t"
      "s_add_u32 s43, s52, 0\n\t"
      "buffer_load_dwordx4 v0, %[rs], s43 offen sc1 lds\n\t"
      "s_add_u32 m0, s54, 4096\n\t"
      "s_add_u32 s43, s52, 4096\n\t"
      "buffer_load_dwordx4 v0, %[rs], s43 offen sc1 lds\n\t"
      "s_add_u32 m0, s54, 8192\n\t"
      "s_add_u32 s43, s52, 8192\n\t"
      "buffer_load_dwordx4 v0, %[rs], s43 offen sc1 lds\n\t"
      "s_add_u32 m0, s54, 12288\n\t"
      "s_add_u32 s43, s52, 12288\n\t"
      "buffer_load_dwordx4 v0, %[rs], s43 offen sc1 lds\n\t"
      "s_add_u32 m0, s54, 16384\n\t"
      "s_add_u32 s43, s52, 16384\n\t"
      "buffer_load_dwordx4 v0, %[rs], s43 offen sc1 lds\n\t"
      "s_mov_b32 exec_hi, 0xff\n\t"
      "s_add_u32 m0, s55, 640\n\t"
      "s_add_u32 s43, s52, 2560\n\t"
      "buffer_load_dwordx4 v2, %[rs], s43 offen sc1 lds\n\t"
      "s_mov_b32 exec_hi, -1\n\t"
      "s_add_u32 m0, s54, 20480\n\t"
      "s_add_u32 s43, s52, 20480\n\t"
      "buffer_load_dwordx4 v0, %[rs], s43 offen sc1 lds\n\t"
      "s_add_u32 m0, s54, 24576\n\t"
      "s_add_u32 s43, s52, 24576\n\t"
      "buffer_load_dwordx4 v0, %[rs], s43 offen sc1 lds\n\t"
      "s_add_u32 m0, s54, 28672\n\t"
      "s_add_u32 s43, s52, 28672\n\t"
      "buffer_load_dwordx4 v0, %[rs], s43 offen sc1 lds\n\t"
      "s_add_u32 m0, s54, 32768\n\t"
      "s_add_u32 s43, s52, 32768\n\t"
      "buffer_load_dwordx4 v0, %[rs], s43 offen sc1 lds\n\t"
      "s_add_u32 m0, s54, 36864\n\t"
      "s_add_u32 s43, s52, 36864\n\t"
      "buffer_load_dwordx4 v0, %[rs], s43 offen sc1 lds\n\t"
      "v_mov_b32 v104, 0\n\t"
      "s_add_u32 s42, s42, 1\n\t"
      "s_cmp_gt_u32 s42, 400000\n\t"
      "s_cbranch_scc1 LFAIL%=\n\t"
      "s_waitcnt vmcnt(6)\n\t"
      "LA%=:\n\t"
      "s_barrier\n\t"
      "ds_read_b128 v[32:35], v3 offset:0\n\t"
      "ds_read_b128 v[36:39], v3 offset:1024\n\t"
      "ds_read_b128 v[40:43], v3 offset:2048\n\t"
      "ds_read_b128 v[44:47], v3 offset:3072\n\t"
      "ds_read_b64 v[56:57], v4 offset:0\n\t"
      "ds_read_b64 v[58:59], v4 offset:128\n\t"
      "ds_read_b64 v[60:61], v4 offset:256\n\t"
      "ds_read_b64 v[62:63], v4 offset:384\n\t"
      "ds_read_b64 v[64:65], v4 offset:512\n\t"
      "s_waitcnt lgkmcnt(8)\n\t"
      "v_mfma_f32_16x16x32_bf16 v[16:19], a[0:3], v[32:35], 0\n\t"
      "ds_read_b128 v[48:51], v3 offset:4096\n\t"
      "v_mfma_f32_16x16x32_bf16 v[20:23], a[4:7], v[32:35], 0\n\t"
      "s_waitcnt lgkmcnt(8)\n\t"
      "v_mfma_f32_16x16x32_bf16 v[16:19], a[8:11], v[36:39], v[16:19]\n\t"
      "ds_read_b128 v[52:55], v3 offset:5120\n\t"
      "v_mfma_f32_16x16x32_bf16 v[20:23], a[12:15], v[36:39], v[20:23]\n\t"
      "s_waitcnt lgkmcnt(8)\n\t"
      "v_mfma_f32_16x16x32_bf16 v[16:19], a[16:19], v[40:43], v[16:19]\n\t"
      "ds_read_b128 v[32:35], v3 offset:6144\n\t"
      "s_waitcnt lgkmcnt(7)\n\t"
      "v_max3_u32 v104, v104, v56, v57\n\t"
      "s_waitcnt lgkmcnt(6)\n\t"
      "v_max3_u32 v104, v104, v58, v59\n\t"
      "v_mfma_f32_16x16x32_bf16 v[20:23], a[20:23], v[40:43], v[20:23]\n\t"
      "s_waitcnt lgkmcnt(5)\n\t"
      "v_max3_u32 v104, v104, v60, v61\n\t"
      "s_waitcnt lgkmcnt(4)\n\t"
      "v_max3_u32 v104, v104, v62, v63\n\t"
      "v_mfma_f32_16x16x32_bf16 v[16:19], a[24:27], v[44:47], v[16:19]\n\t"
      "ds_read_b128 v[36:39], v3 offset:7168\n\t"
      "s_waitcnt lgkmcnt(4)\n\t"
      "v_max3_u32 v104, v104, v64, v65\n\t"
      "v_add_f32 v56, v56, v58\n\t"
      "v_mfma_f32_16x16x32_bf16 v[20:23], a[28:31], v[44:47], v[20:23]\n\t"
      "s_waitcnt lgkmcnt(3)\n\t"
      "v_add_f32 v57, v57, v59\n\t"
      "v_add_f32 v60, v60, v62\n\t"
      "v_mfma_f32_16x16x32_bf16 v[16:19], a[32:35], v[48:51], v[16:19]\n\t"
      "ds_read_b128 v[40:43], v3 offset:8192\n\t"
      "v_add_f32 v61, v61, v63\n\t"
      "v_add_f32 v56, v56, v64\n\t"
      "v_mfma_f32_16x16x32_bf16 v[20:23], a[36:39], v[48:51], v[20:23]\n\t"
      "s_waitcnt lgkmcnt(3)\n\t"
      "v_add_f32 v57, v57, v65\n\t"
      "v_add_f32 v56, v56, v60\n\t"
      "v_mfma_f32_16x16x32_bf16 v[16:19], a[40:43], v[52:55], v[16:19]\n\t"
      "ds_read_b128 v[44:47], v3 offset:9216\n\t"
      "v_add_f32 v57, v57, v61\n\t"
      "v_mov_b32 v58, v56\n\t"
      "v_mfma_f32_16x16x32_bf16 v[20:23], a[44:47], v[52:55], v[20:23]\n\t"
      "s_waitcnt lgkmcnt(3)\n\t"
      "v_mov_b32 v59, v57\n\t"
      "s_nop 0\n\t"
      "v_mfma_f32_16x16x32_bf16 v[16:19], a[48:51], v[32:35], v[16:19]\n\t"
      "ds_read_b128 v[48:51], v3 offset:10240\n\t"
      "v_permlane16_swap_b32 v56, v58\n\t"
      "v_mfma_f32_16x16x32_bf16 v[20:23], a[52:55], v[32:35], v[20:23]\n\t"
      "s_waitcnt lgkmcnt(3)\n\t"
      "v_permlane16_swap_b32 v57, v59\n\t"
      "v_mfma_f32_16x16x32_bf16 v[16:19], a[56:59], v[36:39], v[16:19]\n\t"
      "ds_read_b128 v[52:55], v3 offset:11264\n\t"
      "v_add_f32 v56, v56, v58\n\t"
      "v_add_f32 v57, v57, v59\n\t"
      "v_mfma_f32_16x16x32_bf16 v[20:23], a[60:63], v[36:39], v[20:23]\n\t"
      "s_waitcnt lgkmcnt(3)\n\t"
      "v_mov_b32 v58, v56\n\t"
      "v_mov_b32 v59, v57\n\t"
      "v_mfma_f32_16x16x32_bf16 v[16:19], a[64:67], v[40:43], v[16:19]\n\t"
      "ds_read_b128 v[32:35], v3 offset:12288\n\t"
      "s_nop 0\n\t"
      "v_mfma_f32_16x16x32_bf16 v[20:23], a[68:71], v[40:43], v[20:23]\n\t"
      "s_waitcnt lgkmcnt(3)\n\t"
      "s_mov_b64 exec, 0x3ff\n\t"
      "buffer_load_dword v96, v1, %[rs], 0 offen sc1\n\t"
      "s_mov_b64 exec, -1\n\t"
      "v_mfma_f32_16x16x32_bf16 v[16:19], a[72:75], v[44:47], v[16:19]\n\t"
      "ds_read_b128 v[36:39], v3 offset:13312\n\t"
      "v_permlane32_swap_b32 v56, v58\n\t"
      "v_mfma_f32_16x16x32_bf16 v[20:23], a[76:79], v[44:47], v[20:23]\n\t"
      "s_waitcnt lgkmcnt(3)\n\t"
      "v_permlane32_swap_b32 v57, v59\n\t"
      "v_mfma_f32_16x16x32_bf16 v[16:19], a[80:83], v[48:51], v[16:19]\n\t"
      "ds_read_b128 v[40:43], v3 offset:14336\n\t"
      "v_add_f32 v56, v56, v58\n\t"
      "v_add_f32 v57, v57, v59\n\t"
      "v_mfma_f32_16x16x32_bf16 v[20:23], a[84:87], v[48:51], v[20:23]\n\t"
      "s_waitcnt lgkmcnt(3)\n\t"
      "v_mul_f32 v56, s57, v56\n\t"
      "v_mul_f32 v57, s57, v57\n\t"
      "v_mfma_f32_16x16x32_bf16 v[16:19], a[88:91], v[52:55], v[16:19]\n\t"
      "ds_read_b128 v[44:47], v3 offset:15360\n\t"
      "v_fma_f32 v57, -v56, v56, v57\n\t"
      "v_max_f32 v57, 0, v57\n\t"
      "v_mfma_f32_16x16x32_bf16 v[20:23], a[92:95], v[52:55], v[20:23]\n\t"
      "s_waitcnt lgkmcnt(3)\n\t"
      "v_add_f32 v57, 0x3727c5ac, v57\n\t"
      "v_mfma_f32_16x16x32_bf16 v[16:19], a[96:99], v[32:35], v[16:19]\n\t"
      "ds_read_b128 v[48:51], v3 offset:16384\n\t"
      "v_rsq_f32 v66, v57\n\t"
      "v_mfma_f32_16x16x32_bf16 v[20:23], a[100:103], v[32:35], v[20:23]\n\t"
      "s_waitcnt lgkmcnt(3)\n\t"
      "s_nop 0\n\t"
      "v_mul_f32 v56, v56, v66\n\t"
      "v_mfma_f32_16x16x32_bf16 v[16:19], a[104:107], v[36:39], v[16:19]\n\t"
      "ds_read_b128 v[52:55], v3 offset:17408\n\t"
      "v_mul_f32 v68, v8, v56\n\t"
      "v_mul_f32 v69, v9, v56\n\t"
      "v_mfma_f32_16x16x32_bf16 v[20:23], a[108:111], v[36:39], v[20:23]\n\t"
      "s_waitcnt lgkmcnt(3)\n\t"
      "v_mul_f32 v70, v10, v56\n\t"
      "v_mul_f32 v71, v11, v56\n\t"
      "v_mfma_f32_16x16x32_bf16 v[16:19], a[112:115], v[40:43], v[16:19]\n\t"
      "ds_read_b128 v[32:35], v3 offset:18432\n\t"
      "v_mul_f32 v72, v12, v56\n\t"
      "v_mul_f32 v73, v13, v56\n\t"
      "v_mfma_f32_16x16x32_bf16 v[20:23], a[116:119], v[40:43], v[20:23]\n\t"
      "s_waitcnt lgkmcnt(3)\n\t"
      "v_mul_f32 v74, v14, v56\n\t"
      "v_mul_f32 v75, v15, v56\n\t"
      "v_mfma_f32_16x16x32_bf16 v[16:19], a[120:123], v[44:47], v[16:19]\n\t"
      "ds_read_b128 v[36:39], v3 offset:19456\n\t"
      "v_mfma_f32_16x16x32_bf16 v[20:23], a[124:127], v[44:47], v[20:23]\n\t"
      "s_waitcnt lgkmcnt(3)\n\t"
      "s_waitcnt vmcnt(1)\n\t"
      "s_barrier\n\t"
      "v_mfma_f32_16x16x32_bf16 v[16:19], a[128:131], v[48:51], v[16:19]\n\t"
      "ds_read_b128 v[40:43], v3 offset:20480\n\t"
      "v_mfma_f32_16x16x32_bf16 v[20:23], a[132:135], v[48:51], v[20:23]\n\t"
      "s_waitcnt lgkmcnt(3)\n\t"
      "ds_read_b64 v[56:57], v4 offset:640\n\t"
      "ds_read_b64 v[58:59], v4 offset:768\n\t"
      "v_mfma_f32_16x16x32_bf16 v[16:19], a[136:139], v[52:55], v[16:19]\n\t"
      "ds_read_b128 v[44:47], v3 offset:21504\n\t"
      "v_mfma_f32_16x16x32_bf16 v[20:23], a[140:143], v[52:55], v[20:23]\n\t"
      "s_waitcnt lgkmcnt(5)\n\t"
      "ds_read_b64 v[60:61], v4 offset:896\n\t"
      "ds_read_b64 v[62:63], v4 offset:1024\n\t"
      "ds_read_b64 v[64:65], v4 offset:1152\n\t"
      "v_mfma_f32_16x16x32_bf16 v[16:19], a[144:147], v[32:35], v[16:19]\n\t"
      "ds_read_b128 v[48:51], v3 offset:22528\n\t"
      "v_mfma_f32_16x16x32_bf16 v[20:23], a[148:151], v[32:35], v[20:23]\n\t"
      "s_waitcnt lgkmcnt(8)\n\t"
      "v_mfma_f32_16x16x32_bf16 v[16:19], a[152:155], v[36:39], v[16:19]\n\t"
      "ds_read_b128 v[52:55], v3 offset:23552\n\t"
      "s_waitcnt lgkmcnt(7)\n\t"
      "v_max3_u32 v104, v104, v56, v57\n\t"
      "s_waitcnt lgkmcnt(6)\n\t"
      "v_max3_u32 v104, v104, v58, v59\n\t"
      "v_mfma_f32_16x16x32_bf16 v[20:23], a[156:159], v[36:39], v[20:23]\n\t"
      "s_waitcnt lgkmcnt(4)\n\t"
      "v_max3_u32 v104, v104, v60, v61\n\t"
      "s_waitcnt lgkmcnt(3)\n\t"
      "v_max3_u32 v104, v104, v62, v63\n\t"
      "v_mfma_f32_16x16x32_bf16 v[24:27], a[0:3], v[40:43], 0\n\t"
      "ds_read_b128 v[32:35], v3 offset:24576\n\t"
      "s_waitcnt lgkmcnt(3)\n\t"
      "v_max3_u32 v104, v104, v64, v65\n\t"
      "v_add_f32 v56, v56, v58\n\t"
      "v_mfma_f32_16x16x32_bf16 v[28:31], a[4:7], v[40:43], 0\n\t"
      "v_add_f32 v57, v57, v59\n\t"
      "v_add_f32 v60, v60, v62\n\t"
      "v_mfma_f32_16x16x32_bf16 v[24:27], a[8:11], v[44:47], v[24:27]\n\t"
      "ds_read_b128 v[36:39], v3 offset:25600\n\t"
      "v_add_f32 v61, v61, v63\n\t"
      "v_add_f32 v56, v56, v64\n\t"
      "v_mfma_f32_16x16x32_bf16 v[28:31], a[12:15], v[44:47], v[28:31]\n\t"
      "s_waitcnt lgkmcnt(3)\n\t"
      "v_add_f32 v57, v57, v65\n\t"
      "v_add_f32 v56, v56, v60\n\t"
      "v_mfma_f32_16x16x32_bf16 v[24:27], a[16:19], v[48:51], v[24:27]\n\t"
      "ds_read_b128 v[40:43], v3 offset:26624\n\t"
      "v_add_f32 v57, v57, v61\n\t"
      "v_mov_b32 v58, v56\n\t"
      "v_mfma_f32_16x16x32_bf16 v[28:31], a[20:23], v[48:51], v[28:31]\n\t"
      "s_waitcnt lgkmcnt(3)\n\t"
      "v_mov_b32 v59, v57\n\t"
      "s_nop 0\n\t"
      "v_mfma_f32_16x16x32_bf16 v[24:27], a[24:27], v[52:55], v[24:27]\n\t"
      "ds_read_b128 v[44:47], v3 offset:27648\n\t"
      "s_waitcnt vmcnt(0)\n\t"
      "s_mov_b64 exec, 0x3ff\n\t"
      "v_cmp_gt_i32 vcc, s47, v96\n\t"
      "s_mov_b64 exec, -1\n\t"
      "v_mfma_f32_16x16x32_bf16 v[28:31], a[28:31], v[52:55], v[28:31]\n\t"
      "s_waitcnt lgkmcnt(3)\n\t"
      "v_permlane16_swap_b32 v56, v58\n\t"
      "s_cbranch_vccz LPRE%=\n\t"
      "s_mov_b32 s41, 0\n\t"
      "v_mfma_f32_16x16x32_bf16 v[24:27], a[32:35], v[32:35], v[24:27]\n\t"
      "ds_read_b128 v[48:51], v3 offset:28672\n\t"
      "v_permlane16_swap_b32 v57, v59\n\t"
      "v_mfma_f32_16x16x32_bf16 v[28:31], a[36:39], v[32:35], v[28:31]\n\t"
      "s_waitcnt lgkmcnt(3)\n\t"
      "v_add_f32 v56, v56, v58\n\t"
      "v_add_f32 v57, v57, v59\n\t"
      "v_mfma_f32_16x16x32_bf16 v[24:27], a[40:43], v[36:39], v[24:27]\n\t"
      "ds_read_b128 v[52:55], v3 offset:29696\n\t"
      "v_mov_b32 v58, v56\n\t"
      "v_mov_b32 v59, v57\n\t"
      "v_mfma_f32_16x16x32_bf16 v[28:31], a[44:47], v[36:39], v[28:31]\n\t"
      "s_waitcnt lgkmcnt(3)\n\t"
      "s_nop 0\n\t"
      "v_mfma_f32_16x16x32_bf16 v[24:27], a[48:51], v[40:43], v[24:27]\n\t"
      "ds_read_b128 v[32:35], v3 offset:30720\n\t"
      "v_permlane32_swap_b32 v56, v58\n\t"
      "v_mfma_f32_16x16x32_bf16 v[28:31], a[52:55], v[40:43], v[28:31]\n\t"
      "s_waitcnt lgkmcnt(3)\n\t"
      "v_permlane32_swap_b32 v57, v59\n\t"
      "v_mfma_f32_16x16x32_bf16 v[24:27], a[56:59], v[44:47], v[24:27]\n\t"
      "ds_read_b128 v[36:39], v3 offset:31744\n\t"
      "v_add_f32 v56, v56, v58\n\t"
      "v_add_f32 v57, v57, v59\n\t"
      "v_mfma_f32_16x16x32_bf16 v[28:31], a[60:63], v[44:47], v[28:31]\n\t"
      "s_waitcnt lgkmcnt(3)\n\t"
      "v_mul_f32 v56, s57, v56\n\t"
      "v_mul_f32 v57, s57, v57\n\t"
      "v_mfma_f32_16x16x32_bf16 v[24:27], a[64:67], v[48:51], v[24:27]\n\t"
      "ds_read_b128 v[40:43], v3 offset:32768\n\t"
      "v_fma_f32 v57, -v56, v56, v57\n\t"
      "v_max_f32 v57, 0, v57\n\t"
      "v_mfma_f32_16x16x32_bf16 v[28:31], a[68:71], v[48:51], v[28:31]\n\t"
      "s_waitcnt lgkmcnt(3)\n\t"
      "v_add_f32 v57, 0x3727c5ac, v57\n\t"
      "v_mfma_f32_16x16x32_bf16 v[24:27], a[72:75], v[52:55], v[24:27]\n\t"
      "ds_read_b128 v[44:47], v3 offset:33792\n\t"
      "v_rsq_f32 v67, v57\n\t"
      "v_mfma_f32_16x16x32_bf16 v[28:31], a[76:79], v[52:55], v[28:31]\n\t"
      "s_waitcnt lgkmcnt(3)\n\t"
      "s_nop 0\n\t"
      "v_mul_f32 v56, v56, v67\n\t"
      "v_mfma_f32_16x16x32_bf16 v[24:27], a[80:83], v[32:35], v[24:27]\n\t"
      "ds_read_b128 v[48:51], v3 offset:34816\n\t"
      "v_mul_f32 v76, v8, v56\n\t"
      "v_mul_f32 v77, v9, v56\n\t"
      "v_mfma_f32_16x16x32_bf16 v[28:31], a[84:87], v[32:35], v[28:31]\n\t"
      "s_waitcnt lgkmcnt(3)\n\t"
      "v_mul_f32 v78, v10, v56\n\t"
      "v_mul_f32 v79, v11, v56\n\t"
      "v_mfma_f32_16x16x32_bf16 v[24:27], a[88:91], v[36:39], v[24:27]\n\t"
      "ds_read_b128 v[52:55], v3 offset:35840\n\t"
      "v_mul_f32 v80, v12, v56\n\t"
      "v_mul_f32 v81, v13, v56\n\t"
      "v_mfma_f32_16x16x32_bf16 v[28:31], a[92:95], v[36:39], v[28:31]\n\t"
      "s_waitcnt lgkmcnt(3)\n\t"
      "v_mul_f32 v82, v14, v56\n\t"
      "v_mul_f32 v83, v15, v56\n\t"
      "v_mfma_f32_16x16x32_bf16 v[24:27], a[96:99], v[40:43], v[24:27]\n\t"
      "ds_read_b128 v[32:35], v3 offset:36864\n\t"
      "v_fma_f32 v16, v16, v66, -v68\n\t"
      "v_fma_f32 v17, v17, v66, -v69\n\t"
      "v_mfma_f32_16x16x32_bf16 v[28:31], a[100:103], v[40:43], v[28:31]\n\t"
      "s_waitcnt lgkmcnt(3)\n\t"
      "v_fma_f32 v18, v18, v66, -v70\n\t"
      "v_fma_f32 v19, v19, v66, -v71\n\t"
      "v_mfma_f32_16x16x32_bf16 v[24:27], a[104:107], v[44:47], v[24:27]\n\t"
      "ds_read_b128 v[36:39], v3 offset:37888\n\t"
      "v_fma_f32 v20, v20, v66, -v72\n\t"
      "v_fma_f32 v21, v21, v66, -v73\n\t"
      "v_mfma_f32_16x16x32_bf16 v[28:31], a[108:111], v[44:47], v[28:31]\n\t"
      "s_waitcnt lgkmcnt(3)\n\t"
      "v_fma_f32 v22, v22, v66, -v74\n\t"
      "v_fma_f32 v23, v23, v66, -v75\n\t"
      "v_mfma_f32_16x16x32_bf16 v[24:27], a[112:115], v[48:51], v[24:27]\n\t"
      "ds_read_b128 v[40:43], v3 offset:38912\n\t"
      "v_mul_f32 v88, s58, v16\n\t"
      "v_mul_f32 v89, s58, v17\n\t"
      "v_mfma_f32_16x16x32_bf16 v[28:31], a[116:119], v[48:51], v[28:31]\n\t"
      "s_waitcnt lgkmcnt(3)\n\t"
      "v_mul_f32 v90, s58, v18\n\t"
      "v_mul_f32 v91, s58, v19\n\t"
      "v_mfma_f32_16x16x32_bf16 v[24:27], a[120:123], v[52:55], v[24:27]\n\t"
      "ds_read_b128 v[44:47], v3 offset:39936\n\t"
      "v_exp_f32 v88, v88\n\t"
      "v_mfma_f32_16x16x32_bf16 v[28:31], a[124:127], v[52:55], v[28:31]\n\t"
      "s_waitcnt lgkmcnt(3)\n\t"
      "v_exp_f32 v89, v89\n\t"
      "v_mfma_f32_16x16x32_bf16 v[24:27], a[128:131], v[32:35], v[24:27]\n\t"
      "v_exp_f32 v90, v90\n\t"
      "v_mfma_f32_16x16x32_bf16 v[28:31], a[132:135], v[32:35], v[28:31]\n\t"
      "s_waitcnt lgkmcnt(2)\n\t"
      "v_exp_f32 v91, v91\n\t"
      "v_mfma_f32_16x16x32_bf16 v[24:27], a[136:139], v[36:39], v[24:27]\n\t"
      "v_mul_f32 v16, v16, v20\n\t"
      "v_mul_f32 v17, v17, v21\n\t"
      "v_mfma_f32_16x16x32_bf16 v[28:31], a[140:143], v[36:39], v[28:31]\n\t"
      "s_waitcnt lgkmcnt(1)\n\t"
      "v_mul_f32 v18, v18, v22\n\t"
      "v_mul_f32 v19, v19, v23\n\t"
      "v_mfma_f32_16x16x32_bf16 v[24:27], a[144:147], v[40:43], v[24:27]\n\t"
      "v_add_f32 v88, 1.0, v88\n\t"
      "v_add_f32 v89, 1.0, v89\n\t"
      "v_mfma_f32_16x16x32_bf16 v[28:31], a[148:151], v[40:43], v[28:31]\n\t"
      "s_waitcnt lgkmcnt(0)\n\t"
      "v_add_f32 v90, 1.0, v90\n\t"
      "v_add_f32 v91, 1.0, v91\n\t"
      "v_mfma_f32_16x16x32_bf16 v[24:27], a[152:155], v[44:47], v[24:27]\n\t"
      "v_rcp_f32 v88, v88\n\t"
      "v_mfma_f32_16x16x32_bf16 v[28:31], a[156:159], v[44:47], v[28:31]\n\t"
      "v_rcp_f32 v89, v89\n\t"
      "v_rcp_f32 v90, v90\n\t"
      "v_rcp_f32 v91, v91\n\t"
      "s_nop 0\n\t"
      "v_mul_f32 v16, v16, v88\n\t"
      "v_mul_f32 v17, v17, v89\n\t"
      "v_mul_f32 v18, v18, v90\n\t"
      "v_mul_f32 v19, v19, v91\n\t"
      "v_cvt_pk_bf16_f32 v84, v16, v17\n\t"
      "v_cvt_pk_bf16_f32 v85, v18, v19\n\t"
      "LJOIN%=:\n\t"
      "v_cmp_u_f32 vcc, v20, v28\n\t"
      "s_cbranch_vccnz LNAN%=\n\t"
      "LCHK%=:\n\t"
      "v_cmp_eq_u32 vcc, -1, v104\n\t"
      "s_cbranch_vccnz LRETRY%=\n\t"
      "buffer_store_dword v7, v6, %[rs], %[ewo] offen sc1\n\t"
      "v_fma_f32 v24, v24, v67, -v76\n\t"
      "v_fma_f32 v25, v25, v67, -v77\n\t"
      "v_fma_f32 v26, v26, v67, -v78\n\t"
      "v_fma_f32 v27, v27, v67, -v79\n\t"
      "v_fma_f32 v28, v28, v67, -v80\n\t"
      "v_fma_f32 v29, v29, v67, -v81\n\t"
      "v_fma_f32 v30, v30, v67, -v82\n\t"
      "v_fma_f32 v31, v31, v67, -v83\n\t"
      "v_mul_f32 v88, s58, v24\n\t"
      "v_mul_f32 v89, s58, v25\n\t"
      "v_mul_f32 v90, s58, v26\n\t"
      "v_mul_f32 v91, s58, v27\n\t"
      "v_exp_f32 v88, v88\n\t"
      "v_exp_f32 v89, v89\n\t"
      "v_exp_f32 v90, v90\n\t"
      "v_exp_f32 v91, v91\n\t"
      "v_mul_f32 v24, v24, v28\n\t"
      "v_mul_f32 v25, v25, v29\n\t"
      "v_mul_f32 v26, v26, v30\n\t"
      "v_mul_f32 v27, v27, v31\n\t"
      "v_add_f32 v88, 1.0, v88\n\t"
      "v_add_f32 v89, 1.0, v89\n\t"
      "v_add_f32 v90, 1.0, v90\n\t"
      "v_add_f32 v91, 1.0, v91\n\t"
      "v_rcp_f32 v88, v88\n\t"
      "v_rcp_f32 v89, v89\n\t"
      "v_rcp_f32 v90, v90\n\t"
      "v_rcp_f32 v91, v91\n\t"
      "s_nop 0\n\t"
      "v_mul_f32 v24, v24, v88\n\t"
      "v_mul_f32 v25, v25, v89\n\t"
      "v_mul_f32 v26, v26, v90\n\t"
      "v_mul_f32 v27, v27, v91\n\t"
      "v_cvt_pk_bf16_f32 v86, v24, v25\n\t"
      "v_cvt_pk_bf16_f32 v87, v26, v27\n\t"
      "s_nop 1\n\t"
      "v_permlane16_swap_b32 v84, v86\n\t"
      "v_permlane16_swap_b32 v85, v87\n\t"
      "buffer_store_dwordx4 v[84:87], v5, %[rs], s53 offen sc1\n\t"
      "v_xor_b32 v3, 65536, v3\n\t"
      "v_xor_b32 v4, 65536, v4\n\t"
      "s_xor_b32 s54, s54, 65536\n\t"
      "s_xor_b32 s55, s55, 65536\n\t"
      "v_and_b32 v5, 0x7fffffff, v5\n\t"
      "s_add_u32 s52, s52, s36\n\t"
      "s_add_u32 s53, s53, s37\n\t"
      "s_add_u32 s40, s40, %[step]\n\t"
      "s_cmp_lt_u32 s40, %[end]\n\t"
      "s_cbranch_scc1 LOOP%=\n\t"
      "s_branch LEND%=\n\t"
      "LRETRY%=:\n\t"
      "s_barrier\n\t"
      "s_branch LPOLL%=\n\t"
      "LPRE%=:\n\t"
      "s_mov_b32 s41, 1\n\t"
      "v_mfma_f32_16x16x32_bf16 v[24:27], a[32:35], v[32:35], v[24:27]\n\t"
      "ds_read_b128 v[48:51], v3 offset:28672\n\t"
      "s_mov_b32 exec_hi, 0xff\n\t"
      "s_add_u32 m0, s46, 0\n\t"
      "s_add_u32 s43, s44, 0\n\t"
      "buffer_load_dwordx4 v2, %[rs], s43 offen sc1 lds\n\t"
      "s_mov_b32 exec_hi, -1\n\t"
      "v_mfma_f32_16x16x32_bf16 v[28:31], a[36:39], v[32:35], v[28:31]\n\t"
      "s_waitcnt lgkmcnt(3)\n\t"
      "v_permlane16_swap_b32 v57, v59\n\t"
      "v_mfma_f32_16x16x32_bf16 v[24:27], a[40:43], v[36:39], v[24:27]\n\t"
      "ds_read_b128 v[52:55], v3 offset:29696\n\t"
      "s_add_u32 m0, s45, 0\n\t"
      "s_add_u32 s43, s44, 0\n\t"
      "buffer_load_dwordx4 v0, %[rs], s43 offen sc1 lds\n\t"
      "v_mfma_f32_16x16x32_bf16 v[28:31], a[44:47], v[36:39], v[28:31]\n\t"
      "s_waitcnt lgkmcnt(3)\n\t"
      "v_add_f32 v56, v56, v58\n\t"
      "v_add_f32 v57, v57, v59\n\t"
      "v_mfma_f32_16x16x32_bf16 v[24:27], a[48:51], v[40:43], v[24:27]\n\t"
      "ds_read_b128 v[32:35], v3 offset:30720\n\t"
      "s_add_u32 m0, s45, 4096\n\t"
      "s_add_u32 s43, s44, 4096\n\t"
      "buffer_load_dwordx4 v0, %[rs], s43 offen sc1 lds\n\t"
      "v_mfma_f32_16x16x32_bf16 v[28:31], a[52:55], v[40:43], v[28:31]\n\t"
      "s_waitcnt lgkmcnt(3)\n\t"
      "v_mov_b32 v58, v56\n\t"
      "v_mov_b32 v59, v57\n\t"
      "v_mfma_f32_16x16x32_bf16 v[24:27], a[56:59], v[44:47], v[24:27]\n\t"
      "ds_read_b128 v[36:39], v3 offset:31744\n\t"
      "s_add_u32 m0, s45, 8192\n\t"
      "s_add_u32 s43, s44, 8192\n\t"
      "buffer_load_dwordx4 v0, %[rs], s43 offen sc1 lds\n\t"
      "v_mfma_f32_16x16x32_bf16 v[28:31], a[60:63], v[44:47], v[28:31]\n\t"
      "s_waitcnt lgkmcnt(3)\n\t"
      "s_nop 0\n\t"
      "v_mfma_f32_16x16x32_bf16 v[24:27], a[64:67], v[48:51], v[24:27]\n\t"
      "ds_read_b128 v[40:43], v3 offset:32768\n\t"
      "s_add_u32 m0, s45, 12288\n\t"
      "s_add_u32 s43, s44, 12288\n\t"
      "buffer_load_dwordx4 v0, %[rs], s43 offen sc1 lds\n\t"
      "v_mfma_f32_16x16x32_bf16 v[28:31], a[68:71], v[48:51], v[28:31]\n\t"
      "s_waitcnt lgkmcnt(3)\n\t"
      "v_permlane32_swap_b32 v56, v58\n\t"
      "v_mfma_f32_16x16x32_bf16 v[24:27], a[72:75], v[52:55], v[24:27]\n\t"
      "ds_read_b128 v[44:47], v3 offset:33792\n\t"
      "s_add_u32 m0, s45, 16384\n\t"
      "s_add_u32 s43, s44, 16384\n\t"
      "buffer_load_dwordx4 v0, %[rs], s43 offen sc1 lds\n\t"
      "v_mfma_f32_16x16x32_bf16 v[28:31], a[76:79], v[52:55], v[28:31]\n\t"
      "s_waitcnt lgkmcnt(3)\n\t"
      "v_permlane32_swap_b32 v57, v59\n\t"
      "v_mfma_f32_16x16x32_bf16 v[24:27], a[80:83], v[32:35], v[24:27]\n\t"
      "ds_read_b128 v[48:51], v3 offset:34816\n\t"
      "s_mov_b32 exec_hi, 0xff\n\t"
      "s_add_u32 m0, s46, 640\n\t"
      "s_add_u32 s43, s44, 2560\n\t"
      "buffer_load_dwordx4 v2, %[rs], s43 offen sc1 lds\n\t"
      "s_mov_b32 exec_hi, -1\n\t"
      "v_mfma_f32_16x16x32_bf16 v[28:31], a[84:87], v[32:35], v[28:31]\n\t"
      "s_waitcnt lgkmcnt(3)\n\t"
      "v_add_f32 v56, v56, v58\n\t"
      "v_add_f32 v57, v57, v59\n\t"
      "v_mfma_f32_16x16x32_bf16 v[24:27], a[88:91], v[36:39], v[24:27]\n\t"
      "ds_read_b128 v[52:55], v3 offset:35840\n\t"
      "s_add_u32 m0, s45, 20480\n\t"
      "s_add_u32 s43, s44, 20480\n\t"
      "buffer_load_dwordx4 v0, %[rs], s43 offen sc1 lds\n\t"
      "v_mfma_f32_16x16x32_bf16 v[28:31], a[92:95], v[36:39], v[28:31]\n\t"
      "s_waitcnt lgkmcnt(3)\n\t"
      "v_mul_f32 v56, s57, v56\n\t"
      "v_mul_f32 v57, s57, v57\n\t"
      "v_mfma_f32_16x16x32_bf16 v[24:27], a[96:99], v[40:43], v[24:27]\n\t"
      "ds_read_b128 v[32:35], v3 offset:36864\n\t"
      "s_add_u32 m0, s45, 24576\n\t"
      "s_add_u32 s43, s44, 24576\n\t"
      "buffer_load_dwordx4 v0, %[rs], s43 offen sc1 lds\n\t"
      "v_mfma_f32_16x16x32_bf16 v[28:31], a[100:103], v[40:43], v[28:31]\n\t"
      "s_waitcnt lgkmcnt(3)\n\t"
      "v_fma_f32 v57, -v56, v56, v57\n\t"
      "v_max_f32 v57, 0, v57\n\t"
      "v_mfma_f32_16x16x32_bf16 v[24:27], a[104:107], v[44:47], v[24:27]\n\t"
      "ds_read_b128 v[36:39], v3 offset:37888\n\t"
      "s_add_u32 m0, s45, 28672\n\t"
      "s_add_u32 s43, s44, 28672\n\t"
      "buffer_load_dwordx4 v0, %[rs], s43 offen sc1 lds\n\t"
      "v_mfma_f32_16x16x32_bf16 v[28:31], a[108:111], v[44:47], v[28:31]\n\t"
      "s_waitcnt lgkmcnt(3)\n\t"
      "v_add_f32 v57, 0x3727c5ac, v57\n\t"
      "v_mfma_f32_16x16x32_bf16 v[24:27], a[112:115], v[48:51], v[24:27]\n\t"
      "ds_read_b128 v[40:43], v3 offset:38912\n\t"
      "s_add_u32 m0, s45, 32768\n\t"
      "s_add_u32 s43, s44, 32768\n\t"
      "buffer_load_dwordx4 v0, %[rs], s43 offen sc1 lds\n\t"
      "v_mfma_f32_16x16x32_bf16 v[28:31], a[116:119], v[48:51], v[28:31]\n\t"
      "s_waitcnt lgkmcnt(3)\n\t"
      "v_rsq_f32 v67, v57\n\t"
      "v_mfma_f32_16x16x32_bf16 v[24:27], a[120:123], v[52:55], v[24:27]\n\t"
      "ds_read_b128 v[44:47], v3 offset:39936\n\t"
      "s_add_u32 m0, s45, 36864\n\t"
      "s_add_u32 s43, s44, 36864\n\t"
      "buffer_load_dwordx4 v0, %[rs], s43 offen sc1 lds\n\t"
      "v_mfma_f32_16x16x32_bf16 v[28:31], a[124:127], v[52:55], v[28:31]\n\t"
      "s_waitcnt lgkmcnt(3)\n\t"
      "s_nop 0\n\t"
      "v_mul_f32 v56, v56, v67\n\t"
      "v_mfma_f32_16x16x32_bf16 v[24:27], a[128:131], v[32:35], v[24:27]\n\t"
      "v_mul_f32 v76, v8, v56\n\t"
      "v_mul_f32 v77, v9, v56\n\t"
      "v_mfma_f32_16x16x32_bf16 v[28:31], a[132:135], v[32:35], v[28:31]\n\t"
      "s_waitcnt lgkmcnt(2)\n\t"
      "v_mul_f32 v78, v10, v56\n\t"
      "v_mul_f32 v79, v11, v56\n\t"
      "v_mfma_f32_16x16x32_bf16 v[24:27], a[136:139], v[36:39], v[24:27]\n\t"
      "v_mul_f32 v80, v12, v56\n\t"
      "v_mul_f32 v81, v13, v56\n\t"
      "v_mfma_f32_16x16x32_bf16 v[28:31], a[140:143], v[36:39], v[28:31]\n\t"
      "s_waitcnt lgkmcnt(1)\n\t"
      "v_mul_f32 v82, v14, v56\n\t"
      "v_mul_f32 v83, v15, v56\n\t"
      "v_mfma_f32_16x16x32_bf16 v[24:27], a[144:147], v[40:43], v[24:27]\n\t"
      "v_fma_f32 v16, v16, v66, -v68\n\t"
      "v_fma_f32 v17, v17, v66, -v69\n\t"
      "v_mfma_f32_16x16x32_bf16 v[28:31], a[148:151], v[40:43], v[28:31]\n\t"
      "s_waitcnt lgkmcnt(0)\n\t"
      "v_fma_f32 v18, v18, v66, -v70\n\t"
      "v_fma_f32 v19, v19, v66, -v71\n\t"
      "v_mfma_f32_16x16x32_bf16 v[24:27], a[152:155], v[44:47], v[24:27]\n\t"
      "v_fma_f32 v20, v20, v66, -v72\n\t"
      "v_fma_f32 v21, v21, v66, -v73\n\t"
      "v_mfma_f32_16x16x32_bf16 v[28:31], a[156:159], v[44:47], v[28:31]\n\t"
      "v_fma_f32 v22, v22, v66, -v74\n\t"
      "v_fma_f32 v23, v23, v66, -v75\n\t"
      "v_mul_f32 v88, s58, v16\n\t"
      "v_mul_f32 v89, s58, v17\n\t"
      "v_mul_f32 v90, s58, v18\n\t"
      "v_mul_f32 v91, s58, v19\n\t"
      "v_exp_f32 v88, v88\n\t"
      "v_exp_f32 v89, v89\n\t"
      "v_exp_f32 v90, v90\n\t"
      "v_exp_f32 v91, v91\n\t"
      "v_mul_f32 v16, v16, v20\n\t"
      "v_mul_f32 v17, v17, v21\n\t"
      "v_mul_f32 v18, v18, v22\n\t"
      "v_mul_f32 v19, v19, v23\n\t"
      "v_add_f32 v88, 1.0, v88\n\t"
      "v_add_f32 v89, 1.0, v89\n\t"
      "v_add_f32 v90, 1.0, v90\n\t"
      "v_add_f32 v91, 1.0, v91\n\t"
      "v_rcp_f32 v88, v88\n\t"
      "v_rcp_f32 v89, v89\n\t"
      "v_rcp_f32 v90, v90\n\t"
      "v_rcp_f32 v91, v91\n\t"
      "s_nop 0\n\t"
      "v_mul_f32 v16, v16, v88\n\t"
      "v_mul_f32 v17, v17, v89\n\t"
      "v_mul_f32 v18, v18, v90\n\t"
      "v_mul_f32 v19, v19, v91\n\t"
      "v_cvt_pk_bf16_f32 v84, v16, v17\n\t"
      "v_cvt_pk_bf16_f32 v85, v18, v19\n\t"
      "s_branch LJOIN%=\n\t"
      "LNAN%=:\n\t"
      "s_mov_b32 s43, 0\n\t"
      "v_mov_b32 v97, v3\n\t"
      "LSC%=:\n\t"
      "ds_read_b128 v[32:35], v97\n\t"
      "v_add_u32 v97, 1024, v97\n\t"
      "s_add_u32 s43, s43, 1\n\t"
      "s_waitcnt lgkmcnt(0)\n\t"
      "v_max3_u32 v104, v104, v32, v33\n\t"
      "v_max3_u32 v104, v104, v34, v35\n\t"
      "s_cmp_lt_u32 s43, 40\n\t"
      "s_cbranch_scc1 LSC%=\n\t"
      "s_branch LCHK%=\n\t"
      "LFAIL%=:\n\t"
      "v_mov_b32 v105, 1\n\t"
      "s_mov_b32 s43, 688128\n\t"
      "buffer_store_dword v105, off, %[rs], s43 sc1\n\t"
      "LEND%=:\n\t"
      "s_waitcnt vmcnt(0) lgkmcnt(0)\n\t"
      "s_mov_b64 exec, -1\n\t"
      "s_mov_b32 m0, s39\n\t"
      : "+{v[0:7]}"(ad), "+{v[16:23]}"(ld)
      : [rs] "s"(rs), [rg] "s"(rg), [ru] "s"(ru), [w] "s"(w), [st] "s"(uni(stage)), [cb] "s"(uni((idx * 64 + w * 16) * 2)),
        [ewo] "s"(uni(ring_phys((last ? EW_LAST : EW_ACT) + idx * 128 + w * 4, flip ^ 1))), [qw] "s"(uni(ring_phys(EW_H + idx * 128, flip ^ 1))),
        [step] "s"(uni(last ? 63 : 1)), [end] "s"(uni(63 + last))
      : "memory", "vcc", "scc", "v8", "v9", "v10", "v11", "v12", "v13", "v14", "v15", "v24", "v25", "v26", "v27", "v28", "v29", "v30", "v31", "v32", "v33",
        "v34", "v35", "v36", "v37", "v38", "v39", "v40", "v41", "v42", "v43", "v44", "v45", "v46", "v47", "v48", "v49", "v50", "v51", "v52", "v53", "v54",
        "v55", "v56", "v57", "v58", "v59", "v60", "v61", "v62", "v63", "v64", "v65", "v66", "v67", "v68", "v69", "v70", "v71", "v72", "v73", "v74", "v75",
        "v76", "v77", "v78", "v79", "v80", "v81", "v82", "v83", "v84", "v85", "v86", "v87", "v88", "v89", "v90", "v91", "v92", "v93", "v94", "v95", "v96",
        "v97", "v98", "v99", "v100", "v101", "v102", "v103", "v104", "v105", "a0", "a1", "a2", "a3", "a4", "a5", "a6", "a7", "a8", "a9", "a10", "a11", "a12",
        "a13", "a14", "a15", "a16", "a17", "a18", "a19", "a20", "a21", "a22", "a23", "a24", "a25", "a26", "a27", "a28", "a29", "a30", "a31", "a32", "a33",
        "a34", "a35", "a36", "a37", "a38", "a39", "a40", "a41", "a42", "a43", "a44", "a45", "a46", "a47", "a48", "a49", "a50", "a51", "a52", "a53", "a54",
        "a55", "a56", "a57", "a58", "a59", "a60", "a61", "a62", "a63", "a64", "a65", "a66", "a67", "a68", "a69", "a70", "a71", "a72", "a73", "a74", "a75",
        "a76", "a77", "a78", "a79", "a80", "a81", "a82", "a83", "a84", "a85", "a86", "a87", "a88", "a89", "a90", "a91", "a92", "a93", "a94", "a95", "a96",
        "a97", "a98", "a99", "a100", "a101", "a102", "a103", "a104", "a105", "a106", "a107", "a108", "a109", "a110", "a111", "a112", "a113", "a114", "a115",
        "a116", "a117", "a118", "a119", "a120", "a121", "a122", "a123", "a124", "a125", "a126", "a127", "a128", "a129", "a130", "a131", "a132", "a133",
        "a134", "a135", "a136", "a137", "a138", "a139", "a140", "a141", "a142", "a143", "a144", "a145", "a146", "a147", "a148", "a149", "a150", "a151",
        "a152", "a153", "a154", "a155", "a156", "a157", "a158", "a159", "s33", "s34", "s35", "s36", "s37", "s38", "s39", "s40", "s41", "s42", "s43", "s44",
        "s45", "s46", "s47", "s48", "s49", "s50", "s51", "s52", "s53", "s54", "s55", "s56", "s57", "s58", "s59", "s60", "s61", "s62", "s63", "s64", "s65",
        "s66", "s67"
  );
}

DEV void role_wo(const Params& p, LAS char* lds, unsigned flip, int mt, int cg) {
  const int lane = threadIdx.x & 63, w = uni(threadIdx.x >> 6), g = lane >> 4, c = lane & 15, blk = mt * 20 + cg;
  if (!wait_words<8>(p.ws, CTL_QKV + (6 * lane + 1) * 128, LATE_WO)) return;
  const int rot = 4 * ((cg >> 1) & 7);
  const unsigned lb = (unsigned)(size_t)lds, stage = lb + w * 32768;
  u8v ad, ld = loader_regs<RM_NAT>(lane, DM * 2, stage, 3);
  ad[0] = lane * 16;
  ad[1] = ring_phys(EW_OX + (lane & 31) * 128, flip ^ 1);
  ad[2] = (w == 0) ? (16 * mt + c) * (DM * 2) + cg * 64 + g * 16 : DROP;
  ad[3] = lb + w * 2048 + lane * 16;
  ad[4] = lb + lane * 16;
  ad[5] = (w == 0 && !(lane & 16)) ? 40960 + blk * 128 + c * 8 + (lane >> 5) * 4 : DROP;
  ad[6] = (threadIdx.x == 0) ? 0u : DROP;
  ad[7] = (w == 0) ? blk * 1024 + lane * 16 : DROP;
  const u4 rs = mk_rs(p.ws, WS_END), rx = mk_rs(p.x, 2048 * DM * 2), rw = mk_rs(p.wo, 4096 * DM * 2);
  asm volatile(
      "s_nop 4\n\t"
      "s_mov_b32 s39, m0\n\t"
      "s_cmp_eq_u32 %[w], 0\n\t"
      "s_cselect_b64 s[66:67], 1, 0\n\t"
      "s_add_u32 m0, %[st], 0\n\t"
      "s_add_u32 s43, %[rot], 0\n\t"
      "s_and_b32 s43, s43, 31\n\t"
      "s_mul_i32 s43, s43, 40960\n\t"
      "s_add_u32 s43, s43, %[cb]\n\t"
      "buffer_load_dwordx4 v16, %[rw], s43 offen lds\n\t"
      "buffer_load_dwordx4 v17, %[rw], s43 offen offset:1024 lds\n\t"
      "s_add_u32 m0, %[st], 2048\n\t"
      "s_add_u32 s43, %[rot], 1\n\t"
      "s_and_b32 s43, s43, 31\n\t"
      "s_mul_i32 s43, s43, 40960\n\t"
      "s_add_u32 s43, s43, %[cb]\n\t"
      "buffer_load_dwordx4 v16, %[rw], s43 offen lds\n\t"
      "buffer_load_dwordx4 v17, %[rw], s43 offen offset:1024 lds\n\t"
      "s_add_u32 m0, %[st], 4096\n\t"
      "s_add_u32 s43, %[rot], 2\n\t"
      "s_and_b32 s43, s43, 31\n\t"
      "s_mul_i32 s43, s43, 40960\n\t"
      "s_add_u32 s43, s43, %[cb]\n\t"
      "buffer_load_dwordx4 v16, %[rw], s43 offen lds\n\t"
      "buffer_load_dwordx4 v17, %[rw], s43 offen offset:1024 lds\n\t"
      "s_add_u32 m0, %[st], 6144\n\t"
      "s_add_u32 s43, %[rot], 3\n\t"
      "s_and_b32 s43, s43, 31\n\t"
      "s_mul_i32 s43, s43, 40960\n\t"
      "s_add_u32 s43, s43, %[cb]\n\t"
      "buffer_load_dwordx4 v16, %[rw], s43 offen lds\n\t"
      "buffer_load_dwordx4 v17, %[rw], s43 offen offset:1024 lds\n\t"
      "s_add_u32 m0, %[st], 8192\n\t"
      "s_add_u32 s43, %[rot], 4\n\t"
      "s_and_b32 s43, s43, 31\n\t"
      "s_mul_i32 s43, s43, 40960\n\t"
      "s_add_u32 s43, s43, %[cb]\n\t"
      "buffer_load_dwordx4 v16, %[rw], s43 offen lds\n\t"
      "buffer_load_dwordx4 v17, %[rw], s43 offen offset:1024 lds\n\t"
      "s_add_u32 m0, %[st], 10240\n\t"
      "s_add_u32 s43, %[rot], 5\n\t"
      "s_and_b32 s43, s43, 31\n\t"
      "s_mul_i32 s43, s43, 40960\n\t"
      "s_add_u32 s43, s43, %[cb]\n\t"
      "buffer_load_dwordx4 v16, %[rw], s43 offen lds\n\t"
      "buffer_load_dwordx4 v17, %[rw], s43 offen offset:1024 lds\n\t"
      "s_add_u32 m0, %[st], 12288\n\t"
      "s_add_u32 s43, %[rot], 6\n\t"
      "s_and_b32 s43, s43, 31\n\t"
      "s_mul_i32 s43, s43, 40960\n\t"
      "s_add_u32 s43, s43, %[cb]\n\t"
      "buffer_load_dwordx4 v16, %[rw], s43 offen lds\n\t"
      "buffer_load_dwordx4 v17, %[rw], s43 offen offset:1024 lds\n\t"
      "s_add_u32 m0, %[st], 14336\n\t"
      "s_add_u32 s43, %[rot], 7\n\t"
      "s_and_b32 s43, s43, 31\n\t"
      "s_mul_i32 s43, s43, 40960\n\t"
      "s_add_u32 s43, s43, %[cb]\n\t"
      "buffer_load_dwordx4 v16, %[rw], s43 offen lds\n\t"
      "buffer_load_dwordx4 v17, %[rw], s43 offen offset:1024 lds\n\t"
      "s_waitcnt vmcnt(12)\n\t"
      "ds_read_b64_tr_b16 a[0:1], v20 offset:0\n\t"
      "ds_read_b64_tr_b16 a[2:3], v20 offset:1024\n\t"
      "ds_read_b64_tr_b16 a[4:5], v20 offset:8\n\t"
      "ds_read_b64_tr_b16 a[6:7], v20 offset:1032\n\t"
      "ds_read_b64_tr_b16 a[8:9], v20 offset:2048\n\t"
      "ds_read_b64_tr_b16 a[10:11], v20 offset:3072\n\t"
      "ds_read_b64_tr_b16 a[12:13], v20 offset:2056\n\t"
      "ds_read_b64_tr_b16 a[14:15], v20 offset:3080\n\t"
      "s_waitcnt lgkmcnt(0)\n\t"
      "s_add_u32 m0, %[st], 0\n\t"
      "s_add_u32 s43, %[rot], 8\n\t"
      "s_and_b32 s43, s43, 31\n\t"
      "s_mul_i32 s43, s43, 40960\n\t"
      "s_add_u32 s43, s43, %[cb]\n\t"
      "buffer_load_dwordx4 v16, %[rw], s43 offen lds\n\t"
      "buffer_load_dwordx4 v17, %[rw], s43 offen offset:1024 lds\n\t"
      "s_add_u32 m0, %[st], 2048\n\t"
      "s_add_u32 s43, %[rot], 9\n\t"
      "s_and_b32 s43, s43, 31\n\t"
      "s_mul_i32 s43, s43, 40960\n\t"
      "s_add_u32 s43, s43, %[cb]\n\t"
      "buffer_load_dwordx4 v16, %[rw], s43 offen lds\n\t"
      "buffer_load_dwordx4 v17, %[rw], s43 offen offset:1024 lds\n\t"
      "s_waitcnt vmcnt(12)\n\t"
      "ds_read_b64_tr_b16 a[16:17], v20 offset:4096\n\t"
      "ds_read_b64_tr_b16 a[18:19], v20 offset:5120\n\t"
      "ds_read_b64_tr_b16 a[20:21], v20 offset:4104\n\t"
      "ds_read_b64_tr_b16 a[22:23], v20 offset:5128\n\t"
      "ds_read_b64_tr_b16 a[24:25], v20 offset:6144\n\t"
      "ds_read_b64_tr_b16 a[26:27], v20 offset:7168\n\t"
      "ds_read_b64_tr_b16 a[28:29], v20 offset:6152\n\t"
      "ds_read_b64_tr_b16 a[30:31], v20 offset:7176\n\t"
      "s_waitcnt lgkmcnt(0)\n\t"
      "s_add_u32 m0, %[st], 4096\n\t"
      "s_add_u32 s43, %[rot], 10\n\t"
      "s_and_b32 s43, s43, 31\n\t"
      "s_mul_i32 s43, s43, 40960\n\t"
      "s_add_u32 s43, s43, %[cb]\n\t"
      "buffer_load_dwordx4 v16, %[rw], s43 offen lds\n\t"
      "buffer_load_dwordx4 v17, %[rw], s43 offen offset:1024 lds\n\t"
      "s_add_u32 m0, %[st], 6144\n\t"
      "s_add_u32 s43, %[rot], 11\n\t"
      "s_and_b32 s43, s43, 31\n\t"
      "s_mul_i32 s43, s43, 40960\n\t"
      "s_add_u32 s43, s43, %[cb]\n\t"
      "buffer_load_dwordx4 v16, %[rw], s43 offen lds\n\t"
      "buffer_load_dwordx4 v17, %[rw], s43 offen offset:1024 lds\n\t"
      "s_waitcnt vmcnt(12)\n\t"
      "ds_read_b64_tr_b16 a[32:33], v20 offset:8192\n\t"
      "ds_read_b64_tr_b16 a[34:35], v20 offset:9216\n\t"
      "ds_read_b64_tr_b16 a[36:37], v20 offset:8200\n\t"
      "ds_read_b64_tr_b16 a[38:39], v20 offset:9224\n\t"
      "ds_read_b64_tr_b16 a[40:41], v20 offset:10240\n\t"
      "ds_read_b64_tr_b16 a[42:43], v20 offset:11264\n\t"
      "ds_read_b64_tr_b16 a[44:45], v20 offset:10248\n\t"
      "ds_read_b64_tr_b16 a[46:47], v20 offset:11272\n\t"
      "s_waitcnt lgkmcnt(0)\n\t"
      "s_add_u32 m0, %[st], 8192\n\t"
      "s_add_u32 s43, %[rot], 12\n\t"
      "s_and_b32 s43, s43, 31\n\t"
      "s_mul_i32 s43, s43, 40960\n\t"
      "s_add_u32 s43, s43, %[cb]\n\t"
      "buffer_load_dwordx4 v16, %[rw], s43 offen lds\n\t"
      "buffer_load_dwordx4 v17, %[rw], s43 offen offset:1024 lds\n\t"
      "s_add_u32 m0, %[st], 10240\n\t"
      "s_add_u32 s43, %[rot], 13\n\t"
      "s_and_b32 s43, s43, 31\n\t"
      "s_mul_i32 s43, s43, 40960\n\t"
      "s_add_u32 s43, s43, %[cb]\n\t"
      "buffer_load_dwordx4 v16, %[rw], s43 offen lds\n\t"
      "buffer_load_dwordx4 v17, %[rw], s43 offen offset:1024 lds\n\t"
      "s_waitcnt vmcnt(12)\n\t"
      "ds_read_b64_tr_b16 a[48:49], v20 offset:12288\n\t"
      "ds_read_b64_tr_b16 a[50:51], v20 offset:13312\n\t"
      "ds_read_b64_tr_b16 a[52:53], v20 offset:12296\n\t"
      "ds_read_b64_tr_b16 a[54:55], v20 offset:13320\n\t"
      "ds_read_b64_tr_b16 a[56:57], v20 offset:14336\n\t"
      "ds_read_b64_tr_b16 a[58:59], v20 offset:15360\n\t"
      "ds_read_b64_tr_b16 a[60:61], v20 offset:14344\n\t"
      "ds_read_b64_tr_b16 a[62:63], v20 offset:15368\n\t"
      "s_waitcnt lgkmcnt(0)\n\t"
      "s_add_u32 m0, %[st], 12288\n\t"
      "s_add_u32 s43, %[rot], 14\n\t"
      "s_and_b32 s43, s43, 31\n\t"
      "s_mul_i32 s43, s43, 40960\n\t"
      "s_add_u32 s43, s43, %[cb]\n\t"
      "buffer_load_dwordx4 v16, %[rw], s43 offen lds\n\t"
      "buffer_load_dwordx4 v17, %[rw], s43 offen offset:1024 lds\n\t"
      "s_add_u32 m0, %[st], 14336\n\t"
      "s_add_u32 s43, %[rot], 15\n\t"
      "s_and_b32 s43, s43, 31\n\t"
      "s_mul_i32 s43, s43, 40960\n\t"
      "s_add_u32 s43, s43, %[cb]\n\t"
      "buffer_load_dwordx4 v16, %[rw], s43 offen lds\n\t"
      "buffer_load_dwordx4 v17, %[rw], s43 offen offset:1024 lds\n\t"
      "s_waitcnt vmcnt(12)\n\t"
      "ds_read_b64_tr_b16 a[64:65], v20 offset:0\n\t"
      "ds_read_b64_tr_b16 a[66:67], v20 offset:1024\n\t"
      "ds_read_b64_tr_b16 a[68:69], v20 offset:8\n\t"
      "ds_read_b64_tr_b16 a[70:71], v20 offset:1032\n\t"
      "ds_read_b64_tr_b16 a[72:73], v20 offset:2048\n\t"
      "ds_read_b64_tr_b16 a[74:75], v20 offset:3072\n\t"
      "ds_read_b64_tr_b16 a[76:77], v20 offset:2056\n\t"
      "ds_read_b64_tr_b16 a[78:79], v20 offset:3080\n\t"
      "s_waitcnt lgkmcnt(0)\n\t"
      "s_add_u32 m0, %[st], 0\n\t"
      "s_add_u32 s43, %[rot], 16\n\t"
      "s_and_b32 s43, s43, 31\n\t"
      "s_mul_i32 s43, s43, 40960\n\t"
      "s_add_u32 s43, s43, %[cb]\n\t"
      "buffer_load_dwordx4 v16, %[rw], s43 offen lds\n\t"
      "buffer_load_dwordx4 v17, %[rw], s43 offen offset:1024 lds\n\t"
      "s_add_u32 m0, %[st], 2048\n\t"
      "s_add_u32 s43, %[rot], 17\n\t"
      "s_and_b32 s43, s43, 31\n\t"
      "s_mul_i32 s43, s43, 40960\n\t"
      "s_add_u32 s43, s43, %[cb]\n\t"
      "buffer_load_dwordx4 v16, %[rw], s43 offen lds\n\t"
      "buffer_load_dwordx4 v17, %[rw], s43 offen offset:1024 lds\n\t"
      "s_waitcnt vmcnt(12)\n\t"
      "ds_read_b64_tr_b16 a[80:81], v20 offset:4096\n\t"
      "ds_read_b64_tr_b16 a[82:83], v20 offset:5120\n\t"
      "ds_read_b64_tr_b16 a[84:85], v20 offset:4104\n\t"
      "ds_read_b64_tr_b16 a[86:87], v20 offset:5128\n\t"
      "ds_read_b64_tr_b16 a[88:89], v20 offset:6144\n\t"
      "ds_read_b64_tr_b16 a[90:91], v20 offset:7168\n\t"
      "ds_read_b64_tr_b16 a[92:93], v20 offset:6152\n\t"
      "ds_read_b64_tr_b16 a[94:95], v20 offset:7176\n\t"
      "s_waitcnt lgkmcnt(0)\n\t"
      "s_add_u32 m0, %[st], 4096\n\t"
      "s_add_u32 s43, %[rot], 18\n\t"
      "s_and_b32 s43, s43, 31\n\t"
      "s_mul_i32 s43, s43, 40960\n\t"
      "s_add_u32 s43, s43, %[cb]\n\t"
      "buffer_load_dwordx4 v16, %[rw], s43 offen lds\n\t"
      "buffer_load_dwordx4 v17, %[rw], s43 offen offset:1024 lds\n\t"
      "s_add_u32 m0, %[st], 6144\n\t"
      "s_add_u32 s43, %[rot], 19\n\t"
      "s_and_b32 s43, s43, 31\n\t"
      "s_mul_i32 s43, s43, 40960\n\t"
      "s_add_u32 s43, s43, %[cb]\n\t"
      "buffer_load_dwordx4 v16, %[rw], s43 offen lds\n\t"
      "buffer_load_dwordx4 v17, %[rw], s43 offen offset:1024 lds\n\t"
      "s_waitcnt vmcnt(12)\n\t"
      "ds_read_b64_tr_b16 a[96:97], v20 offset:8192\n\t"
      "ds_read_b64_tr_b16 a[98:99], v20 offset:9216\n\t"
      "ds_read_b64_tr_b16 a[100:101], v20 offset:8200\n\t"
      "ds_read_b64_tr_b16 a[102:103], v20 offset:9224\n\t"
      "ds_read_b64_tr_b16 a[104:105], v20 offset:10240\n\t"
      "ds_read_b64_tr_b16 a[106:107], v20 offset:11264\n\t"
      "ds_read_b64_tr_b16 a[108:109], v20 offset:10248\n\t"
      "ds_read_b64_tr_b16 a[110:111], v20 offset:11272\n\t"
      "s_waitcnt lgkmcnt(0)\n\t"
      "s_add_u32 m0, %[st], 8192\n\t"
      "s_add_u32 s43, %[rot], 20\n\t"
      "s_and_b32 s43, s43, 31\n\t"
      "s_mul_i32 s43, s43, 40960\n\t"
      "s_add_u32 s43, s43, %[cb]\n\t"
      "buffer_load_dwordx4 v16, %[rw], s43 offen lds\n\t"
      "buffer_load_dwordx4 v17, %[rw], s43 offen offset:1024 lds\n\t"
      "s_add_u32 m0, %[st], 10240\n\t"
      "s_add_u32 s43, %[rot], 21\n\t"
      "s_and_b32 s43, s43, 31\n\t"
      "s_mul_i32 s43, s43, 40960\n\t"
      "s_add_u32 s43, s43, %[cb]\n\t"
      "buffer_load_dwordx4 v16, %[rw], s43 offen lds\n\t"
      "buffer_load_dwordx4 v17, %[rw], s43 offen offset:1024 lds\n\t"
      "s_waitcnt vmcnt(12)\n\t"
      "ds_read_b64_tr_b16 a[112:113], v20 offset:12288\n\t"
      "ds_read_b64_tr_b16 a[114:115], v20 offset:13312\n\t"
      "ds_read_b64_tr_b16 a[116:117], v20 offset:12296\n\t"
      "ds_read_b64_tr_b16 a[118:119], v20 offset:13320\n\t"
      "ds_read_b64_tr_b16 a[120:121], v20 offset:14336\n\t"
      "ds_read_b64_tr_b16 a[122:123], v20 offset:15360\n\t"
      "ds_read_b64_tr_b16 a[124:125], v20 offset:14344\n\t"
      "ds_read_b64_tr_b16 a[126:127], v20 offset:15368\n\t"
      "s_waitcnt lgkmcnt(0)\n\t"
      "s_add_u32 m0, %[st], 12288\n\t"
      "s_add_u32 s43, %[rot], 22\n\t"
      "s_and_b32 s43, s43, 31\n\t"
      "s_mul_i32 s43, s43, 40960\n\t"
      "s_add_u32 s43, s43, %[cb]\n\t"
      "buffer_load_dwordx4 v16, %[rw], s43 offen lds\n\t"
      "buffer_load_dwordx4 v17, %[rw], s43 offen offset:1024 lds\n\t"
      "s_add_u32 m0, %[st], 14336\n\t"
      "s_add_u32 s43, %[rot], 23\n\t"
      "s_and_b32 s43, s43, 31\n\t"
      "s_mul_i32 s43, s43, 40960\n\t"
      "s_add_u32 s43, s43, %[cb]\n\t"
      "buffer_load_dwordx4 v16, %[rw], s43 offen lds\n\t"
      "buffer_load_dwordx4 v17, %[rw], s43 offen offset:1024 lds\n\t"
      "s_waitcnt vmcnt(12)\n\t"
      "ds_read_b64_tr_b16 a[128:129], v20 offset:0\n\t"
      "ds_read_b64_tr_b16 a[130:131], v20 offset:1024\n\t"
      "ds_read_b64_tr_b16 a[132:133], v20 offset:8\n\t"
      "ds_read_b64_tr_b16 a[134:135], v20 offset:1032\n\t"
      "ds_read_b64_tr_b16 a[136:137], v20 offset:2048\n\t"
      "ds_read_b64_tr_b16 a[138:139], v20 offset:3072\n\t"
      "ds_read_b64_tr_b16 a[140:141], v20 offset:2056\n\t"
      "ds_read_b64_tr_b16 a[142:143], v20 offset:3080\n\t"
      "s_waitcnt lgkmcnt(0)\n\t"
      "s_add_u32 m0, %[st], 0\n\t"
      "s_add_u32 s43, %[rot], 24\n\t"
      "s_and_b32 s43, s43, 31\n\t"
      "s_mul_i32 s43, s43, 40960\n\t"
      "s_add_u32 s43, s43, %[cb]\n\t"
      "buffer_load_dwordx4 v16, %[rw], s43 offen lds\n\t"
      "buffer_load_dwordx4 v17, %[rw], s43 offen offset:1024 lds\n\t"
      "s_add_u32 m0, %[st], 2048\n\t"
      "s_add_u32 s43, %[rot], 25\n\t"
      "s_and_b32 s43, s43, 31\n\t"
      "s_mul_i32 s43, s43, 40960\n\t"
      "s_add_u32 s43, s43, %[cb]\n\t"
      "buffer_load_dwordx4 v16, %[rw], s43 offen lds\n\t"
      "buffer_load_dwordx4 v17, %[rw], s43 offen offset:1024 lds\n\t"
      "s_waitcnt vmcnt(12)\n\t"
      "ds_read_b64_tr_b16 a[144:145], v20 offset:4096\n\t"
      "ds_read_b64_tr_b16 a[146:147], v20 offset:5120\n\t"
      "ds_read_b64_tr_b16 a[148:149], v20 offset:4104\n\t"
      "ds_read_b64_tr_b16 a[150:151], v20 offset:5128\n\t"
      "ds_read_b64_tr_b16 a[152:153], v20 offset:6144\n\t"
      "ds_read_b64_tr_b16 a[154:155], v20 offset:7168\n\t"
      "ds_read_b64_tr_b16 a[156:157], v20 offset:6152\n\t"
      "ds_read_b64_tr_b16 a[158:159], v20 offset:7176\n\t"
      "s_waitcnt lgkmcnt(0)\n\t"
      "s_add_u32 m0, %[st], 4096\n\t"
      "s_add_u32 s43, %[rot], 26\n\t"
      "s_and_b32 s43, s43, 31\n\t"
      "s_mul_i32 s43, s43, 40960\n\t"
      "s_add_u32 s43, s43, %[cb]\n\t"
      "buffer_load_dwordx4 v16, %[rw], s43 offen lds\n\t"
      "buffer_load_dwordx4 v17, %[rw], s43 offen offset:1024 lds\n\t"
      "s_add_u32 m0, %[st], 6144\n\t"
      "s_add_u32 s43, %[rot], 27\n\t"
      "s_and_b32 s43, s43, 31\n\t"
      "s_mul_i32 s43, s43, 40960\n\t"
      "s_add_u32 s43, s43, %[cb]\n\t"
      "buffer_load_dwordx4 v16, %[rw], s43 offen lds\n\t"
      "buffer_load_dwordx4 v17, %[rw], s43 offen offset:1024 lds\n\t"
      "s_waitcnt vmcnt(12)\n\t"
      "ds_read_b64_tr_b16 a[160:161], v20 offset:8192\n\t"
      "ds_read_b64_tr_b16 a[162:163], v20 offset:9216\n\t"
      "ds_read_b64_tr_b16 a[164:165], v20 offset:8200\n\t"
      "ds_read_b64_tr_b16 a[166:167], v20 offset:9224\n\t"
      "ds_read_b64_tr_b16 a[168:169], v20 offset:10240\n\t"
      "ds_read_b64_tr_b16 a[170:171], v20 offset:11264\n\t"
      "ds_read_b64_tr_b16 a[172:173], v20 offset:10248\n\t"
      "ds_read_b64_tr_b16 a[174:175], v20 offset:11272\n\t"
      "s_waitcnt lgkmcnt(0)\n\t"
      "s_add_u32 m0, %[st], 8192\n\t"
      "s_add_u32 s43, %[rot], 28\n\t"
      "s_and_b32 s43, s43, 31\n\t"
      "s_mul_i32 s43, s43, 40960\n\t"
      "s_add_u32 s43, s43, %[cb]\n\t"
      "buffer_load_dwordx4 v16, %[rw], s43 offen lds\n\t"
      "buffer_load_dwordx4 v17, %[rw], s43 offen offset:1024 lds\n\t"
      "s_add_u32 m0, %[st], 10240\n\t"
      "s_add_u32 s43, %[rot], 29\n\t"
      "s_and_b32 s43, s43, 31\n\t"
      "s_mul_i32 s43, s43, 40960\n\t"
      "s_add_u32 s43, s43, %[cb]\n\t"
      "buffer_load_dwordx4 v16, %[rw], s43 offen lds\n\t"
      "buffer_load_dwordx4 v17, %[rw], s43 offen offset:1024 lds\n\t"
      "s_waitcnt vmcnt(12)\n\t"
      "ds_read_b64_tr_b16 a[176:177], v20 offset:12288\n\t"
      "ds_read_b64_tr_b16 a[178:179], v20 offset:13312\n\t"
      "ds_read_b64_tr_b16 a[180:181], v20 offset:12296\n\t"
      "ds_read_b64_tr_b16 a[182:183], v20 offset:13320\n\t"
      "ds_read_b64_tr_b16 a[184:185], v20 offset:14336\n\t"
      "ds_read_b64_tr_b16 a[186:187], v20 offset:15360\n\t"
      "ds_read_b64_tr_b16 a[188:189], v20 offset:14344\n\t"
      "ds_read_b64_tr_b16 a[190:191], v20 offset:15368\n\t"
      "s_waitcnt lgkmcnt(0)\n\t"
      "s_add_u32 m0, %[st], 12288\n\t"
      "s_add_u32 s43, %[rot], 30\n\t"
      "s_and_b32 s43, s43, 31\n\t"
      "s_mul_i32 s43, s43, 40960\n\t"
      "s_add_u32 s43, s43, %[cb]\n\t"
      "buffer_load_dwordx4 v16, %[rw], s43 offen lds\n\t"
      "buffer_load_dwordx4 v17, %[rw], s43 offen offset:1024 lds\n\t"
      "s_add_u32 m0, %[st], 14336\n\t"
      "s_add_u32 s43, %[rot], 31\n\t"
      "s_and_b32 s43, s43, 31\n\t"
      "s_mul_i32 s43, s43, 40960\n\t"
      "s_add_u32 s43, s43, %[cb]\n\t"
      "buffer_load_dwordx4 v16, %[rw], s43 offen lds\n\t"
      "buffer_load_dwordx4 v17, %[rw], s43 offen offset:1024 lds\n\t"
      "s_waitcnt vmcnt(12)\n\t"
      "ds_read_b64_tr_b16 a[192:193], v20 offset:0\n\t"
      "ds_read_b64_tr_b16 a[194:195], v20 offset:1024\n\t"
      "ds_read_b64_tr_b16 a[196:197], v20 offset:8\n\t"
      "ds_read_b64_tr_b16 a[198:199], v20 offset:1032\n\t"
      "ds_read_b64_tr_b16 a[200:201], v20 offset:2048\n\t"
      "ds_read_b64_tr_b16 a[202:203], v20 offset:3072\n\t"
      "ds_read_b64_tr_b16 a[204:205], v20 offset:2056\n\t"
      "ds_read_b64_tr_b16 a[206:207], v20 offset:3080\n\t"
      "s_waitcnt lgkmcnt(0)\n\t"
      "s_waitcnt vmcnt(8)\n\t"
      "ds_read_b64_tr_b16 a[208:209], v20 offset:4096\n\t"
      "ds_read_b64_tr_b16 a[210:211], v20 offset:5120\n\t"
      "ds_read_b64_tr_b16 a[212:213], v20 offset:4104\n\t"
      "ds_read_b64_tr_b16 a[214:215], v20 offset:5128\n\t"
      "ds_read_b64_tr_b16 a[216:217], v20 offset:6144\n\t"
      "ds_read_b64_tr_b16 a[218:219], v20 offset:7168\n\t"
      "ds_read_b64_tr_b16 a[220:221], v20 offset:6152\n\t"
      "ds_read_b64_tr_b16 a[222:223], v20 offset:7176\n\t"
      "s_waitcnt lgkmcnt(0)\n\t"
      "s_waitcnt vmcnt(4)\n\t"
      "ds_read_b64_tr_b16 a[224:225], v20 offset:8192\n\t"
      "ds_read_b64_tr_b16 a[226:227], v20 offset:9216\n\t"
      "ds_read_b64_tr_b16 a[228:229], v20 offset:8200\n\t"
      "ds_read_b64_tr_b16 a[230:231], v20 offset:9224\n\t"
      "ds_read_b64_tr_b16 a[232:233], v20 offset:10240\n\t"
      "ds_read_b64_tr_b16 a[234:235], v20 offset:11264\n\t"
      "ds_read_b64_tr_b16 a[236:237], v20 offset:10248\n\t"
      "ds_read_b64_tr_b16 a[238:239], v20 offset:11272\n\t"
      "s_waitcnt lgkmcnt(0)\n\t"
      "s_waitcnt vmcnt(0)\n\t"
      "ds_read_b64_tr_b16 a[240:241], v20 offset:12288\n\t"
      "ds_read_b64_tr_b16 a[242:243], v20 offset:13312\n\t"
      "ds_read_b64_tr_b16 a[244:245], v20 offset:12296\n\t"
      "ds_read_b64_tr_b16 a[246:247], v20 offset:13320\n\t"
      "ds_read_b64_tr_b16 a[248:249], v20 offset:14336\n\t"
      "ds_read_b64_tr_b16 a[250:251], v20 offset:15360\n\t"
      "ds_read_b64_tr_b16 a[252:253], v20 offset:14344\n\t"
      "ds_read_b64_tr_b16 a[254:255], v20 offset:15368\n\t"
      "s_waitcnt lgkmcnt(0)\n\t"
      "s_waitcnt lgkmcnt(0)\n\t"
      "s_barrier\n\t"
      "s_mov_b32 s40, 0\n\t"
      "s_mov_b32 s52, 70254592\n\t"
      "s_mov_b32 s53, 0\n\t"
      "s_mov_b32 s54, 0x3f803f80\n\t"
      "s_add_u32 s43, %[rot], 0\n\t"
      "s_and_b32 s43, s43, 31\n\t"
      "s_lshl_b32 s43, s43, 10\n\t"
      "s_add_u32 s44, s43, %[oxb]\n\t"
      "s_add_u32 s43, %[rot], 4\n\t"
      "s_and_b32 s43, s43, 31\n\t"
      "s_lshl_b32 s43, s43, 10\n\t"
      "s_add_u32 s45, s43, %[oxb]\n\t"
      "s_add_u32 s43, %[rot], 8\n\t"
      "s_and_b32 s43, s43, 31\n\t"
      "s_lshl_b32 s43, s43, 10\n\t"
      "s_add_u32 s46, s43, %[oxb]\n\t"
      "s_add_u32 s43, %[rot], 12\n\t"
      "s_and_b32 s43, s43, 31\n\t"
      "s_lshl_b32 s43, s43, 10\n\t"
      "s_add_u32 s47, s43, %[oxb]\n\t"
      "s_add_u32 s43, %[rot], 16\n\t"
      "s_and_b32 s43, s43, 31\n\t"
      "s_lshl_b32 s43, s43, 10\n\t"
      "s_add_u32 s48, s43, %[oxb]\n\t"
      "s_add_u32 s43, %[rot], 20\n\t"
      "s_and_b32 s43, s43, 31\n\t"
      "s_lshl_b32 s43, s43, 10\n\t"
      "s_add_u32 s49, s43, %[oxb]\n\t"
      "s_add_u32 s43, %[rot], 24\n\t"
      "s_and_b32 s43, s43, 31\n\t"
      "s_lshl_b32 s43, s43, 10\n\t"
      "s_add_u32 s50, s43, %[oxb]\n\t"
      "s_add_u32 s43, %[rot], 28\n\t"
      "s_and_b32 s43, s43, 31\n\t"
      "s_lshl_b32 s43, s43, 10\n\t"
      "s_add_u32 s51, s43, %[oxb]\n\t"
      "buffer_load_dwordx4 v[12:15], v2, %[rx], 0 offen\n\t"
      "s_waitcnt vmcnt(0)\n\t"
      "s_mov_b32 s41, 0\n\t"
      "LOOP%=:\n\t"
      "v_mov_b64 v[24:25], v[12:13]\n\t"
      "v_mov_b64 v[26:27], v[14:15]\n\t"
      "v_mov_b32 v9, s40\n\t"
      "s_mov_b32 s42, 0\n\t"
      "s_add_u32 s53, s53, 40960\n\t"
      "s_add_u32 s58, s40, 1\n\t"
      "s_cmp_eq_u32 s41, 0\n\t"
      "s_cbranch_scc1 LWAIT%=\n\t"
      "s_barrier\n\t"
      "s_branch LHAVE%=\n\t"
      "LWAIT%=:\n\t"
      "s_cmp_lg_u32 %[w], 1\n\t"
      "s_cbranch_scc1 LEWB%=\n\t"
      "s_mov_b32 exec_lo, -1\n\t"
      "s_mov_b32 exec_hi, 0\n\t"
      "buffer_load_dword v32, v1, %[rs], 0 offen sc1\n\t"
      "s_nop 12\n\t"
      "buffer_load_dword v33, v1, %[rs], 0 offen sc1\n\t"
      "s_nop 12\n\t"
      "buffer_load_dword v34, v1, %[rs], 0 offen sc1\n\t"
      "s_nop 12\n\t"
      "buffer_load_dword v35, v1, %[rs], 0 offen sc1\n\t"
      "s_nop 12\n\t"
      "LEW%=:\n\t"
      "s_waitcnt vmcnt(3)\n\t"
      "v_cmp_gt_i32 vcc, s40, v32\n\t"
      "s_cbranch_vccz LEWD%=\n\t"
      "buffer_load_dword v32, v1, %[rs], 0 offen sc1\n\t"
      "s_nop 9\n\t"
      "s_waitcnt vmcnt(3)\n\t"
      "v_cmp_gt_i32 vcc, s40, v33\n\t"
      "s_cbranch_vccz LEWD%=\n\t"
      "buffer_load_dword v33, v1, %[rs], 0 offen sc1\n\t"
      "s_nop 9\n\t"
      "s_waitcnt vmcnt(3)\n\t"
      "v_cmp_gt_i32 vcc, s40, v34\n\t"
      "s_cbranch_vccz LEWD%=\n\t"
      "buffer_load_dword v34, v1, %[rs], 0 offen sc1\n\t"
      "s_nop 9\n\t"
      "s_waitcnt vmcnt(3)\n\t"
      "v_cmp_gt_i32 vcc, s40, v35\n\t"
      "s_cbranch_vccz LEWD%=\n\t"
      "buffer_load_dword v35, v1, %[rs], 0 offen sc1\n\t"
      "s_nop 9\n\t"
      "s_add_u32 s42, s42, 1\n\t"
      "s_cmp_lt_u32 s42, 100000\n\t"
      "s_cbranch_scc1 LEW%=\n\t"
      "s_branch LFAIL%=\n\t"
      "LEWD%=:\n\t"
      "s_mov_b64 exec, -1\n\t"
      "LEWB%=:\n\t"
      "s_barrier\n\t"
      "s_mov_b32 s42, 0\n\t"
      "LPOLL%=:\n\t"
      "buffer_load_dwordx4 v[64:67], v0, %[rs], s44 offen offset:0 sc1\n\t"
      "s_nop 13\n\t"
      "buffer_load_dwordx4 v[68:71], v0, %[rs], s44 offen offset:1024 sc1\n\t"
      "s_nop 13\n\t"
      "buffer_load_dwordx4 v[72:75], v0, %[rs], s44 offen offset:2048 sc1\n\t"
      "s_nop 13\n\t"
      "buffer_load_dwordx4 v[76:79], v0, %[rs], s44 offen offset:3072 sc1\n\t"
      "s_nop 13\n\t"
      "buffer_load_dwordx4 v[80:83], v0, %[rs], s45 offen offset:0 sc1\n\t"
      "s_nop 13\n\t"
      "buffer_load_dwordx4 v[84:87], v0, %[rs], s45 offen offset:1024 sc1\n\t"
      "s_nop 13\n\t"
      "buffer_load_dwordx4 v[88:91], v0, %[rs], s45 offen offset:2048 sc1\n\t"
      "s_nop 13\n\t"
      "buffer_load_dwordx4 v[92:95], v0, %[rs], s45 offen offset:3072 sc1\n\t"
      "s_nop 13\n\t"
      "LHAVE%=:\n\t"
      "v_lshlrev_b32 v16, 16, v24\n\t"
      "v_and_b32 v17, 0xffff0000, v24\n\t"
      "v_lshlrev_b32 v18, 16, v25\n\t"
      "v_and_b32 v19, 0xffff0000, v25\n\t"
      "v_lshlrev_b32 v20, 16, v26\n\t"
      "v_and_b32 v21, 0xffff0000, v26\n\t"
      "v_lshlrev_b32 v22, 16, v27\n\t"
      "v_and_b32 v23, 0xffff0000, v27\n\t"
      "v_mov_b32 v8, 0\n\t"
      "s_add_u32 s42, s42, 1\n\t"
      "s_cmp_gt_u32 s42, 400000\n\t"
      "s_cbranch_scc1 LFAIL%=\n\t"
      "buffer_load_dwordx4 v[96:99], v0, %[rs], s46 offen offset:0 sc1\n\t"
      "s_waitcnt vmcnt(8)\n\t"
      "v_mfma_f32_16x16x32_bf16 v[16:19], a[0:3], v[64:67], v[16:19]\n\t"
      "v_max3_u32 v8, v8, v64, v65\n\t"
      "v_max3_u32 v8, v8, v66, v67\n\t"
      "v_mfma_f32_16x16x32_bf16 v[20:23], a[4:7], v[64:67], v[20:23]\n\t"
      "buffer_load_dwordx4 v[100:103], v0, %[rs], s46 offen offset:1024 sc1\n\t"
      "s_waitcnt vmcnt(8)\n\t"
      "v_mfma_f32_16x16x32_bf16 v[16:19], a[8:11], v[68:71], v[16:19]\n\t"
      "v_max3_u32 v8, v8, v68, v69\n\t"
      "v_max3_u32 v8, v8, v70, v71\n\t"
      "v_mfma_f32_16x16x32_bf16 v[20:23], a[12:15], v[68:71], v[20:23]\n\t"
      "buffer_load_dwordx4 v[104:107], v0, %[rs], s46 offen offset:2048 sc1\n\t"
      "s_waitcnt vmcnt(8)\n\t"
      "v_mfma_f32_16x16x32_bf16 v[16:19], a[16:19], v[72:75], v[16:19]\n\t"
      "v_max3_u32 v8, v8, v72, v73\n\t"
      "v_max3_u32 v8, v8, v74, v75\n\t"
      "v_mfma_f32_16x16x32_bf16 v[20:23], a[20:23], v[72:75], v[20:23]\n\t"
      "buffer_load_dwordx4 v[108:111], v0, %[rs], s46 offen offset:3072 sc1\n\t"
      "s_waitcnt vmcnt(8)\n\t"
      "v_mfma_f32_16x16x32_bf16 v[16:19], a[24:27], v[76:79], v[16:19]\n\t"
      "v_max3_u32 v8, v8, v76, v77\n\t"
      "v_max3_u32 v8, v8, v78, v79\n\t"
      "v_mfma_f32_16x16x32_bf16 v[20:23], a[28:31], v[76:79], v[20:23]\n\t"
      "buffer_load_dwordx4 v[112:115], v0, %[rs], s47 offen offset:0 sc1\n\t"
      "s_waitcnt vmcnt(8)\n\t"
      "v_mfma_f32_16x16x32_bf16 v[16:19], a[32:35], v[80:83], v[16:19]\n\t"
      "v_max3_u32 v8, v8, v80, v81\n\t"
      "v_max3_u32 v8, v8, v82, v83\n\t"
      "v_mfma_f32_16x16x32_bf16 v[20:23], a[36:39], v[80:83], v[20:23]\n\t"
      "buffer_load_dwordx4 v[116:119], v0, %[rs], s47 offen offset:1024 sc1\n\t"
      "s_waitcnt vmcnt(8)\n\t"
      "v_mfma_f32_16x16x32_bf16 v[16:19], a[40:43], v[84:87], v[16:19]\n\t"
      "v_max3_u32 v8, v8, v84, v85\n\t"
      "v_max3_u32 v8, v8, v86, v87\n\t"
      "v_mfma_f32_16x16x32_bf16 v[20:23], a[44:47], v[84:87], v[20:23]\n\t"
      "buffer_load_dwordx4 v[120:123], v0, %[rs], s47 offen offset:2048 sc1\n\t"
      "s_waitcnt vmcnt(8)\n\t"
      "v_mfma_f32_16x16x32_bf16 v[16:19], a[48:51], v[88:91], v[16:19]\n\t"
      "v_max3_u32 v8, v8, v88, v89\n\t"
      "v_max3_u32 v8, v8, v90, v91\n\t"
      "v_mfma_f32_16x16x32_bf16 v[20:23], a[52:55], v[88:91], v[20:23]\n\t"
      "buffer_load_dwordx4 v[124:127], v0, %[rs], s47 offen offset:3072 sc1\n\t"
      "s_waitcnt vmcnt(8)\n\t"
      "v_mfma_f32_16x16x32_bf16 v[16:19], a[56:59], v[92:95], v[16:19]\n\t"
      "v_max3_u32 v8, v8, v92, v93\n\t"
      "v_max3_u32 v8, v8, v94, v95\n\t"
      "v_mfma_f32_16x16x32_bf16 v[20:23], a[60:63], v[92:95], v[20:23]\n\t"
      "buffer_load_dwordx4 v[128:131], v0, %[rs], s48 offen offset:0 sc1\n\t"
      "s_waitcnt vmcnt(8)\n\t"
      "v_mfma_f32_16x16x32_bf16 v[16:19], a[64:67], v[96:99], v[16:19]\n\t"
      "v_max3_u32 v8, v8, v96, v97\n\t"
      "v_max3_u32 v8, v8, v98, v99\n\t"
      "v_mfma_f32_16x16x32_bf16 v[20:23], a[68:71], v[96:99], v[20:23]\n\t"
      "buffer_load_dwordx4 v[132:135], v0, %[rs], s48 offen offset:1024 sc1\n\t"
      "s_waitcnt vmcnt(8)\n\t"
      "v_mfma_f32_16x16x32_bf16 v[16:19], a[72:75], v[100:103], v[16:19]\n\t"
      "v_max3_u32 v8, v8, v100, v101\n\t"
      "v_max3_u32 v8, v8, v102, v103\n\t"
      "v_mfma_f32_16x16x32_bf16 v[20:23], a[76:79], v[100:103], v[20:23]\n\t"
      "buffer_load_dwordx4 v[136:139], v0, %[rs], s48 offen offset:2048 sc1\n\t"
      "s_waitcnt vmcnt(8)\n\t"
      "v_mfma_f32_16x16x32_bf16 v[16:19], a[80:83], v[104:107], v[16:19]\n\t"
      "v_max3_u32 v8, v8, v104, v105\n\t"
      "v_max3_u32 v8, v8, v106, v107\n\t"
      "v_mfma_f32_16x16x32_bf16 v[20:23], a[84:87], v[104:107], v[20:23]\n\t"
      "buffer_load_dwordx4 v[140:143], v0, %[rs], s48 offen offset:3072 sc1\n\t"
      "s_waitcnt vmcnt(8)\n\t"
      "v_mfma_f32_16x16x32_bf16 v[16:19], a[88:91], v[108:111], v[16:19]\n\t"
      "v_max3_u32 v8, v8, v108, v109\n\t"
      "v_max3_u32 v8, v8, v110, v111\n\t"
      "v_mfma_f32_16x16x32_bf16 v[20:23], a[92:95], v[108:111], v[20:23]\n\t"
      "buffer_load_dwordx4 v[144:147], v0, %[rs], s49 offen offset:0 sc1\n\t"
      "s_waitcnt vmcnt(8)\n\t"
      "v_mfma_f32_16x16x32_bf16 v[16:19], a[96:99], v[112:115], v[16:19]\n\t"
      "v_max3_u32 v8, v8, v112, v113\n\t"
      "v_max3_u32 v8, v8, v114, v115\n\t"
      "v_mfma_f32_16x16x32_bf16 v[20:23], a[100:103], v[112:115], v[20:23]\n\t"
      "buffer_load_dwordx4 v[148:151], v0, %[rs], s49 offen offset:1024 sc1\n\t"
      "s_waitcnt vmcnt(8)\n\t"
      "v_mfma_f32_16x16x32_bf16 v[16:19], a[104:107], v[116:119], v[16:19]\n\t"
      "v_max3_u32 v8, v8, v116, v117\n\t"
      "v_max3_u32 v8, v8, v118, v119\n\t"
      "v_mfma_f32_16x16x32_bf16 v[20:23], a[108:111], v[116:119], v[20:23]\n\t"
      "buffer_load_dwordx4 v[152:155], v0, %[rs], s49 offen offset:2048 sc1\n\t"
      "s_waitcnt vmcnt(8)\n\t"
      "v_mfma_f32_16x16x32_bf16 v[16:19], a[112:115], v[120:123], v[16:19]\n\t"
      "v_max3_u32 v8, v8, v120, v121\n\t"
      "v_max3_u32 v8, v8, v122, v123\n\t"
      "v_mfma_f32_16x16x32_bf16 v[20:23], a[116:119], v[120:123], v[20:23]\n\t"
      "buffer_load_dwordx4 v[156:159], v0, %[rs], s49 offen offset:3072 sc1\n\t"
      "s_waitcnt vmcnt(8)\n\t"
      "v_mfma_f32_16x16x32_bf16 v[16:19], a[120:123], v[124:127], v[16:19]\n\t"
      "v_max3_u32 v8, v8, v124, v125\n\t"
      "v_max3_u32 v8, v8, v126, v127\n\t"
      "v_mfma_f32_16x16x32_bf16 v[20:23], a[124:127], v[124:127], v[20:23]\n\t"
      "buffer_load_dwordx4 v[160:163], v0, %[rs], s50 offen offset:0 sc1\n\t"
      "s_waitcnt vmcnt(8)\n\t"
      "v_mfma_f32_16x16x32_bf16 v[16:19], a[128:131], v[128:131], v[16:19]\n\t"
      "v_max3_u32 v8, v8, v128, v129\n\t"
      "v_max3_u32 v8, v8, v130, v131\n\t"
      "v_mfma_f32_16x16x32_bf16 v[20:23], a[132:135], v[128:131], v[20:23]\n\t"
      "buffer_load_dwordx4 v[164:167], v0, %[rs], s50 offen offset:1024 sc1\n\t"
      "s_waitcnt vmcnt(8)\n\t"
      "v_mfma_f32_16x16x32_bf16 v[16:19], a[136:139], v[132:135], v[16:19]\n\t"
      "v_max3_u32 v8, v8, v132, v133\n\t"
      "v_max3_u32 v8, v8, v134, v135\n\t"
      "v_mfma_f32_16x16x32_bf16 v[20:23], a[140:143], v[132:135], v[20:23]\n\t"
      "buffer_load_dwordx4 v[168:171], v0, %[rs], s50 offen offset:2048 sc1\n\t"
      "s_waitcnt vmcnt(8)\n\t"
      "v_mfma_f32_16x16x32_bf16 v[16:19], a[144:147], v[136:139], v[16:19]\n\t"
      "v_max3_u32 v8, v8, v136, v137\n\t"
      "v_max3_u32 v8, v8, v138, v139\n\t"
      "v_mfma_f32_16x16x32_bf16 v[20:23], a[148:151], v[136:139], v[20:23]\n\t"
      "buffer_load_dwordx4 v[172:175], v0, %[rs], s50 offen offset:3072 sc1\n\t"
      "s_waitcnt vmcnt(8)\n\t"
      "v_mfma_f32_16x16x32_bf16 v[16:19], a[152:155], v[140:143], v[16:19]\n\t"
      "v_max3_u32 v8, v8, v140, v141\n\t"
      "v_max3_u32 v8, v8, v142, v143\n\t"
      "v_mfma_f32_16x16x32_bf16 v[20:23], a[156:159], v[140:143], v[20:23]\n\t"
      "buffer_load_dwordx4 v[176:179], v0, %[rs], s51 offen offset:0 sc1\n\t"
      "s_waitcnt vmcnt(8)\n\t"
      "v_mfma_f32_16x16x32_bf16 v[16:19], a[160:163], v[144:147], v[16:19]\n\t"
      "v_max3_u32 v8, v8, v144, v145\n\t"
      "v_max3_u32 v8, v8, v146, v147\n\t"
      "v_mfma_f32_16x16x32_bf16 v[20:23], a[164:167], v[144:147], v[20:23]\n\t"
      "buffer_load_dwordx4 v[180:183], v0, %[rs], s51 offen offset:1024 sc1\n\t"
      "s_waitcnt vmcnt(8)\n\t"
      "v_mfma_f32_16x16x32_bf16 v[16:19], a[168:171], v[148:151], v[16:19]\n\t"
      "v_max3_u32 v8, v8, v148, v149\n\t"
      "v_max3_u32 v8, v8, v150, v151\n\t"
      "v_mfma_f32_16x16x32_bf16 v[20:23], a[172:175], v[148:151], v[20:23]\n\t"
      "buffer_load_dwordx4 v[184:187], v0, %[rs], s51 offen offset:2048 sc1\n\t"
      "s_waitcnt vmcnt(8)\n\t"
      "v_mfma_f32_16x16x32_bf16 v[16:19], a[176:179], v[152:155], v[16:19]\n\t"
      "v_max3_u32 v8, v8, v152, v153\n\t"
      "v_max3_u32 v8, v8, v154, v155\n\t"
      "v_mfma_f32_16x16x32_bf16 v[20:23], a[180:183], v[152:155], v[20:23]\n\t"
      "buffer_load_dwordx4 v[188:191], v0, %[rs], s51 offen offset:3072 sc1\n\t"
      "s_waitcnt vmcnt(8)\n\t"
      "v_mfma_f32_16x16x32_bf16 v[16:19], a[184:187], v[156:159], v[16:19]\n\t"
      "v_max3_u32 v8, v8, v156, v157\n\t"
      "v_max3_u32 v8, v8, v158, v159\n\t"
      "v_mfma_f32_16x16x32_bf16 v[20:23], a[188:191], v[156:159], v[20:23]\n\t"
      "s_mov_b32 exec_hi, 0\n\t"
      "buffer_load_dword v56, v1, %[rs], 0 offen sc1\n\t"
      "s_mov_b32 exec_hi, -1\n\t"
      "buffer_load_dwordx4 v[12:15], v2, %[rx], s53 offen\n\t"
      "s_waitcnt vmcnt(9)\n\t"
      "v_mfma_f32_16x16x32_bf16 v[16:19], a[192:195], v[160:163], v[16:19]\n\t"
      "v_max3_u32 v8, v8, v160, v161\n\t"
      "v_max3_u32 v8, v8, v162, v163\n\t"
      "v_mfma_f32_16x16x32_bf16 v[20:23], a[196:199], v[160:163], v[20:23]\n\t"
      "s_waitcnt vmcnt(8)\n\t"
      "v_mfma_f32_16x16x32_bf16 v[16:19], a[200:203], v[164:167], v[16:19]\n\t"
      "v_max3_u32 v8, v8, v164, v165\n\t"
      "v_max3_u32 v8, v8, v166, v167\n\t"
      "v_mfma_f32_16x16x32_bf16 v[20:23], a[204:207], v[164:167], v[20:23]\n\t"
      "s_waitcnt vmcnt(7)\n\t"
      "v_mfma_f32_16x16x32_bf16 v[16:19], a[208:211], v[168:171], v[16:19]\n\t"
      "v_max3_u32 v8, v8, v168, v169\n\t"
      "v_max3_u32 v8, v8, v170, v171\n\t"
      "v_mfma_f32_16x16x32_bf16 v[20:23], a[212:215], v[168:171], v[20:23]\n\t"
      "s_waitcnt vmcnt(6)\n\t"
      "v_mfma_f32_16x16x32_bf16 v[16:19], a[216:219], v[172:175], v[16:19]\n\t"
      "v_max3_u32 v8, v8, v172, v173\n\t"
      "v_max3_u32 v8, v8, v174, v175\n\t"
      "v_mfma_f32_16x16x32_bf16 v[20:23], a[220:223], v[172:175], v[20:23]\n\t"
      "s_waitcnt vmcnt(5)\n\t"
      "v_mfma_f32_16x16x32_bf16 v[16:19], a[224:227], v[176:179], v[16:19]\n\t"
      "v_max3_u32 v8, v8, v176, v177\n\t"
      "v_max3_u32 v8, v8, v178, v179\n\t"
      "v_mfma_f32_16x16x32_bf16 v[20:23], a[228:231], v[176:179], v[20:23]\n\t"
      "s_waitcnt vmcnt(4)\n\t"
      "v_mfma_f32_16x16x32_bf16 v[16:19], a[232:235], v[180:183], v[16:19]\n\t"
      "v_max3_u32 v8, v8, v180, v181\n\t"
      "v_max3_u32 v8, v8, v182, v183\n\t"
      "v_mfma_f32_16x16x32_bf16 v[20:23], a[236:239], v[180:183], v[20:23]\n\t"
      "s_waitcnt vmcnt(3)\n\t"
      "v_mfma_f32_16x16x32_bf16 v[16:19], a[240:243], v[184:187], v[16:19]\n\t"
      "v_max3_u32 v8, v8, v184, v185\n\t"
      "v_max3_u32 v8, v8, v186, v187\n\t"
      "v_mfma_f32_16x16x32_bf16 v[20:23], a[244:247], v[184:187], v[20:23]\n\t"
      "s_waitcnt vmcnt(2)\n\t"
      "v_mfma_f32_16x16x32_bf16 v[16:19], a[248:251], v[188:191], v[16:19]\n\t"
      "v_max3_u32 v8, v8, v188, v189\n\t"
      "v_max3_u32 v8, v8, v190, v191\n\t"
      "v_mfma_f32_16x16x32_bf16 v[20:23], a[252:255], v[188:191], v[20:23]\n\t"
      "v_cmp_eq_u32 vcc, -1, v8\n\t"
      "s_nop 3\n\t"
      "s_cbranch_vccnz LRETRY%=\n\t"
      "s_cmp_eq_u32 %[w], 0\n\t"
      "s_cbranch_scc1 LW0%=\n\t"
      "ds_write_b128 v3, v[16:19]\n\t"
      "ds_write_b128 v3, v[20:23] offset:1024\n\t"
      "s_waitcnt lgkmcnt(0)\n\t"
      "s_barrier\n\t"
      "s_waitcnt vmcnt(0)\n\t"
      "v_cmp_gt_i32 vcc, s58, v56\n\t"
      "s_add_u32 s44, s44, 32768\n\t"
      "s_add_u32 s45, s45, 32768\n\t"
      "s_add_u32 s46, s46, 32768\n\t"
      "s_add_u32 s47, s47, 32768\n\t"
      "s_add_u32 s48, s48, 32768\n\t"
      "s_add_u32 s49, s49, 32768\n\t"
      "s_add_u32 s50, s50, 32768\n\t"
      "s_add_u32 s51, s51, 32768\n\t"
      "s_cmp_eq_u32 vcc_lo, 0\n\t"
      "s_cselect_b32 s41, 1, 0\n\t"
      "s_cbranch_scc0 LNa%=\n\t"
      "buffer_load_dwordx4 v[64:67], v0, %[rs], s44 offen offset:0 sc1\n\t"
      "buffer_load_dwordx4 v[68:71], v0, %[rs], s44 offen offset:1024 sc1\n\t"
      "buffer_load_dwordx4 v[72:75], v0, %[rs], s44 offen offset:2048 sc1\n\t"
      "buffer_load_dwordx4 v[76:79], v0, %[rs], s44 offen offset:3072 sc1\n\t"
      "buffer_load_dwordx4 v[80:83], v0, %[rs], s45 offen offset:0 sc1\n\t"
      "buffer_load_dwordx4 v[84:87], v0, %[rs], s45 offen offset:1024 sc1\n\t"
      "buffer_load_dwordx4 v[88:91], v0, %[rs], s45 offen offset:2048 sc1\n\t"
      "buffer_load_dwordx4 v[92:95], v0, %[rs], s45 offen offset:3072 sc1\n\t"
      "LNa%=:\n\t"
      "s_branch LNEXT%=\n\t"
      "LRETRY%=:\n\t"
      "s_nop 7\n\t"
      "s_branch LPOLL%=\n\t"
      "LW0%=:\n\t"
      "s_barrier\n\t"
      "ds_read_b128 v[28:31], v4 offset:2048\n\t"
      "ds_read_b128 v[32:35], v4 offset:3072\n\t"
      "ds_read_b128 v[36:39], v4 offset:4096\n\t"
      "ds_read_b128 v[40:43], v4 offset:5120\n\t"
      "ds_read_b128 v[44:47], v4 offset:6144\n\t"
      "ds_read_b128 v[48:51], v4 offset:7168\n\t"
      "buffer_store_dword v9, v6, %[rs], %[ewo] offen sc1\n\t"
      "s_waitcnt lgkmcnt(5)\n\t"
      "v_pk_add_f32 v[16:17], v[16:17], v[28:29]\n\t"
      "v_pk_add_f32 v[18:19], v[18:19], v[30:31]\n\t"
      "s_waitcnt lgkmcnt(4)\n\t"
      "v_pk_add_f32 v[20:21], v[20:21], v[32:33]\n\t"
      "v_pk_add_f32 v[22:23], v[22:23], v[34:35]\n\t"
      "s_waitcnt lgkmcnt(3)\n\t"
      "v_pk_add_f32 v[16:17], v[16:17], v[36:37]\n\t"
      "v_pk_add_f32 v[18:19], v[18:19], v[38:39]\n\t"
      "s_waitcnt lgkmcnt(2)\n\t"
      "v_pk_add_f32 v[20:21], v[20:21], v[40:41]\n\t"
      "v_pk_add_f32 v[22:23], v[22:23], v[42:43]\n\t"
      "s_waitcnt lgkmcnt(1)\n\t"
      "v_pk_add_f32 v[16:17], v[16:17], v[44:45]\n\t"
      "v_pk_add_f32 v[18:19], v[18:19], v[46:47]\n\t"
      "s_waitcnt lgkmcnt(0)\n\t"
      "v_pk_add_f32 v[20:21], v[20:21], v[48:49]\n\t"
      "v_pk_add_f32 v[22:23], v[22:23], v[50:51]\n\t"
      "v_cvt_pk_bf16_f32 v28, v16, v17\n\t"
      "v_cvt_pk_bf16_f32 v29, v18, v19\n\t"
      "v_cvt_pk_bf16_f32 v30, v20, v21\n\t"
      "v_cvt_pk_bf16_f32 v31, v22, v23\n\t"
      "buffer_store_dwordx4 v[28:31], v7, %[rs], s52 offen sc1\n\t"
      "v_mov_b64 v[10:11], 0\n\t"
      "v_dot2c_f32_bf16 v10, s54, v28\n\t"
      "v_dot2c_f32_bf16 v11, v28, v28\n\t"
      "v_dot2c_f32_bf16 v10, s54, v29\n\t"
      "v_dot2c_f32_bf16 v11, v29, v29\n\t"
      "v_dot2c_f32_bf16 v10, s54, v30\n\t"
      "v_dot2c_f32_bf16 v11, v30, v30\n\t"
      "v_dot2c_f32_bf16 v10, s54, v31\n\t"
      "v_dot2c_f32_bf16 v11, v31, v31\n\t"
      "s_nop 2\n\t"
      "v_permlane32_swap_b32 v10, v11\n\t"
      "v_add_f32 v10, v10, v11\n\t"
      "v_mov_b32 v11, v10\n\t"
      "s_nop 1\n\t"
      "v_permlane16_swap_b32 v10, v11\n\t"
      "v_add_f32 v10, v10, v11\n\t"
      "buffer_store_dword v10, v5, %[rs], s52 offen sc1\n\t"
      "s_waitcnt vmcnt(3)\n\t"
      "v_cmp_gt_i32 vcc, s58, v56\n\t"
      "s_add_u32 s44, s44, 32768\n\t"
      "s_add_u32 s45, s45, 32768\n\t"
      "s_add_u32 s46, s46, 32768\n\t"
      "s_add_u32 s47, s47, 32768\n\t"
      "s_add_u32 s48, s48, 32768\n\t"
      "s_add_u32 s49, s49, 32768\n\t"
      "s_add_u32 s50, s50, 32768\n\t"
      "s_add_u32 s51, s51, 32768\n\t"
      "s_cmp_eq_u32 vcc_lo, 0\n\t"
      "s_cselect_b32 s41, 1, 0\n\t"
      "s_cbranch_scc0 LNb%=\n\t"
      "buffer_load_dwordx4 v[64:67], v0, %[rs], s44 offen offset:0 sc1\n\t"
      "buffer_load_dwordx4 v[68:71], v0, %[rs], s44 offen offset:1024 sc1\n\t"
      "buffer_load_dwordx4 v[72:75], v0, %[rs], s44 offen offset:2048 sc1\n\t"
      "buffer_load_dwordx4 v[76:79], v0, %[rs], s44 offen offset:3072 sc1\n\t"
      "buffer_load_dwordx4 v[80:83], v0, %[rs], s45 offen offset:0 sc1\n\t"
      "buffer_load_dwordx4 v[84:87], v0, %[rs], s45 offen offset:1024 sc1\n\t"
      "buffer_load_dwordx4 v[88:91], v0, %[rs], s45 offen offset:2048 sc1\n\t"
      "buffer_load_dwordx4 v[92:95], v0, %[rs], s45 offen offset:3072 sc1\n\t"
      "LNb%=:\n\t"
      "LNEXT%=:\n\t"
      "v_xor_b32 v3, 8192, v3\n\t"
      "v_xor_b32 v4, 8192, v4\n\t"
      "s_add_u32 s52, s52, 46080\n\t"
      "s_add_u32 s40, s40, 1\n\t"
      "s_cmp_lt_u32 s40, 64\n\t"
      "s_cbranch_scc1 LOOP%=\n\t"
      "s_branch LEND%=\n\t"
      "LFAIL%=:\n\t"
      "v_mov_b32 v8, 1\n\t"
      "s_mov_b32 s43, 688128\n\t"
      "buffer_store_dword v8, off, %[rs], s43 sc1\n\t"
      "LEND%=:\n\t"
      "s_waitcnt vmcnt(0) lgkmcnt(0)\n\t"
      "s_mov_b64 exec, -1\n\t"
      "s_mov_b32 m0, s39\n\t"
      : "+{v[0:7]}"(ad), "+{v[16:23]}"(ld)
      : [rs] "s"(rs), [rx] "s"(rx), [rw] "s"(rw), [w] "s"(w), [st] "s"(uni(stage)), [cb] "s"(uni(w * 1024 * (DM * 2) + cg * 64)), [rot] "s"(rot),
        [ewo] "s"(uni(ring_phys(EW_H + blk * 128, flip ^ 1))), [oxb] "s"(uni(WS_OX + (w * 2 + mt) * OX_MT))
      : "memory", "vcc", "scc", "v8", "v9", "v10", "v11", "v12", "v13", "v14", "v15", "v24", "v25", "v26", "v27", "v28", "v29", "v30", "v31", "v32", "v33",
        "v34", "v35", "v36", "v37", "v38", "v39", "v40", "v41", "v42", "v43", "v44", "v45", "v46", "v47", "v48", "v49", "v50", "v51", "v52", "v53", "v54",
        "v55", "v56", "v57", "v58", "v59", "v60", "v61", "v62", "v63", "v64", "v65", "v66", "v67", "v68", "v69", "v70", "v71", "v72", "v73", "v74", "v75",
        "v76", "v77", "v78", "v79", "v80", "v81", "v82", "v83", "v84", "v85", "v86", "v87", "v88", "v89", "v90", "v91", "v92", "v93", "v94", "v95", "v96",
        "v97", "v98", "v99", "v100", "v101", "v102", "v103", "v104", "v105", "v106", "v107", "v108", "v109", "v110", "v111", "v112", "v113", "v114", "v115",
        "v116", "v117", "v118", "v119", "v120", "v121", "v122", "v123", "v124", "v125", "v126", "v127", "v128", "v129", "v130", "v131", "v132", "v133",
        "v134", "v135", "v136", "v137", "v138", "v139", "v140", "v141", "v142", "v143", "v144", "v145", "v146", "v147", "v148", "v149", "v150", "v151",
        "v152", "v153", "v154", "v155", "v156", "v157", "v158", "v159", "v160", "v161", "v162", "v163", "v164", "v165", "v166", "v167", "v168", "v169",
        "v170", "v171", "v172", "v173", "v174", "v175", "v176", "v177", "v178", "v179", "v180", "v181", "v182", "v183", "v184", "v185", "v186", "v187",
        "v188", "v189", "v190", "v191", "a0", "a1", "a2", "a3", "a4", "a5", "a6", "a7", "a8", "a9", "a10", "a11", "a12", "a13", "a14", "a15", "a16", "a17",
        "a18", "a19", "a20", "a21", "a22", "a23", "a24", "a25", "a26", "a27", "a28", "a29", "a30", "a31", "a32", "a33", "a34", "a35", "a36", "a37", "a38",
        "a39", "a40", "a41", "a42", "a43", "a44", "a45", "a46", "a47", "a48", "a49", "a50", "a51", "a52", "a53", "a54", "a55", "a56", "a57", "a58", "a59",
        "a60", "a61", "a62", "a63", "a64", "a65", "a66", "a67", "a68", "a69", "a70", "a71", "a72", "a73", "a74", "a75", "a76", "a77", "a78", "a79", "a80",
        "a81", "a82", "a83", "a84", "a85", "a86", "a87", "a88", "a89", "a90", "a91", "a92", "a93", "a94", "a95", "a96", "a97", "a98", "a99", "a100", "a101",
        "a102", "a103", "a104", "a105", "a106", "a107", "a108", "a109", "a110", "a111", "a112", "a113", "a114", "a115", "a116", "a117", "a118", "a119",
        "a120", "a121", "a122", "a123", "a124", "a125", "a126", "a127", "a128", "a129", "a130", "a131", "a132", "a133", "a134", "a135", "a136", "a137",
        "a138", "a139", "a140", "a141", "a142", "a143", "a144", "a145", "a146", "a147", "a148", "a149", "a150", "a151", "a152", "a153", "a154", "a155",
        "a156", "a157", "a158", "a159", "a160", "a161", "a162", "a163", "a164", "a165", "a166", "a167", "a168", "a169", "a170", "a171", "a172", "a173",
        "a174", "a175", "a176", "a177", "a178", "a179", "a180", "a181", "a182", "a183", "a184", "a185", "a186", "a187", "a188", "a189", "a190", "a191",
        "a192", "a193", "a194", "a195", "a196", "a197", "a198", "a199", "a200", "a201", "a202", "a203", "a204", "a205", "a206", "a207", "a208", "a209",
        "a210", "a211", "a212", "a213", "a214", "a215", "a216", "a217", "a218", "a219", "a220", "a221", "a222", "a223", "a224", "a225", "a226", "a227",
        "a228", "a229", "a230", "a231", "a232", "a233", "a234", "a235", "a236", "a237", "a238", "a239", "a240", "a241", "a242", "a243", "a244", "a245",
        "a246", "a247", "a248", "a249", "a250", "a251", "a252", "a253", "a254", "a255", "s33", "s34", "s35", "s36", "s37", "s38", "s39", "s40", "s41", "s42",
        "s43", "s44", "s45", "s46", "s47", "s48", "s49", "s50", "s51", "s52", "s53", "s54", "s55", "s56", "s57", "s58", "s59", "s60", "s61", "s62", "s63",
        "s64", "s65", "s66", "s67"
  );
}

DEV void role_dn(const Params& p, LAS char* lds, unsigned flip, int cg) {
  const int lane = threadIdx.x & 63, w = uni(threadIdx.x >> 6), g = lane >> 4, c = lane & 15;
  const int k0 = (w < 2) ? 14 * w : 13 * w + 1;
  if (!wait_words<8>(p.ws, CTL_QKV + (6 * lane + 1) * 128, LATE_START)) return;
  const unsigned lb = (unsigned)(size_t)lds, stage = lb + w * 32768;
  u8v ad, ld = loader_regs<RM_NAT>(lane, DM * 2, stage, 3);
  ad[0] = WS_ACT + ((cg & 1) * 54 + k0) * 1024 + lane * 16;
  ad[1] = ring_phys(EW_ACT + min((k0 >> 1) + (lane & 7), 26) * 128, flip ^ 1);
  ad[2] = (w == 0) ? lane * 16 : DROP;
  const int row = 8 * (w & 1) + (lane >> 3), pc = lane & 7;
  ad[3] = lb + w * 4096 + g * 256 + (c ^ (2 * g)) * 16;
  ad[4] = lb + (w >> 1) * 16384 + (pc >> 2) * 2048 + (pc & 3) * 256 + (row ^ (2 * pc)) * 16;
  ad[5] = (16 * ((w >> 1) ^ (cg & 1)) + row) * (DM * 2) + cg * 128 + pc * 16;
  ad[6] = WS_ACT + (((cg & 1) ^ 1) * 54 + k0) * 1024 + lane * 16;
  ad[7] = ad[3] ^ 128;
  const u4 rs = mk_rs(p.ws, WS_END), ro = mk_rs(p.out, 2048 * DM * 2), rw = mk_rs(p.wd, FF * DM * 2);
  asm volatile(
      "s_add_u32 s43, %[rot], 0\n\t"
      "s_sub_u32 s60, s43, 14\n\t"
      "s_cselect_b32 s44, s43, s60\n\t"
      "s_add_u32 s43, %[rot], 1\n\t"
      "s_sub_u32 s60, s43, 14\n\t"
      "s_cselect_b32 s45, s43, s60\n\t"
      "s_add_u32 s43, %[rot], 2\n\t"
      "s_sub_u32 s60, s43, 14\n\t"
      "s_cselect_b32 s46, s43, s60\n\t"
      "s_add_u32 s43, %[rot], 3\n\t"
      "s_sub_u32 s60, s43, 14\n\t"
      "s_cselect_b32 s47, s43, s60\n\t"
      "s_add_u32 s43, %[rot], 4\n\t"
      "s_sub_u32 s60, s43, 14\n\t"
      "s_cselect_b32 s48, s43, s60\n\t"
      "s_add_u32 s43, %[rot], 5\n\t"
      "s_sub_u32 s60, s43, 14\n\t"
      "s_cselect_b32 s49, s43, s60\n\t"
      "s_add_u32 s43, %[rot], 6\n\t"
      "s_sub_u32 s60, s43, 14\n\t"
      "s_cselect_b32 s50, s43, s60\n\t"
      "s_add_u32 s43, %[rot], 7\n\t"
      "s_sub_u32 s60, s43, 14\n\t"
      "s_cselect_b32 s51, s43, s60\n\t"
      "s_add_u32 s43, %[rot], 8\n\t"
      "s_sub_u32 s60, s43, 14\n\t"
      "s_cselect_b32 s52, s43, s60\n\t"
      "s_add_u32 s43, %[rot], 9\n\t"
      "s_sub_u32 s60, s43, 14\n\t"
      "s_cselect_b32 s53, s43, s60\n\t"
      "s_add_u32 s43, %[rot], 10\n\t"
      "s_sub_u32 s60, s43, 14\n\t"
      "s_cselect_b32 s54, s43, s60\n\t"
      "s_add_u32 s43, %[rot], 11\n\t"
      "s_sub_u32 s60, s43, 14\n\t"
      "s_cselect_b32 s55, s43, s60\n\t"
      "s_add_u32 s43, %[rot], 12\n\t"
      "s_sub_u32 s60, s43, 14\n\t"
      "s_cselect_b32 s56, s43, s60\n\t"
      "s_add_u32 s43, %[rot], 13\n\t"
      "s_sub_u32 s60, s43, 14\n\t"
      "s_cselect_b32 s57, s43, s60\n\t"
      "s_nop 4\n\t"
      "s_mov_b32 s39, m0\n\t"
      "s_cmp_eq_u32 %[w], 0\n\t"
      "s_cselect_b64 s[66:67], 1, 0\n\t"
      "s_add_u32 m0, %[st], 0\n\t"
      "s_mul_i32 s43, s44, 40960\n\t"
      "s_add_u32 s43, s43, %[cb]\n\t"
      "s_add_u32 s43, s43, 0\n\t"
      "buffer_load_dwordx4 v16, %[rw], s43 offen lds\n\t"
      "buffer_load_dwordx4 v17, %[rw], s43 offen offset:1024 lds\n\t"
      "s_add_u32 m0, %[st], 2048\n\t"
      "s_mul_i32 s43, s44, 40960\n\t"
      "s_add_u32 s43, s43, %[cb]\n\t"
      "s_add_u32 s43, s43, 64\n\t"
      "buffer_load_dwordx4 v16, %[rw], s43 offen lds\n\t"
      "buffer_load_dwordx4 v17, %[rw], s43 offen offset:1024 lds\n\t"
      "s_add_u32 m0, %[st], 4096\n\t"
      "s_mul_i32 s43, s45, 40960\n\t"
      "s_add_u32 s43, s43, %[cb]\n\t"
      "s_add_u32 s43, s43, 0\n\t"
      "buffer_load_dwordx4 v16, %[rw], s43 offen lds\n\t"
      "buffer_load_dwordx4 v17, %[rw], s43 offen offset:1024 lds\n\t"
      "s_add_u32 m0, %[st], 6144\n\t"
      "s_mul_i32 s43, s45, 40960\n\t"
      "s_add_u32 s43, s43, %[cb]\n\t"
      "s_add_u32 s43, s43, 64\n\t"
      "buffer_load_dwordx4 v16, %[rw], s43 offen lds\n\t"
      "buffer_load_dwordx4 v17, %[rw], s43 offen offset:1024 lds\n\t"
      "s_add_u32 m0, %[st], 8192\n\t"
      "s_mul_i32 s43, s46, 40960\n\t"
      "s_add_u32 s43, s43, %[cb]\n\t"
      "s_add_u32 s43, s43, 0\n\t"
      "buffer_load_dwordx4 v16, %[rw], s43 offen lds\n\t"
      "buffer_load_dwordx4 v17, %[rw], s43 offen offset:1024 lds\n\t"
      "s_add_u32 m0, %[st], 10240\n\t"
      "s_mul_i32 s43, s46, 40960\n\t"
      "s_add_u32 s43, s43, %[cb]\n\t"
      "s_add_u32 s43, s43, 64\n\t"
      "buffer_load_dwordx4 v16, %[rw], s43 offen lds\n\t"
      "buffer_load_dwordx4 v17, %[rw], s43 offen offset:1024 lds\n\t"
      "s_add_u32 m0, %[st], 12288\n\t"
      "s_mul_i32 s43, s47, 40960\n\t"
      "s_add_u32 s43, s43, %[cb]\n\t"
      "s_add_u32 s43, s43, 0\n\t"
      "buffer_load_dwordx4 v16, %[rw], s43 offen lds\n\t"
      "buffer_load_dwordx4 v17, %[rw], s43 offen offset:1024 lds\n\t"
      "s_add_u32 m0, %[st], 14336\n\t"
      "s_mul_i32 s43, s47, 40960\n\t"
      "s_add_u32 s43, s43, %[cb]\n\t"
      "s_add_u32 s43, s43, 64\n\t"
      "buffer_load_dwordx4 v16, %[rw], s43 offen lds\n\t"
      "buffer_load_dwordx4 v17, %[rw], s43 offen offset:1024 lds\n\t"
      "s_waitcnt vmcnt(12)\n\t"
      "ds_read_b64_tr_b16 a[0:1], v20 offset:0\n\t"
      "ds_read_b64_tr_b16 a[2:3], v20 offset:1024\n\t"
      "ds_read_b64_tr_b16 a[4:5], v20 offset:8\n\t"
      "ds_read_b64_tr_b16 a[6:7], v20 offset:1032\n\t"
      "ds_read_b64_tr_b16 a[8:9], v20 offset:2048\n\t"
      "ds_read_b64_tr_b16 a[10:11], v20 offset:3072\n\t"
      "ds_read_b64_tr_b16 a[12:13], v20 offset:2056\n\t"
      "ds_read_b64_tr_b16 a[14:15], v20 offset:3080\n\t"
      "s_waitcnt lgkmcnt(0)\n\t"
      "s_add_u32 m0, %[st], 0\n\t"
      "s_mul_i32 s43, s48, 40960\n\t"
      "s_add_u32 s43, s43, %[cb]\n\t"
      "s_add_u32 s43, s43, 0\n\t"
      "buffer_load_dwordx4 v16, %[rw], s43 offen lds\n\t"
      "buffer_load_dwordx4 v17, %[rw], s43 offen offset:1024 lds\n\t"
      "s_add_u32 m0, %[st], 2048\n\t"
      "s_mul_i32 s43, s48, 40960\n\t"
      "s_add_u32 s43, s43, %[cb]\n\t"
      "s_add_u32 s43, s43, 64\n\t"
      "buffer_load_dwordx4 v16, %[rw], s43 offen lds\n\t"
      "buffer_load_dwordx4 v17, %[rw], s43 offen offset:1024 lds\n\t"
      "s_waitcnt vmcnt(12)\n\t"
      "ds_read_b64_tr_b16 a[16:17], v20 offset:4096\n\t"
      "ds_read_b64_tr_b16 a[18:19], v20 offset:5120\n\t"
      "ds_read_b64_tr_b16 a[20:21], v20 offset:4104\n\t"
      "ds_read_b64_tr_b16 a[22:23], v20 offset:5128\n\t"
      "ds_read_b64_tr_b16 a[24:25], v20 offset:6144\n\t"
      "ds_read_b64_tr_b16 a[26:27], v20 offset:7168\n\t"
      "ds_read_b64_tr_b16 a[28:29], v20 offset:6152\n\t"
      "ds_read_b64_tr_b16 a[30:31], v20 offset:7176\n\t"
      "s_waitcnt lgkmcnt(0)\n\t"
      "s_add_u32 m0, %[st], 4096\n\t"
      "s_mul_i32 s43, s49, 40960\n\t"
      "s_add_u32 s43, s43, %[cb]\n\t"
      "s_add_u32 s43, s43, 0\n\t"
      "buffer_load_dwordx4 v16, %[rw], s43 offen lds\n\t"
      "buffer_load_dwordx4 v17, %[rw], s43 offen offset:1024 lds\n\t"
      "s_add_u32 m0, %[st], 6144\n\t"
      "s_mul_i32 s43, s49, 40960\n\t"
      "s_add_u32 s43, s43, %[cb]\n\t"
      "s_add_u32 s43, s43, 64\n\t"
      "buffer_load_dwordx4 v16, %[rw], s43 offen lds\n\t"
      "buffer_load_dwordx4 v17, %[rw], s43 offen offset:1024 lds\n\t"
      "s_waitcnt vmcnt(12)\n\t"
      "ds_read_b64_tr_b16 a[32:33], v20 offset:8192\n\t"
      "ds_read_b64_tr_b16 a[34:35], v20 offset:9216\n\t"
      "ds_read_b64_tr_b16 a[36:37], v20 offset:8200\n\t"
      "ds_read_b64_tr_b16 a[38:39], v20 offset:9224\n\t"
      "ds_read_b64_tr_b16 a[40:41], v20 offset:10240\n\t"
      "ds_read_b64_tr_b16 a[42:43], v20 offset:11264\n\t"
      "ds_read_b64_tr_b16 a[44:45], v20 offset:10248\n\t"
      "ds_read_b64_tr_b16 a[46:47], v20 offset:11272\n\t"
      "s_waitcnt lgkmcnt(0)\n\t"
      "s_add_u32 m0, %[st], 8192\n\t"
      "s_mul_i32 s43, s50, 40960\n\t"
      "s_add_u32 s43, s43, %[cb]\n\t"
      "s_add_u32 s43, s43, 0\n\t"
      "buffer_load_dwordx4 v16, %[rw], s43 offen lds\n\t"
      "buffer_load_dwordx4 v17, %[rw], s43 offen offset:1024 lds\n\t"
      "s_add_u32 m0, %[st], 10240\n\t"
      "s_mul_i32 s43, s50, 40960\n\t"
      "s_add_u32 s43, s43, %[cb]\n\t"
      "s_add_u32 s43, s43, 64\n\t"
      "buffer_load_dwordx4 v16, %[rw], s43 offen lds\n\t"
      "buffer_load_dwordx4 v17, %[rw], s43 offen offset:1024 lds\n\t"
      "s_waitcnt vmcnt(12)\n\t"
      "ds_read_b64_tr_b16 a[48:49], v20 offset:12288\n\t"
      "ds_read_b64_tr_b16 a[50:51], v20 offset:13312\n\t"
      "ds_read_b64_tr_b16 a[52:53], v20 offset:12296\n\t"
      "ds_read_b64_tr_b16 a[54:55], v20 offset:13320\n\t"
      "ds_read_b64_tr_b16 a[56:57], v20 offset:14336\n\t"
      "ds_read_b64_tr_b16 a[58:59], v20 offset:15360\n\t"
      "ds_read_b64_tr_b16 a[60:61], v20 offset:14344\n\t"
      "ds_read_b64_tr_b16 a[62:63], v20 offset:15368\n\t"
      "s_waitcnt lgkmcnt(0)\n\t"
      "s_add_u32 m0, %[st], 12288\n\t"
      "s_mul_i32 s43, s51, 40960\n\t"
      "s_add_u32 s43, s43, %[cb]\n\t"
      "s_add_u32 s43, s43, 0\n\t"
      "buffer_load_dwordx4 v16, %[rw], s43 offen lds\n\t"
      "buffer_load_dwordx4 v17, %[rw], s43 offen offset:1024 lds\n\t"
      "s_add_u32 m0, %[st], 14336\n\t"
      "s_mul_i32 s43, s51, 40960\n\t"
      "s_add_u32 s43, s43, %[cb]\n\t"
      "s_add_u32 s43, s43, 64\n\t"
      "buffer_load_dwordx4 v16, %[rw], s43 offen lds\n\t"
      "buffer_load_dwordx4 v17, %[rw], s43 offen offset:1024 lds\n\t"
      "s_waitcnt vmcnt(12)\n\t"
      "ds_read_b64_tr_b16 a[64:65], v20 offset:0\n\t"
      "ds_read_b64_tr_b16 a[66:67], v20 offset:1024\n\t"
      "ds_read_b64_tr_b16 a[68:69], v20 offset:8\n\t"
      "ds_read_b64_tr_b16 a[70:71], v20 offset:1032\n\t"
      "ds_read_b64_tr_b16 a[72:73], v20 offset:2048\n\t"
      "ds_read_b64_tr_b16 a[74:75], v20 offset:3072\n\t"
      "ds_read_b64_tr_b16 a[76:77], v20 offset:2056\n\t"
      "ds_read_b64_tr_b16 a[78:79], v20 offset:3080\n\t"
      "s_waitcnt lgkmcnt(0)\n\t"
      "s_add_u32 m0, %[st], 0\n\t"
      "s_mul_i32 s43, s52, 40960\n\t"
      "s_add_u32 s43, s43, %[cb]\n\t"
      "s_add_u32 s43, s43, 0\n\t"
      "buffer_load_dwordx4 v16, %[rw], s43 offen lds\n\t"
      "buffer_load_dwordx4 v17, %[rw], s43 offen offset:1024 lds\n\t"
      "s_add_u32 m0, %[st], 2048\n\t"
      "s_mul_i32 s43, s52, 40960\n\t"
      "s_add_u32 s43, s43, %[cb]\n\t"
      "s_add_u32 s43, s43, 64\n\t"
      "buffer_load_dwordx4 v16, %[rw], s43 offen lds\n\t"
      "buffer_load_dwordx4 v17, %[rw], s43 offen offset:1024 lds\n\t"
      "s_waitcnt vmcnt(12)\n\t"
      "ds_read_b64_tr_b16 a[80:81], v20 offset:4096\n\t"
      "ds_read_b64_tr_b16 a[82:83], v20 offset:5120\n\t"
      "ds_read_b64_tr_b16 a[84:85], v20 offset:4104\n\t"
      "ds_read_b64_tr_b16 a[86:87], v20 offset:5128\n\t"
      "ds_read_b64_tr_b16 a[88:89], v20 offset:6144\n\t"
      "ds_read_b64_tr_b16 a[90:91], v20 offset:7168\n\t"
      "ds_read_b64_tr_b16 a[92:93], v20 offset:6152\n\t"
      "ds_read_b64_tr_b16 a[94:95], v20 offset:7176\n\t"
      "s_waitcnt lgkmcnt(0)\n\t"
      "s_add_u32 m0, %[st], 4096\n\t"
      "s_mul_i32 s43, s53, 40960\n\t"
      "s_add_u32 s43, s43, %[cb]\n\t"
      "s_add_u32 s43, s43, 0\n\t"
      "buffer_load_dwordx4 v16, %[rw], s43 offen lds\n\t"
      "buffer_load_dwordx4 v17, %[rw], s43 offen offset:1024 lds\n\t"
      "s_add_u32 m0, %[st], 6144\n\t"
      "s_mul_i32 s43, s53, 40960\n\t"
      "s_add_u32 s43, s43, %[cb]\n\t"
      "s_add_u32 s43, s43, 64\n\t"
      "buffer_load_dwordx4 v16, %[rw], s43 offen lds\n\t"
      "buffer_load_dwordx4 v17, %[rw], s43 offen offset:1024 lds\n\t"
      "s_waitcnt vmcnt(12)\n\t"
      "ds_read_b64_tr_b16 a[96:97], v20 offset:8192\n\t"
      "ds_read_b64_tr_b16 a[98:99], v20 offset:9216\n\t"
      "ds_read_b64_tr_b16 a[100:101], v20 offset:8200\n\t"
      "ds_read_b64_tr_b16 a[102:103], v20 offset:9224\n\t"
      "ds_read_b64_tr_b16 a[104:105], v20 offset:10240\n\t"
      "ds_read_b64_tr_b16 a[106:107], v20 offset:11264\n\t"
      "ds_read_b64_tr_b16 a[108:109], v20 offset:10248\n\t"
      "ds_read_b64_tr_b16 a[110:111], v20 offset:11272\n\t"
      "s_waitcnt lgkmcnt(0)\n\t"
      "s_add_u32 m0, %[st], 8192\n\t"
      "s_mul_i32 s43, s54, 40960\n\t"
      "s_add_u32 s43, s43, %[cb]\n\t"
      "s_add_u32 s43, s43, 0\n\t"
      "buffer_load_dwordx4 v16, %[rw], s43 offen lds\n\t"
      "buffer_load_dwordx4 v17, %[rw], s43 offen offset:1024 lds\n\t"
      "s_add_u32 m0, %[st], 10240\n\t"
      "s_mul_i32 s43, s54, 40960\n\t"
      "s_add_u32 s43, s43, %[cb]\n\t"
      "s_add_u32 s43, s43, 64\n\t"
      "buffer_load_dwordx4 v16, %[rw], s43 offen lds\n\t"
      "buffer_load_dwordx4 v17, %[rw], s43 offen offset:1024 lds\n\t"
      "s_waitcnt vmcnt(12)\n\t"
      "ds_read_b64_tr_b16 a[112:113], v20 offset:12288\n\t"
      "ds_read_b64_tr_b16 a[114:115], v20 offset:13312\n\t"
      "ds_read_b64_tr_b16 a[116:117], v20 offset:12296\n\t"
      "ds_read_b64_tr_b16 a[118:119], v20 offset:13320\n\t"
      "ds_read_b64_tr_b16 a[120:121], v20 offset:14336\n\t"
      "ds_read_b64_tr_b16 a[122:123], v20 offset:15360\n\t"
      "ds_read_b64_tr_b16 a[124:125], v20 offset:14344\n\t"
      "ds_read_b64_tr_b16 a[126:127], v20 offset:15368\n\t"
      "s_waitcnt lgkmcnt(0)\n\t"
      "s_add_u32 m0, %[st], 12288\n\t"
      "s_mul_i32 s43, s55, 40960\n\t"
      "s_add_u32 s43, s43, %[cb]\n\t"
      "s_add_u32 s43, s43, 0\n\t"
      "buffer_load_dwordx4 v16, %[rw], s43 offen lds\n\t"
      "buffer_load_dwordx4 v17, %[rw], s43 offen offset:1024 lds\n\t"
      "s_add_u32 m0, %[st], 14336\n\t"
      "s_mul_i32 s43, s55, 40960\n\t"
      "s_add_u32 s43, s43, %[cb]\n\t"
      "s_add_u32 s43, s43, 64\n\t"
      "buffer_load_dwordx4 v16, %[rw], s43 offen lds\n\t"
      "buffer_load_dwordx4 v17, %[rw], s43 offen offset:1024 lds\n\t"
      "s_waitcnt vmcnt(12)\n\t"
      "ds_read_b64_tr_b16 a[128:129], v20 offset:0\n\t"
      "ds_read_b64_tr_b16 a[130:131], v20 offset:1024\n\t"
      "ds_read_b64_tr_b16 a[132:133], v20 offset:8\n\t"
      "ds_read_b64_tr_b16 a[134:135], v20 offset:1032\n\t"
      "ds_read_b64_tr_b16 a[136:137], v20 offset:2048\n\t"
      "ds_read_b64_tr_b16 a[138:139], v20 offset:3072\n\t"
      "ds_read_b64_tr_b16 a[140:141], v20 offset:2056\n\t"
      "ds_read_b64_tr_b16 a[142:143], v20 offset:3080\n\t"
      "s_waitcnt lgkmcnt(0)\n\t"
      "s_add_u32 m0, %[st], 0\n\t"
      "s_mul_i32 s43, s56, 40960\n\t"
      "s_add_u32 s43, s43, %[cb]\n\t"
      "s_add_u32 s43, s43, 0\n\t"
      "buffer_load_dwordx4 v16, %[rw], s43 offen lds\n\t"
      "buffer_load_dwordx4 v17, %[rw], s43 offen offset:1024 lds\n\t"
      "s_add_u32 m0, %[st], 2048\n\t"
      "s_mul_i32 s43, s56, 40960\n\t"
      "s_add_u32 s43, s43, %[cb]\n\t"
      "s_add_u32 s43, s43, 64\n\t"
      "buffer_load_dwordx4 v16, %[rw], s43 offen lds\n\t"
      "buffer_load_dwordx4 v17, %[rw], s43 offen offset:1024 lds\n\t"
      "s_waitcnt vmcnt(12)\n\t"
      "ds_read_b64_tr_b16 a[144:145], v20 offset:4096\n\t"
      "ds_read_b64_tr_b16 a[146:147], v20 offset:5120\n\t"
      "ds_read_b64_tr_b16 a[148:149], v20 offset:4104\n\t"
      "ds_read_b64_tr_b16 a[150:151], v20 offset:5128\n\t"
      "ds_read_b64_tr_b16 a[152:153], v20 offset:6144\n\t"
      "ds_read_b64_tr_b16 a[154:155], v20 offset:7168\n\t"
      "ds_read_b64_tr_b16 a[156:157], v20 offset:6152\n\t"
      "ds_read_b64_tr_b16 a[158:159], v20 offset:7176\n\t"
      "s_waitcnt lgkmcnt(0)\n\t"
      "s_add_u32 m0, %[st], 4096\n\t"
      "s_mul_i32 s43, s57, 40960\n\t"
      "s_add_u32 s43, s43, %[cb]\n\t"
      "s_add_u32 s43, s43, 0\n\t"
      "buffer_load_dwordx4 v16, %[rw], s43 offen lds\n\t"
      "buffer_load_dwordx4 v17, %[rw], s43 offen offset:1024 lds\n\t"
      "s_add_u32 m0, %[st], 6144\n\t"
      "s_mul_i32 s43, s57, 40960\n\t"
      "s_add_u32 s43, s43, %[cb]\n\t"
      "s_add_u32 s43, s43, 64\n\t"
      "buffer_load_dwordx4 v16, %[rw], s43 offen lds\n\t"
      "buffer_load_dwordx4 v17, %[rw], s43 offen offset:1024 lds\n\t"
      "s_waitcnt vmcnt(12)\n\t"
      "ds_read_b64_tr_b16 a[160:161], v20 offset:8192\n\t"
      "ds_read_b64_tr_b16 a[162:163], v20 offset:9216\n\t"
      "ds_read_b64_tr_b16 a[164:165], v20 offset:8200\n\t"
      "ds_read_b64_tr_b16 a[166:167], v20 offset:9224\n\t"
      "ds_read_b64_tr_b16 a[168:169], v20 offset:10240\n\t"
      "ds_read_b64_tr_b16 a[170:171], v20 offset:11264\n\t"
      "ds_read_b64_tr_b16 a[172:173], v20 offset:10248\n\t"
      "ds_read_b64_tr_b16 a[174:175], v20 offset:11272\n\t"
      "s_waitcnt lgkmcnt(0)\n\t"
      "s_waitcnt vmcnt(8)\n\t"
      "ds_read_b64_tr_b16 a[176:177], v20 offset:12288\n\t"
      "ds_read_b64_tr_b16 a[178:179], v20 offset:13312\n\t"
      "ds_read_b64_tr_b16 a[180:181], v20 offset:12296\n\t"
      "ds_read_b64_tr_b16 a[182:183], v20 offset:13320\n\t"
      "ds_read_b64_tr_b16 a[184:185], v20 offset:14336\n\t"
      "ds_read_b64_tr_b16 a[186:187], v20 offset:15360\n\t"
      "ds_read_b64_tr_b16 a[188:189], v20 offset:14344\n\t"
      "ds_read_b64_tr_b16 a[190:191], v20 offset:15368\n\t"
      "s_waitcnt lgkmcnt(0)\n\t"
      "s_waitcnt vmcnt(4)\n\t"
      "ds_read_b64_tr_b16 a[192:193], v20 offset:0\n\t"
      "ds_read_b64_tr_b16 a[194:195], v20 offset:1024\n\t"
      "ds_read_b64_tr_b16 a[196:197], v20 offset:8\n\t"
      "ds_read_b64_tr_b16 a[198:199], v20 offset:1032\n\t"
      "ds_read_b64_tr_b16 a[200:201], v20 offset:2048\n\t"
      "ds_read_b64_tr_b16 a[202:203], v20 offset:3072\n\t"
      "ds_read_b64_tr_b16 a[204:205], v20 offset:2056\n\t"
      "ds_read_b64_tr_b16 a[206:207], v20 offset:3080\n\t"
      "s_waitcnt lgkmcnt(0)\n\t"
      "s_waitcnt vmcnt(0)\n\t"
      "ds_read_b64_tr_b16 a[208:209], v20 offset:4096\n\t"
      "ds_read_b64_tr_b16 a[210:211], v20 offset:5120\n\t"
      "ds_read_b64_tr_b16 a[212:213], v20 offset:4104\n\t"
      "ds_read_b64_tr_b16 a[214:215], v20 offset:5128\n\t"
      "ds_read_b64_tr_b16 a[216:217], v20 offset:6144\n\t"
      "ds_read_b64_tr_b16 a[218:219], v20 offset:7168\n\t"
      "ds_read_b64_tr_b16 a[220:221], v20 offset:6152\n\t"
      "ds_read_b64_tr_b16 a[222:223], v20 offset:7176\n\t"
      "s_waitcnt lgkmcnt(0)\n\t"
      "s_waitcnt lgkmcnt(0)\n\t"
      "s_barrier\n\t"
      "s_cmp_lt_u32 %[w], 2\n\t"
      "s_cbranch_scc1 LZ%=\n\t"
      "s_cmp_eq_u32 %[rot], 0\n\t"
      "s_cbranch_scc1 LZ0%=\n\t"
      "v_accvgpr_write_b32 a112, 0\n\t"
      "v_accvgpr_write_b32 a113, 0\n\t"
      "v_accvgpr_write_b32 a114, 0\n\t"
      "v_accvgpr_write_b32 a115, 0\n\t"
      "v_accvgpr_write_b32 a116, 0\n\t"
      "v_accvgpr_write_b32 a117, 0\n\t"
      "v_accvgpr_write_b32 a118, 0\n\t"
      "v_accvgpr_write_b32 a119, 0\n\t"
      "v_accvgpr_write_b32 a120, 0\n\t"
      "v_accvgpr_write_b32 a121, 0\n\t"
      "v_accvgpr_write_b32 a122, 0\n\t"
      "v_accvgpr_write_b32 a123, 0\n\t"
      "v_accvgpr_write_b32 a124, 0\n\t"
      "v_accvgpr_write_b32 a125, 0\n\t"
      "v_accvgpr_write_b32 a126, 0\n\t"
      "v_accvgpr_write_b32 a127, 0\n\t"
      "s_branch LZ%=\n\t"
      "LZ0%=:\n\t"
      "v_accvgpr_write_b32 a0, 0\n\t"
      "v_accvgpr_write_b32 a1, 0\n\t"
      "v_accvgpr_write_b32 a2, 0\n\t"
      "v_accvgpr_write_b32 a3, 0\n\t"
      "v_accvgpr_write_b32 a4, 0\n\t"
      "v_accvgpr_write_b32 a5, 0\n\t"
      "v_accvgpr_write_b32 a6, 0\n\t"
      "v_accvgpr_write_b32 a7, 0\n\t"
      "v_accvgpr_write_b32 a8, 0\n\t"
      "v_accvgpr_write_b32 a9, 0\n\t"
      "v_accvgpr_write_b32 a10, 0\n\t"
      "v_accvgpr_write_b32 a11, 0\n\t"
      "v_accvgpr_write_b32 a12, 0\n\t"
      "v_accvgpr_write_b32 a13, 0\n\t"
      "v_accvgpr_write_b32 a14, 0\n\t"
      "v_accvgpr_write_b32 a15, 0\n\t"
      "LZ%=:\n\t"
      "s_lshl_b32 s44, s44, 10\n\t"
      "s_lshl_b32 s45, s45, 10\n\t"
      "s_lshl_b32 s46, s46, 10\n\t"
      "s_lshl_b32 s47, s47, 10\n\t"
      "s_lshl_b32 s48, s48, 10\n\t"
      "s_lshl_b32 s49, s49, 10\n\t"
      "s_lshl_b32 s50, s50, 10\n\t"
      "s_lshl_b32 s51, s51, 10\n\t"
      "s_lshl_b32 s52, s52, 10\n\t"
      "s_lshl_b32 s53, s53, 10\n\t"
      "s_lshl_b32 s54, s54, 10\n\t"
      "s_lshl_b32 s55, s55, 10\n\t"
      "s_lshl_b32 s56, s56, 10\n\t"
      "s_lshl_b32 s57, s57, 10\n\t"
      "s_mov_b32 s40, 0\n\t"
      "s_mul_i32 s36, %[sw], 20480\n\t"
      "s_sub_u32 s37, 20480, s36\n\t"
      "s_add_u32 s60, %[hb], s36\n\t"
      "s_add_u32 s61, %[hb], s37\n\t"
      "s_mov_b32 s41, 0\n\t"
      "s_mov_b32 s36, 0\n\t"
      "LOOP%=:\n\t"
      "s_mov_b32 s42, 0\n\t"
      "s_add_u32 s58, s40, 1\n\t"
      "s_cmp_lg_u32 s41, 0\n\t"
      "s_cbranch_scc1 LHAVE%=\n\t"
      "s_mov_b64 exec, 0xff\n\t"
      "buffer_load_dwordx4 v[64:67], v1, %[rs], 0 offen sc1\n\t"
      "s_nop 11\n\t"
      "buffer_load_dwordx4 v[68:71], v1, %[rs], 0 offen sc1\n\t"
      "s_nop 11\n\t"
      "buffer_load_dwordx4 v[72:75], v1, %[rs], 0 offen sc1\n\t"
      "s_nop 11\n\t"
      "buffer_load_dwordx4 v[76:79], v1, %[rs], 0 offen sc1\n\t"
      "s_nop 11\n\t"
      "LEW%=:\n\t"
      "s_waitcnt vmcnt(3)\n\t"
      "v_min3_i32 v8, v64, v65, v66\n\t"
      "v_min_i32 v8, v8, v67\n\t"
      "v_cmp_gt_i32 vcc, s40, v8\n\t"
      "s_cbranch_vccz LEWD%=\n\t"
      "buffer_load_dwordx4 v[64:67], v1, %[rs], 0 offen sc1\n\t"
      "s_nop 11\n\t"
      "s_waitcnt vmcnt(3)\n\t"
      "v_min3_i32 v8, v68, v69, v70\n\t"
      "v_min_i32 v8, v8, v71\n\t"
      "v_cmp_gt_i32 vcc, s40, v8\n\t"
      "s_cbranch_vccz LEWD%=\n\t"
      "buffer_load_dwordx4 v[68:71], v1, %[rs], 0 offen sc1\n\t"
      "s_nop 11\n\t"
      "s_waitcnt vmcnt(3)\n\t"
      "v_min3_i32 v8, v72, v73, v74\n\t"
      "v_min_i32 v8, v8, v75\n\t"
      "v_cmp_gt_i32 vcc, s40, v8\n\t"
      "s_cbranch_vccz LEWD%=\n\t"
      "buffer_load_dwordx4 v[72:75], v1, %[rs], 0 offen sc1\n\t"
      "s_nop 11\n\t"
      "s_waitcnt vmcnt(3)\n\t"
      "v_min3_i32 v8, v76, v77, v78\n\t"
      "v_min_i32 v8, v8, v79\n\t"
      "v_cmp_gt_i32 vcc, s40, v8\n\t"
      "s_cbranch_vccz LEWD%=\n\t"
      "buffer_load_dwordx4 v[76:79], v1, %[rs], 0 offen sc1\n\t"
      "s_nop 11\n\t"
      "s_add_u32 s42, s42, 1\n\t"
      "s_cmp_lt_u32 s42, 400000\n\t"
      "s_cbranch_scc1 LEW%=\n\t"
      "s_branch LFAIL%=\n\t"
      "LEWD%=:\n\t"
      "s_mov_b64 exec, -1\n\t"
      "s_mov_b32 s42, 0\n\t"
      "LPOLL%=:\n\t"
      "buffer_load_dwordx4 v[48:51], v2, %[rs], s60 offen offset:0 sc1\n\t"
      "buffer_load_dwordx4 v[52:55], v2, %[rs], s60 offen offset:1024 sc1\n\t"
      "buffer_load_dwordx4 v[56:59], v2, %[rs], s61 offen offset:0 sc1\n\t"
      "buffer_load_dwordx4 v[60:63], v2, %[rs], s61 offen offset:1024 sc1\n\t"
      "buffer_load_dwordx4 v[64:67], v0, %[rs], s44 offen sc1\n\t"
      "s_nop 10\n\t"
      "buffer_load_dwordx4 v[68:71], v0, %[rs], s45 offen sc1\n\t"
      "s_nop 10\n\t"
      "buffer_load_dwordx4 v[72:75], v0, %[rs], s46 offen sc1\n\t"
      "s_nop 10\n\t"
      "buffer_load_dwordx4 v[76:79], v0, %[rs], s47 offen sc1\n\t"
      "s_nop 10\n\t"
      "buffer_load_dwordx4 v[80:83], v0, %[rs], s48 offen sc1\n\t"
      "s_nop 10\n\t"
      "buffer_load_dwordx4 v[84:87], v0, %[rs], s49 offen sc1\n\t"
      "s_nop 10\n\t"
      "buffer_load_dwordx4 v[88:91], v0, %[rs], s50 offen sc1\n\t"
      "s_nop 10\n\t"
      "buffer_load_dwordx4 v[92:95], v0, %[rs], s51 offen sc1\n\t"
      "s_nop 10\n\t"
      "LHAVE%=:\n\t"
      "v_mov_b32 v8, 0\n\t"
      "s_add_u32 s42, s42, 1\n\t"
      "s_cmp_gt_u32 s42, 400000\n\t"
      "s_cbranch_scc1 LFAIL%=\n\t"
      "s_waitcnt vmcnt(8)\n\t"
      "v_lshlrev_b32 v16, 16, v48\n\t"
      "v_and_b32 v17, 0xffff0000, v48\n\t"
      "v_lshlrev_b32 v18, 16, v49\n\t"
      "v_and_b32 v19, 0xffff0000, v49\n\t"
      "v_lshlrev_b32 v20, 16, v50\n\t"
      "v_and_b32 v21, 0xffff0000, v50\n\t"
      "v_lshlrev_b32 v22, 16, v51\n\t"
      "v_and_b32 v23, 0xffff0000, v51\n\t"
      "v_lshlrev_b32 v24, 16, v52\n\t"
      "v_and_b32 v25, 0xffff0000, v52\n\t"
      "v_lshlrev_b32 v26, 16, v53\n\t"
      "v_and_b32 v27, 0xffff0000, v53\n\t"
      "v_lshlrev_b32 v28, 16, v54\n\t"
      "v_and_b32 v29, 0xffff0000, v54\n\t"
      "v_lshlrev_b32 v30, 16, v55\n\t"
      "v_and_b32 v31, 0xffff0000, v55\n\t"
      "buffer_load_dwordx4 v[96:99], v0, %[rs], s52 offen sc1\n\t"
      "s_waitcnt vmcnt(8)\n\t"
      "v_mfma_f32_16x16x32_bf16 v[16:19], a[0:3], v[64:67], v[16:19]\n\t"
      "v_max3_u32 v8, v8, v64, v65\n\t"
      "v_mfma_f32_16x16x32_bf16 v[20:23], a[4:7], v[64:67], v[20:23]\n\t"
      "v_max3_u32 v8, v8, v66, v67\n\t"
      "v_mfma_f32_16x16x32_bf16 v[24:27], a[8:11], v[64:67], v[24:27]\n\t"
      "v_lshlrev_b32 v32, 16, v56\n\t"
      "v_and_b32 v33, 0xffff0000, v56\n\t"
      "v_mfma_f32_16x16x32_bf16 v[28:31], a[12:15], v[64:67], v[28:31]\n\t"
      "v_lshlrev_b32 v34, 16, v57\n\t"
      "v_and_b32 v35, 0xffff0000, v57\n\t"
      "buffer_load_dwordx4 v[100:103], v0, %[rs], s53 offen sc1\n\t"
      "s_waitcnt vmcnt(8)\n\t"
      "v_mfma_f32_16x16x32_bf16 v[16:19], a[16:19], v[68:71], v[16:19]\n\t"
      "v_max3_u32 v8, v8, v68, v69\n\t"
      "v_mfma_f32_16x16x32_bf16 v[20:23], a[20:23], v[68:71], v[20:23]\n\t"
      "v_max3_u32 v8, v8, v70, v71\n\t"
      "v_mfma_f32_16x16x32_bf16 v[24:27], a[24:27], v[68:71], v[24:27]\n\t"
      "v_lshlrev_b32 v36, 16, v58\n\t"
      "v_and_b32 v37, 0xffff0000, v58\n\t"
      "v_mfma_f32_16x16x32_bf16 v[28:31], a[28:31], v[68:71], v[28:31]\n\t"
      "v_lshlrev_b32 v38, 16, v59\n\t"
      "v_and_b32 v39, 0xffff0000, v59\n\t"
      "buffer_load_dwordx4 v[104:107], v0, %[rs], s54 offen sc1\n\t"
      "s_waitcnt vmcnt(8)\n\t"
      "v_mfma_f32_16x16x32_bf16 v[16:19], a[32:35], v[72:75], v[16:19]\n\t"
      "v_max3_u32 v8, v8, v72, v73\n\t"
      "v_mfma_f32_16x16x32_bf16 v[20:23], a[36:39], v[72:75], v[20:23]\n\t"
      "v_max3_u32 v8, v8, v74, v75\n\t"
      "v_mfma_f32_16x16x32_bf16 v[24:27], a[40:43], v[72:75], v[24:27]\n\t"
      "v_lshlrev_b32 v40, 16, v60\n\t"
      "v_and_b32 v41, 0xffff0000, v60\n\t"
      "v_mfma_f32_16x16x32_bf16 v[28:31], a[44:47], v[72:75], v[28:31]\n\t"
      "v_lshlrev_b32 v42, 16, v61\n\t"
      "v_and_b32 v43, 0xffff0000, v61\n\t"
      "buffer_load_dwordx4 v[108:111], v0, %[rs], s55 offen sc1\n\t"
      "s_waitcnt vmcnt(8)\n\t"
      "v_mfma_f32_16x16x32_bf16 v[16:19], a[48:51], v[76:79], v[16:19]\n\t"
      "v_max3_u32 v8, v8, v76, v77\n\t"
      "v_mfma_f32_16x16x32_bf16 v[20:23], a[52:55], v[76:79], v[20:23]\n\t"
      "v_max3_u32 v8, v8, v78, v79\n\t"
      "v_mfma_f32_16x16x32_bf16 v[24:27], a[56:59], v[76:79], v[24:27]\n\t"
      "v_lshlrev_b32 v44, 16, v62\n\t"
      "v_and_b32 v45, 0xffff0000, v62\n\t"
      "v_mfma_f32_16x16x32_bf16 v[28:31], a[60:63], v[76:79], v[28:31]\n\t"
      "v_lshlrev_b32 v46, 16, v63\n\t"
      "v_and_b32 v47, 0xffff0000, v63\n\t"
      "buffer_load_dwordx4 v[112:115], v0, %[rs], s56 offen sc1\n\t"
      "s_waitcnt vmcnt(8)\n\t"
      "v_mfma_f32_16x16x32_bf16 v[16:19], a[64:67], v[80:83], v[16:19]\n\t"
      "v_max3_u32 v8, v8, v80, v81\n\t"
      "v_mfma_f32_16x16x32_bf16 v[20:23], a[68:71], v[80:83], v[20:23]\n\t"
      "v_max3_u32 v8, v8, v82, v83\n\t"
      "v_mfma_f32_16x16x32_bf16 v[24:27], a[72:75], v[80:83], v[24:27]\n\t"
      "v_mfma_f32_16x16x32_bf16 v[28:31], a[76:79], v[80:83], v[28:31]\n\t"
      "buffer_load_dwordx4 v[116:119], v0, %[rs], s57 offen sc1\n\t"
      "s_waitcnt vmcnt(8)\n\t"
      "v_mfma_f32_16x16x32_bf16 v[16:19], a[80:83], v[84:87], v[16:19]\n\t"
      "v_max3_u32 v8, v8, v84, v85\n\t"
      "v_mfma_f32_16x16x32_bf16 v[20:23], a[84:87], v[84:87], v[20:23]\n\t"
      "v_max3_u32 v8, v8, v86, v87\n\t"
      "v_mfma_f32_16x16x32_bf16 v[24:27], a[88:91], v[84:87], v[24:27]\n\t"
      "v_mfma_f32_16x16x32_bf16 v[28:31], a[92:95], v[84:87], v[28:31]\n\t"
      "buffer_load_dwordx4 v[120:123], v6, %[rs], s44 offen sc1\n\t"
      "s_waitcnt vmcnt(8)\n\t"
      "v_mfma_f32_16x16x32_bf16 v[16:19], a[96:99], v[88:91], v[16:19]\n\t"
      "v_max3_u32 v8, v8, v88, v89\n\t"
      "v_mfma_f32_16x16x32_bf16 v[20:23], a[100:103], v[88:91], v[20:23]\n\t"
      "v_max3_u32 v8, v8, v90, v91\n\t"
      "v_mfma_f32_16x16x32_bf16 v[24:27], a[104:107], v[88:91], v[24:27]\n\t"
      "v_mfma_f32_16x16x32_bf16 v[28:31], a[108:111], v[88:91], v[28:31]\n\t"
      "buffer_load_dwordx4 v[124:127], v6, %[rs], s45 offen sc1\n\t"
      "s_waitcnt vmcnt(8)\n\t"
      "v_mfma_f32_16x16x32_bf16 v[16:19], a[112:115], v[92:95], v[16:19]\n\t"
      "v_max3_u32 v8, v8, v92, v93\n\t"
      "v_mfma_f32_16x16x32_bf16 v[20:23], a[116:119], v[92:95], v[20:23]\n\t"
      "v_max3_u32 v8, v8, v94, v95\n\t"
      "v_mfma_f32_16x16x32_bf16 v[24:27], a[120:123], v[92:95], v[24:27]\n\t"
      "v_mfma_f32_16x16x32_bf16 v[28:31], a[124:127], v[92:95], v[28:31]\n\t"
      "buffer_load_dwordx4 v[128:131], v6, %[rs], s46 offen sc1\n\t"
      "s_waitcnt vmcnt(8)\n\t"
      "v_mfma_f32_16x16x32_bf16 v[16:19], a[128:131], v[96:99], v[16:19]\n\t"
      "v_max3_u32 v8, v8, v96, v97\n\t"
      "v_mfma_f32_16x16x32_bf16 v[20:23], a[132:135], v[96:99], v[20:23]\n\t"
      "v_max3_u32 v8, v8, v98, v99\n\t"
      "v_mfma_f32_16x16x32_bf16 v[24:27], a[136:139], v[96:99], v[24:27]\n\t"
      "v_mfma_f32_16x16x32_bf16 v[28:31], a[140:143], v[96:99], v[28:31]\n\t"
      "buffer_load_dwordx4 v[132:135], v6, %[rs], s47 offen sc1\n\t"
      "s_waitcnt vmcnt(8)\n\t"
      "v_mfma_f32_16x16x32_bf16 v[16:19], a[144:147], v[100:103], v[16:19]\n\t"
      "v_max3_u32 v8, v8, v100, v101\n\t"
      "v_mfma_f32_16x16x32_bf16 v[20:23], a[148:151], v[100:103], v[20:23]\n\t"
      "v_max3_u32 v8, v8, v102, v103\n\t"
      "v_mfma_f32_16x16x32_bf16 v[24:27], a[152:155], v[100:103], v[24:27]\n\t"
      "v_mfma_f32_16x16x32_bf16 v[28:31], a[156:159], v[100:103], v[28:31]\n\t"
      "buffer_load_dwordx4 v[136:139], v6, %[rs], s48 offen sc1\n\t"
      "s_waitcnt vmcnt(8)\n\t"
      "v_mfma_f32_16x16x32_bf16 v[16:19], a[160:163], v[104:107], v[16:19]\n\t"
      "v_max3_u32 v8, v8, v104, v105\n\t"
      "v_mfma_f32_16x16x32_bf16 v[20:23], a[164:167], v[104:107], v[20:23]\n\t"
      "v_max3_u32 v8, v8, v106, v107\n\t"
      "v_mfma_f32_16x16x32_bf16 v[24:27], a[168:171], v[104:107], v[24:27]\n\t"
      "v_mfma_f32_16x16x32_bf16 v[28:31], a[172:175], v[104:107], v[28:31]\n\t"
      "buffer_load_dwordx4 v[140:143], v6, %[rs], s49 offen sc1\n\t"
      "s_mov_b64 exec, 0xff\n\t"
      "buffer_load_dwordx4 v[12:15], v1, %[rs], 0 offen sc1\n\t"
      "s_mov_b64 exec, -1\n\t"
      "s_waitcnt vmcnt(9)\n\t"
      "v_mfma_f32_16x16x32_bf16 v[16:19], a[176:179], v[108:111], v[16:19]\n\t"
      "v_max3_u32 v8, v8, v108, v109\n\t"
      "v_mfma_f32_16x16x32_bf16 v[20:23], a[180:183], v[108:111], v[20:23]\n\t"
      "v_max3_u32 v8, v8, v110, v111\n\t"
      "v_mfma_f32_16x16x32_bf16 v[24:27], a[184:187], v[108:111], v[24:27]\n\t"
      "v_mfma_f32_16x16x32_bf16 v[28:31], a[188:191], v[108:111], v[28:31]\n\t"
      "buffer_load_dwordx4 v[144:147], v6, %[rs], s50 offen sc1\n\t"
      "s_waitcnt vmcnt(9)\n\t"
      "v_mfma_f32_16x16x32_bf16 v[16:19], a[192:195], v[112:115], v[16:19]\n\t"
      "v_max3_u32 v8, v8, v112, v113\n\t"
      "v_mfma_f32_16x16x32_bf16 v[20:23], a[196:199], v[112:115], v[20:23]\n\t"
      "v_max3_u32 v8, v8, v114, v115\n\t"
      "v_mfma_f32_16x16x32_bf16 v[24:27], a[200:203], v[112:115], v[24:27]\n\t"
      "v_mfma_f32_16x16x32_bf16 v[28:31], a[204:207], v[112:115], v[28:31]\n\t"
      "buffer_load_dwordx4 v[148:151], v6, %[rs], s51 offen sc1\n\t"
      "s_waitcnt vmcnt(9)\n\t"
      "v_mfma_f32_16x16x32_bf16 v[16:19], a[208:211], v[116:119], v[16:19]\n\t"
      "v_max3_u32 v8, v8, v116, v117\n\t"
      "v_mfma_f32_16x16x32_bf16 v[20:23], a[212:215], v[116:119], v[20:23]\n\t"
      "v_max3_u32 v8, v8, v118, v119\n\t"
      "v_mfma_f32_16x16x32_bf16 v[24:27], a[216:219], v[116:119], v[24:27]\n\t"
      "v_mfma_f32_16x16x32_bf16 v[28:31], a[220:223], v[116:119], v[28:31]\n\t"
      "buffer_load_dwordx4 v[152:155], v6, %[rs], s52 offen sc1\n\t"
      "s_waitcnt vmcnt(9)\n\t"
      "v_mfma_f32_16x16x32_bf16 v[32:35], a[0:3], v[120:123], v[32:35]\n\t"
      "v_max3_u32 v8, v8, v120, v121\n\t"
      "v_mfma_f32_16x16x32_bf16 v[36:39], a[4:7], v[120:123], v[36:39]\n\t"
      "v_max3_u32 v8, v8, v122, v123\n\t"
      "v_mfma_f32_16x16x32_bf16 v[40:43], a[8:11], v[120:123], v[40:43]\n\t"
      "v_mfma_f32_16x16x32_bf16 v[44:47], a[12:15], v[120:123], v[44:47]\n\t"
      "buffer_load_dwordx4 v[156:159], v6, %[rs], s53 offen sc1\n\t"
      "s_waitcnt vmcnt(9)\n\t"
      "v_mfma_f32_16x16x32_bf16 v[32:35], a[16:19], v[124:127], v[32:35]\n\t"
      "ds_write_b128 v3, v[16:19] offset:0\n\t"
      "v_max3_u32 v8, v8, v124, v125\n\t"
      "v_mfma_f32_16x16x32_bf16 v[36:39], a[20:23], v[124:127], v[36:39]\n\t"
      "v_max3_u32 v8, v8, v126, v127\n\t"
      "v_mfma_f32_16x16x32_bf16 v[40:43], a[24:27], v[124:127], v[40:43]\n\t"
      "v_mfma_f32_16x16x32_bf16 v[44:47], a[28:31], v[124:127], v[44:47]\n\t"
      "buffer_load_dwordx4 v[160:163], v6, %[rs], s54 offen sc1\n\t"
      "s_waitcnt vmcnt(9)\n\t"
      "v_mfma_f32_16x16x32_bf16 v[32:35], a[32:35], v[128:131], v[32:35]\n\t"
      "ds_write_b128 v3, v[20:23] offset:1024\n\t"
      "v_max3_u32 v8, v8, v128, v129\n\t"
      "v_mfma_f32_16x16x32_bf16 v[36:39], a[36:39], v[128:131], v[36:39]\n\t"
      "v_max3_u32 v8, v8, v130, v131\n\t"
      "v_mfma_f32_16x16x32_bf16 v[40:43], a[40:43], v[128:131], v[40:43]\n\t"
      "v_mfma_f32_16x16x32_bf16 v[44:47], a[44:47], v[128:131], v[44:47]\n\t"
      "buffer_load_dwordx4 v[164:167], v6, %[rs], s55 offen sc1\n\t"
      "s_waitcnt vmcnt(9)\n\t"
      "v_mfma_f32_16x16x32_bf16 v[32:35], a[48:51], v[132:135], v[32:35]\n\t"
      "ds_write_b128 v7, v[24:27] offset:2048\n\t"
      "v_max3_u32 v8, v8, v132, v133\n\t"
      "v_mfma_f32_16x16x32_bf16 v[36:39], a[52:55], v[132:135], v[36:39]\n\t"
      "v_max3_u32 v8, v8, v134, v135\n\t"
      "v_mfma_f32_16x16x32_bf16 v[40:43], a[56:59], v[132:135], v[40:43]\n\t"
      "v_mfma_f32_16x16x32_bf16 v[44:47], a[60:63], v[132:135], v[44:47]\n\t"
      "buffer_load_dwordx4 v[168:171], v6, %[rs], s56 offen sc1\n\t"
      "s_waitcnt vmcnt(9)\n\t"
      "v_mfma_f32_16x16x32_bf16 v[32:35], a[64:67], v[136:139], v[32:35]\n\t"
      "ds_write_b128 v7, v[28:31] offset:3072\n\t"
      "v_max3_u32 v8, v8, v136, v137\n\t"
      "v_mfma_f32_16x16x32_bf16 v[36:39], a[68:71], v[136:139], v[36:39]\n\t"
      "v_max3_u32 v8, v8, v138, v139\n\t"
      "v_mfma_f32_16x16x32_bf16 v[40:43], a[72:75], v[136:139], v[40:43]\n\t"
      "v_mfma_f32_16x16x32_bf16 v[44:47], a[76:79], v[136:139], v[44:47]\n\t"
      "buffer_load_dwordx4 v[172:175], v6, %[rs], s57 offen sc1\n\t"
      "s_waitcnt vmcnt(9)\n\t"
      "v_mfma_f32_16x16x32_bf16 v[32:35], a[80:83], v[140:143], v[32:35]\n\t"
      "v_max3_u32 v8, v8, v140, v141\n\t"
      "v_mfma_f32_16x16x32_bf16 v[36:39], a[84:87], v[140:143], v[36:39]\n\t"
      "v_max3_u32 v8, v8, v142, v143\n\t"
      "v_mfma_f32_16x16x32_bf16 v[40:43], a[88:91], v[140:143], v[40:43]\n\t"
      "v_mfma_f32_16x16x32_bf16 v[44:47], a[92:95], v[140:143], v[44:47]\n\t"
      "s_waitcnt vmcnt(7)\n\t"
      "v_mfma_f32_16x16x32_bf16 v[32:35], a[96:99], v[144:147], v[32:35]\n\t"
      "v_max3_u32 v8, v8, v144, v145\n\t"
      "v_mfma_f32_16x16x32_bf16 v[36:39], a[100:103], v[144:147], v[36:39]\n\t"
      "v_max3_u32 v8, v8, v146, v147\n\t"
      "v_mfma_f32_16x16x32_bf16 v[40:43], a[104:107], v[144:147], v[40:43]\n\t"
      "v_min3_i32 v9, v12, v13, v14\n\t"
      "v_min_i32 v9, v9, v15\n\t"
      "v_mfma_f32_16x16x32_bf16 v[44:47], a[108:111], v[144:147], v[44:47]\n\t"
      "v_cmp_gt_i32 vcc, s58, v9\n\t"
      "v_add_u32 v0, 110592, v0\n\t"
      "v_add_u32 v6, 110592, v6\n\t"
      "s_add_u32 s60, s60, 46080\n\t"
      "s_add_u32 s61, s61, 46080\n\t"
      "s_and_b32 s43, vcc_lo, 0xff\n\t"
      "s_cbranch_scc0 LPRE%=\n\t"
      "s_mov_b32 s41, 0\n\t"
      "s_waitcnt vmcnt(6)\n\t"
      "v_mfma_f32_16x16x32_bf16 v[32:35], a[112:115], v[148:151], v[32:35]\n\t"
      "v_max3_u32 v8, v8, v148, v149\n\t"
      "v_mfma_f32_16x16x32_bf16 v[36:39], a[116:119], v[148:151], v[36:39]\n\t"
      "v_max3_u32 v8, v8, v150, v151\n\t"
      "v_mfma_f32_16x16x32_bf16 v[40:43], a[120:123], v[148:151], v[40:43]\n\t"
      "v_mfma_f32_16x16x32_bf16 v[44:47], a[124:127], v[148:151], v[44:47]\n\t"
      "s_waitcnt vmcnt(5)\n\t"
      "v_mfma_f32_16x16x32_bf16 v[32:35], a[128:131], v[152:155], v[32:35]\n\t"
      "v_max3_u32 v8, v8, v152, v153\n\t"
      "v_mfma_f32_16x16x32_bf16 v[36:39], a[132:135], v[152:155], v[36:39]\n\t"
      "v_max3_u32 v8, v8, v154, v155\n\t"
      "v_mfma_f32_16x16x32_bf16 v[40:43], a[136:139], v[152:155], v[40:43]\n\t"
      "v_mfma_f32_16x16x32_bf16 v[44:47], a[140:143], v[152:155], v[44:47]\n\t"
      "s_waitcnt vmcnt(4)\n\t"
      "v_mfma_f32_16x16x32_bf16 v[32:35], a[144:147], v[156:159], v[32:35]\n\t"
      "v_max3_u32 v8, v8, v156, v157\n\t"
      "v_mfma_f32_16x16x32_bf16 v[36:39], a[148:151], v[156:159], v[36:39]\n\t"
      "v_max3_u32 v8, v8, v158, v159\n\t"
      "v_mfma_f32_16x16x32_bf16 v[40:43], a[152:155], v[156:159], v[40:43]\n\t"
      "v_mfma_f32_16x16x32_bf16 v[44:47], a[156:159], v[156:159], v[44:47]\n\t"
      "s_waitcnt vmcnt(3)\n\t"
      "v_mfma_f32_16x16x32_bf16 v[32:35], a[160:163], v[160:163], v[32:35]\n\t"
      "v_max3_u32 v8, v8, v160, v161\n\t"
      "v_mfma_f32_16x16x32_bf16 v[36:39], a[164:167], v[160:163], v[36:39]\n\t"
      "v_max3_u32 v8, v8, v162, v163\n\t"
      "v_mfma_f32_16x16x32_bf16 v[40:43], a[168:171], v[160:163], v[40:43]\n\t"
      "v_mfma_f32_16x16x32_bf16 v[44:47], a[172:175], v[160:163], v[44:47]\n\t"
      "s_waitcnt vmcnt(2)\n\t"
      "v_mfma_f32_16x16x32_bf16 v[32:35], a[176:179], v[164:167], v[32:35]\n\t"
      "v_max3_u32 v8, v8, v164, v165\n\t"
      "v_mfma_f32_16x16x32_bf16 v[36:39], a[180:183], v[164:167], v[36:39]\n\t"
      "v_max3_u32 v8, v8, v166, v167\n\t"
      "v_mfma_f32_16x16x32_bf16 v[40:43], a[184:187], v[164:167], v[40:43]\n\t"
      "v_mfma_f32_16x16x32_bf16 v[44:47], a[188:191], v[164:167], v[44:47]\n\t"
      "s_waitcnt vmcnt(1)\n\t"
      "v_mfma_f32_16x16x32_bf16 v[32:35], a[192:195], v[168:171], v[32:35]\n\t"
      "v_max3_u32 v8, v8, v168, v169\n\t"
      "v_mfma_f32_16x16x32_bf16 v[36:39], a[196:199], v[168:171], v[36:39]\n\t"
      "v_max3_u32 v8, v8, v170, v171\n\t"
      "v_mfma_f32_16x16x32_bf16 v[40:43], a[200:203], v[168:171], v[40:43]\n\t"
      "v_mfma_f32_16x16x32_bf16 v[44:47], a[204:207], v[168:171], v[44:47]\n\t"
      "s_waitcnt vmcnt(0)\n\t"
      "v_mfma_f32_16x16x32_bf16 v[32:35], a[208:211], v[172:175], v[32:35]\n\t"
      "v_max3_u32 v8, v8, v172, v173\n\t"
      "v_mfma_f32_16x16x32_bf16 v[36:39], a[212:215], v[172:175], v[36:39]\n\t"
      "v_max3_u32 v8, v8, v174, v175\n\t"
      "v_mfma_f32_16x16x32_bf16 v[40:43], a[216:219], v[172:175], v[40:43]\n\t"
      "v_cmp_eq_u32 vcc, -1, v8\n\t"
      "v_mfma_f32_16x16x32_bf16 v[44:47], a[220:223], v[172:175], v[44:47]\n\t"
      "LJOIN%=:\n\t"
      "s_cbranch_vccnz LRETRY%=\n\t"
      "ds_write_b128 v3, v[32:35] offset:16384\n\t"
      "ds_write_b128 v3, v[36:39] offset:17408\n\t"
      "s_nop 0\n\t"
      "ds_write_b128 v7, v[40:43] offset:18432\n\t"
      "s_nop 2\n\t"
      "ds_write_b128 v7, v[44:47] offset:19456\n\t"
      "s_waitcnt lgkmcnt(0)\n\t"
      "s_barrier\n\t"
      "ds_read_b128 v[16:19], v4 offset:0\n\t"
      "ds_read_b128 v[20:23], v4 offset:4096\n\t"
      "ds_read_b128 v[24:27], v4 offset:8192\n\t"
      "ds_read_b128 v[28:31], v4 offset:12288\n\t"
      "ds_read_b128 v[32:35], v4 offset:1024\n\t"
      "ds_read_b128 v[36:39], v4 offset:5120\n\t"
      "ds_read_b128 v[40:43], v4 offset:9216\n\t"
      "ds_read_b128 v[44:47], v4 offset:13312\n\t"
      "s_waitcnt lgkmcnt(6)\n\t"
      "v_pk_add_f32 v[16:17], v[16:17], v[20:21]\n\t"
      "v_pk_add_f32 v[18:19], v[18:19], v[22:23]\n\t"
      "s_waitcnt lgkmcnt(4)\n\t"
      "v_pk_add_f32 v[24:25], v[24:25], v[28:29]\n\t"
      "v_pk_add_f32 v[26:27], v[26:27], v[30:31]\n\t"
      "v_pk_add_f32 v[16:17], v[16:17], v[24:25]\n\t"
      "v_pk_add_f32 v[18:19], v[18:19], v[26:27]\n\t"
      "v_cvt_pk_bf16_f32 v12, v16, v17\n\t"
      "v_cvt_pk_bf16_f32 v13, v18, v19\n\t"
      "s_waitcnt lgkmcnt(2)\n\t"
      "v_pk_add_f32 v[32:33], v[32:33], v[36:37]\n\t"
      "v_pk_add_f32 v[34:35], v[34:35], v[38:39]\n\t"
      "s_waitcnt lgkmcnt(0)\n\t"
      "v_pk_add_f32 v[40:41], v[40:41], v[44:45]\n\t"
      "v_pk_add_f32 v[42:43], v[42:43], v[46:47]\n\t"
      "v_pk_add_f32 v[32:33], v[32:33], v[40:41]\n\t"
      "v_pk_add_f32 v[34:35], v[34:35], v[42:43]\n\t"
      "v_cvt_pk_bf16_f32 v14, v32, v33\n\t"
      "v_cvt_pk_bf16_f32 v15, v34, v35\n\t"
      "buffer_store_dwordx4 v[12:15], v5, %[ro], s36 offen\n\t"
      "v_xor_b32 v3, 32768, v3\n\t"
      "v_xor_b32 v4, 32768, v4\n\t"
      "v_xor_b32 v7, 32768, v7\n\t"
      "s_add_u32 s36, s36, 40960\n\t"
      "s_add_u32 s40, s40, 1\n\t"
      "s_cmp_lt_u32 s40, 63\n\t"
      "s_cbranch_scc1 LOOP%=\n\t"
      "s_branch LEND%=\n\t"
      "LRETRY%=:\n\t"
      "v_subrev_u32 v0, 110592, v0\n\t"
      "v_subrev_u32 v6, 110592, v6\n\t"
      "s_sub_u32 s60, s60, 46080\n\t"
      "s_sub_u32 s61, s61, 46080\n\t"
      "s_nop 7\n\t"
      "s_branch LPOLL%=\n\t"
      "LPRE%=:\n\t"
      "s_mov_b32 s41, 1\n\t"
      "buffer_load_dwordx4 v[48:51], v2, %[rs], s60 offen offset:0 sc1\n\t"
      "s_waitcnt vmcnt(7)\n\t"
      "v_mfma_f32_16x16x32_bf16 v[32:35], a[112:115], v[148:151], v[32:35]\n\t"
      "v_max3_u32 v8, v8, v148, v149\n\t"
      "v_mfma_f32_16x16x32_bf16 v[36:39], a[116:119], v[148:151], v[36:39]\n\t"
      "v_max3_u32 v8, v8, v150, v151\n\t"
      "v_mfma_f32_16x16x32_bf16 v[40:43], a[120:123], v[148:151], v[40:43]\n\t"
      "v_mfma_f32_16x16x32_bf16 v[44:47], a[124:127], v[148:151], v[44:47]\n\t"
      "buffer_load_dwordx4 v[52:55], v2, %[rs], s60 offen offset:1024 sc1\n\t"
      "buffer_load_dwordx4 v[56:59], v2, %[rs], s61 offen offset:0 sc1\n\t"
      "s_waitcnt vmcnt(8)\n\t"
      "v_mfma_f32_16x16x32_bf16 v[32:35], a[128:131], v[152:155], v[32:35]\n\t"
      "v_max3_u32 v8, v8, v152, v153\n\t"
      "v_mfma_f32_16x16x32_bf16 v[36:39], a[132:135], v[152:155], v[36:39]\n\t"
      "v_max3_u32 v8, v8, v154, v155\n\t"
      "v_mfma_f32_16x16x32_bf16 v[40:43], a[136:139], v[152:155], v[40:43]\n\t"
      "v_mfma_f32_16x16x32_bf16 v[44:47], a[140:143], v[152:155], v[44:47]\n\t"
      "buffer_load_dwordx4 v[60:63], v2, %[rs], s61 offen offset:1024 sc1\n\t"
      "buffer_load_dwordx4 v[64:67], v0, %[rs], s44 offen sc1\n\t"
      "s_waitcnt vmcnt(9)\n\t"
      "v_mfma_f32_16x16x32_bf16 v[32:35], a[144:147], v[156:159], v[32:35]\n\t"
      "v_max3_u32 v8, v8, v156, v157\n\t"
      "v_mfma_f32_16x16x32_bf16 v[36:39], a[148:151], v[156:159], v[36:39]\n\t"
      "v_max3_u32 v8, v8, v158, v159\n\t"
      "v_mfma_f32_16x16x32_bf16 v[40:43], a[152:155], v[156:159], v[40:43]\n\t"
      "v_mfma_f32_16x16x32_bf16 v[44:47], a[156:159], v[156:159], v[44:47]\n\t"
      "buffer_load_dwordx4 v[68:71], v0, %[rs], s45 offen sc1\n\t"
      "s_waitcnt vmcnt(9)\n\t"
      "v_mfma_f32_16x16x32_bf16 v[32:35], a[160:163], v[160:163], v[32:35]\n\t"
      "v_max3_u32 v8, v8, v160, v161\n\t"
      "v_mfma_f32_16x16x32_bf16 v[36:39], a[164:167], v[160:163], v[36:39]\n\t"
      "v_max3_u32 v8, v8, v162, v163\n\t"
      "v_mfma_f32_16x16x32_bf16 v[40:43], a[168:171], v[160:163], v[40:43]\n\t"
      "v_mfma_f32_16x16x32_bf16 v[44:47], a[172:175], v[160:163], v[44:47]\n\t"
      "buffer_load_dwordx4 v[72:75], v0, %[rs], s46 offen sc1\n\t"
      "buffer_load_dwordx4 v[76:79], v0, %[rs], s47 offen sc1\n\t"
      "s_waitcnt vmcnt(10)\n\t"
      "v_mfma_f32_16x16x32_bf16 v[32:35], a[176:179], v[164:167], v[32:35]\n\t"
      "v_max3_u32 v8, v8, v164, v165\n\t"
      "v_mfma_f32_16x16x32_bf16 v[36:39], a[180:183], v[164:167], v[36:39]\n\t"
      "v_max3_u32 v8, v8, v166, v167\n\t"
      "v_mfma_f32_16x16x32_bf16 v[40:43], a[184:187], v[164:167], v[40:43]\n\t"
      "v_mfma_f32_16x16x32_bf16 v[44:47], a[188:191], v[164:167], v[44:47]\n\t"
      "buffer_load_dwordx4 v[80:83], v0, %[rs], s48 offen sc1\n\t"
      "buffer_load_dwordx4 v[84:87], v0, %[rs], s49 offen sc1\n\t"
      "s_waitcnt vmcnt(11)\n\t"
      "v_mfma_f32_16x16x32_bf16 v[32:35], a[192:195], v[168:171], v[32:35]\n\t"
      "v_max3_u32 v8, v8, v168, v169\n\t"
      "v_mfma_f32_16x16x32_bf16 v[36:39], a[196:199], v[168:171], v[36:39]\n\t"
      "v_max3_u32 v8, v8, v170, v171\n\t"
      "v_mfma_f32_16x16x32_bf16 v[40:43], a[200:203], v[168:171], v[40:43]\n\t"
      "v_mfma_f32_16x16x32_bf16 v[44:47], a[204:207], v[168:171], v[44:47]\n\t"
      "buffer_load_dwordx4 v[88:91], v0, %[rs], s50 offen sc1\n\t"
      "buffer_load_dwordx4 v[92:95], v0, %[rs], s51 offen sc1\n\t"
      "s_waitcnt vmcnt(12)\n\t"
      "v_mfma_f32_16x16x32_bf16 v[32:35], a[208:211], v[172:175], v[32:35]\n\t"
      "v_max3_u32 v8, v8, v172, v173\n\t"
      "v_mfma_f32_16x16x32_bf16 v[36:39], a[212:215], v[172:175], v[36:39]\n\t"
      "v_max3_u32 v8, v8, v174, v175\n\t"
      "v_mfma_f32_16x16x32_bf16 v[40:43], a[216:219], v[172:175], v[40:43]\n\t"
      "v_cmp_eq_u32 vcc, -1, v8\n\t"
      "v_mfma_f32_16x16x32_bf16 v[44:47], a[220:223], v[172:175], v[44:47]\n\t"
      "s_branch LJOIN%=\n\t"
      "LFAIL%=:\n\t"
      "v_mov_b32 v8, 1\n\t"
      "s_mov_b32 s43, 688128\n\t"
      "buffer_store_dword v8, off, %[rs], s43 sc1\n\t"
      "LEND%=:\n\t"
      "s_waitcnt vmcnt(0) lgkmcnt(0)\n\t"
      "s_mov_b64 exec, -1\n\t"
      "s_mov_b32 m0, s39\n\t"
      : "+{v[0:7]}"(ad), "+{v[16:23]}"(ld)
      : [rs] "s"(rs), [ro] "s"(ro), [rw] "s"(rw), [w] "s"(w), [st] "s"(uni(stage)), [cb] "s"(uni(k0 * 32 * (DM * 2) + cg * 128)),
        [hb] "s"(uni(WS_H + cg * 2048)), [sw] "s"(cg & 1), [rot] "s"(7 * ((cg >> 1) & 1))
      : "memory", "vcc", "scc", "v8", "v9", "v10", "v11", "v12", "v13", "v14", "v15", "v24", "v25", "v26", "v27", "v28", "v29", "v30", "v31", "v32", "v33",
        "v34", "v35", "v36", "v37", "v38", "v39", "v40", "v41", "v42", "v43", "v44", "v45", "v46", "v47", "v48", "v49", "v50", "v51", "v52", "v53", "v54",
        "v55", "v56", "v57", "v58", "v59", "v60", "v61", "v62", "v63", "v64", "v65", "v66", "v67", "v68", "v69", "v70", "v71", "v72", "v73", "v74", "v75",
        "v76", "v77", "v78", "v79", "v80", "v81", "v82", "v83", "v84", "v85", "v86", "v87", "v88", "v89", "v90", "v91", "v92", "v93", "v94", "v95", "v96",
        "v97", "v98", "v99", "v100", "v101", "v102", "v103", "v104", "v105", "v106", "v107", "v108", "v109", "v110", "v111", "v112", "v113", "v114", "v115",
        "v116", "v117", "v118", "v119", "v120", "v121", "v122", "v123", "v124", "v125", "v126", "v127", "v128", "v129", "v130", "v131", "v132", "v133",
        "v134", "v135", "v136", "v137", "v138", "v139", "v140", "v141", "v142", "v143", "v144", "v145", "v146", "v147", "v148", "v149", "v150", "v151",
        "v152", "v153", "v154", "v155", "v156", "v157", "v158", "v159", "v160", "v161", "v162", "v163", "v164", "v165", "v166", "v167", "v168", "v169",
        "v170", "v171", "v172", "v173", "v174", "v175", "a0", "a1", "a2", "a3", "a4", "a5", "a6", "a7", "a8", "a9", "a10", "a11", "a12", "a13", "a14", "a15",
        "a16", "a17", "a18", "a19", "a20", "a21", "a22", "a23", "a24", "a25", "a26", "a27", "a28", "a29", "a30", "a31", "a32", "a33", "a34", "a35", "a36",
        "a37", "a38", "a39", "a40", "a41", "a42", "a43", "a44", "a45", "a46", "a47", "a48", "a49", "a50", "a51", "a52", "a53", "a54", "a55", "a56", "a57",
        "a58", "a59", "a60", "a61", "a62", "a63", "a64", "a65", "a66", "a67", "a68", "a69", "a70", "a71", "a72", "a73", "a74", "a75", "a76", "a77", "a78",
        "a79", "a80", "a81", "a82", "a83", "a84", "a85", "a86", "a87", "a88", "a89", "a90", "a91", "a92", "a93", "a94", "a95", "a96", "a97", "a98", "a99",
        "a100", "a101", "a102", "a103", "a104", "a105", "a106", "a107", "a108", "a109", "a110", "a111", "a112", "a113", "a114", "a115", "a116", "a117",
        "a118", "a119", "a120", "a121", "a122", "a123", "a124", "a125", "a126", "a127", "a128", "a129", "a130", "a131", "a132", "a133", "a134", "a135",
        "a136", "a137", "a138", "a139", "a140", "a141", "a142", "a143", "a144", "a145", "a146", "a147", "a148", "a149", "a150", "a151", "a152", "a153",
        "a154", "a155", "a156", "a157", "a158", "a159", "a160", "a161", "a162", "a163", "a164", "a165", "a166", "a167", "a168", "a169", "a170", "a171",
        "a172", "a173", "a174", "a175", "a176", "a177", "a178", "a179", "a180", "a181", "a182", "a183", "a184", "a185", "a186", "a187", "a188", "a189",
        "a190", "a191", "a192", "a193", "a194", "a195", "a196", "a197", "a198", "a199", "a200", "a201", "a202", "a203", "a204", "a205", "a206", "a207",
        "a208", "a209", "a210", "a211", "a212", "a213", "a214", "a215", "a216", "a217", "a218", "a219", "a220", "a221", "a222", "a223", "s33", "s34", "s35",
        "s36", "s37", "s38", "s39", "s40", "s41", "s42", "s43", "s44", "s45", "s46", "s47", "s48", "s49", "s50", "s51", "s52", "s53", "s54", "s55", "s56",
        "s57", "s58", "s59", "s60", "s61", "s62", "s63", "s64", "s65", "s66", "s67"
  );
}

DEV void role_dl(const Params& p, LAS char* lds, unsigned flip, int mt, int cg) {
  const int lane = threadIdx.x & 63, w = uni(threadIdx.x >> 6), g = lane >> 4, c = lane & 15;
  const int k0 = (w < 2) ? 14 * w : 13 * w + 1;
  const unsigned lb = (unsigned)(size_t)lds, stage = lb + w * 32768;
  u8v ad, ld = loader_regs<RM_NAT>(lane, DM * 2, stage, 3);
  ad[0] = WS_ACT + (mt * 54 + k0) * 1024 + lane * 16;
  ad[1] = ring_phys(EW_LAST + min((k0 >> 1) + (lane & 7), 26) * 128, flip ^ 1);
  ad[2] = (w == 0) ? lane * 16 : DROP;
  const int row = 4 * w + g, pc = c >> 1;
  ad[3] = lb + w * 4096 + g * 256 + (c ^ (2 * g)) * 16;
  ad[4] = lb + (pc >> 2) * 2048 + (c & 1) * 1024 + (pc & 3) * 256 + (row ^ (2 * pc)) * 16;
  ad[5] = ((63 * 32 + 16 * mt + row) * (DM * 2) + cg * 128 + c * 8) | DROP;
  ad[6] = 0;
  ad[7] = ad[3] ^ 128;
  const u4 rs = mk_rs(p.ws, WS_END), ro = mk_rs(p.out, 2048 * DM * 2), rw = mk_rs(p.wd, FF * DM * 2);
  asm volatile(
      "s_add_u32 s43, %[rot], 0\n\t"
      "s_sub_u32 s60, s43, 14\n\t"
      "s_cselect_b32 s44, s43, s60\n\t"
      "s_add_u32 s43, %[rot], 1\n\t"
      "s_sub_u32 s60, s43, 14\n\t"
      "s_cselect_b32 s45, s43, s60\n\t"
      "s_add_u32 s43, %[rot], 2\n\t"
      "s_sub_u32 s60, s43, 14\n\t"
      "s_cselect_b32 s46, s43, s60\n\t"
      "s_add_u32 s43, %[rot], 3\n\t"
      "s_sub_u32 s60, s43, 14\n\t"
      "s_cselect_b32 s47, s43, s60\n\t"
      "s_add_u32 s43, %[rot], 4\n\t"
      "s_sub_u32 s60, s43, 14\n\t"
      "s_cselect_b32 s48, s43, s60\n\t"
      "s_add_u32 s43, %[rot], 5\n\t"
      "s_sub_u32 s60, s43, 14\n\t"
      "s_cselect_b32 s49, s43, s60\n\t"
      "s_add_u32 s43, %[rot], 6\n\t"
      "s_sub_u32 s60, s43, 14\n\t"
      "s_cselect_b32 s50, s43, s60\n\t"
      "s_add_u32 s43, %[rot], 7\n\t"
      "s_sub_u32 s60, s43, 14\n\t"
      "s_cselect_b32 s51, s43, s60\n\t"
      "s_add_u32 s43, %[rot], 8\n\t"
      "s_sub_u32 s60, s43, 14\n\t"
      "s_cselect_b32 s52, s43, s60\n\t"
      "s_add_u32 s43, %[rot], 9\n\t"
      "s_sub_u32 s60, s43, 14\n\t"
      "s_cselect_b32 s53, s43, s60\n\t"
      "s_add_u32 s43, %[rot], 10\n\t"
      "s_sub_u32 s60, s43, 14\n\t"
      "s_cselect_b32 s54, s43, s60\n\t"
      "s_add_u32 s43, %[rot], 11\n\t"
      "s_sub_u32 s60, s43, 14\n\t"
      "s_cselect_b32 s55, s43, s60\n\t"
      "s_add_u32 s43, %[rot], 12\n\t"
      "s_sub_u32 s60, s43, 14\n\t"
      "s_cselect_b32 s56, s43, s60\n\t"
      "s_add_u32 s43, %[rot], 13\n\t"
      "s_sub_u32 s60, s43, 14\n\t"
      "s_cselect_b32 s57, s43, s60\n\t"
      "s_nop 4\n\t"
      "s_mov_b32 s39, m0\n\t"
      "s_cmp_eq_u32 %[w], 0\n\t"
      "s_cselect_b64 s[66:67], 1, 0\n\t"
      "s_add_u32 m0, %[st], 0\n\t"
      "s_mul_i32 s43, s44, 40960\n\t"
      "s_add_u32 s43, s43, %[cb]\n\t"
      "s_add_u32 s43, s43, 0\n\t"
      "buffer_load_dwordx4 v16, %[rw], s43 offen lds\n\t"
      "buffer_load_dwordx4 v17, %[rw], s43 offen offset:1024 lds\n\t"
      "s_add_u32 m0, %[st], 2048\n\t"
      "s_mul_i32 s43, s44, 40960\n\t"
      "s_add_u32 s43, s43, %[cb]\n\t"
      "s_add_u32 s43, s43, 64\n\t"
      "buffer_load_dwordx4 v16, %[rw], s43 offen lds\n\t"
      "buffer_load_dwordx4 v17, %[rw], s43 offen offset:1024 lds\n\t"
      "s_add_u32 m0, %[st], 4096\n\t"
      "s_mul_i32 s43, s45, 40960\n\t"
      "s_add_u32 s43, s43, %[cb]\n\t"
      "s_add_u32 s43, s43, 0\n\t"
      "buffer_load_dwordx4 v16, %[rw], s43 offen lds\n\t"
      "buffer_load_dwordx4 v17, %[rw], s43 offen offset:1024 lds\n\t"
      "s_add_u32 m0, %[st], 6144\n\t"
      "s_mul_i32 s43, s45, 40960\n\t"
      "s_add_u32 s43, s43, %[cb]\n\t"
      "s_add_u32 s43, s43, 64\n\t"
      "buffer_load_dwordx4 v16, %[rw], s43 offen lds\n\t"
      "buffer_load_dwordx4 v17, %[rw], s43 offen offset:1024 lds\n\t"
      "s_add_u32 m0, %[st], 8192\n\t"
      "s_mul_i32 s43, s46, 40960\n\t"
      "s_add_u32 s43, s43, %[cb]\n\t"
      "s_add_u32 s43, s43, 0\n\t"
      "buffer_load_dwordx4 v16, %[rw], s43 offen lds\n\t"
      "buffer_load_dwordx4 v17, %[rw], s43 offen offset:1024 lds\n\t"
      "s_add_u32 m0, %[st], 10240\n\t"
      "s_mul_i32 s43, s46, 40960\n\t"
      "s_add_u32 s43, s43, %[cb]\n\t"
      "s_add_u32 s43, s43, 64\n\t"
      "buffer_load_dwordx4 v16, %[rw], s43 offen lds\n\t"
      "buffer_load_dwordx4 v17, %[rw], s43 offen offset:1024 lds\n\t"
      "s_add_u32 m0, %[st], 12288\n\t"
      "s_mul_i32 s43, s47, 40960\n\t"
      "s_add_u32 s43, s43, %[cb]\n\t"
      "s_add_u32 s43, s43, 0\n\t"
      "buffer_load_dwordx4 v16, %[rw], s43 offen lds\n\t"
      "buffer_load_dwordx4 v17, %[rw], s43 offen offset:1024 lds\n\t"
      "s_add_u32 m0, %[st], 14336\n\t"
      "s_mul_i32 s43, s47, 40960\n\t"
      "s_add_u32 s43, s43, %[cb]\n\t"
      "s_add_u32 s43, s43, 64\n\t"
      "buffer_load_dwordx4 v16, %[rw], s43 offen lds\n\t"
      "buffer_load_dwordx4 v17, %[rw], s43 offen offset:1024 lds\n\t"
      "s_waitcnt vmcnt(12)\n\t"
      "ds_read_b64_tr_b16 a[0:1], v20 offset:0\n\t"
      "ds_read_b64_tr_b16 a[2:3], v20 offset:1024\n\t"
      "ds_read_b64_tr_b16 a[4:5], v20 offset:8\n\t"
      "ds_read_b64_tr_b16 a[6:7], v20 offset:1032\n\t"
      "ds_read_b64_tr_b16 a[8:9], v20 offset:2048\n\t"
      "ds_read_b64_tr_b16 a[10:11], v20 offset:3072\n\t"
      "ds_read_b64_tr_b16 a[12:13], v20 offset:2056\n\t"
      "ds_read_b64_tr_b16 a[14:15], v20 offset:3080\n\t"
      "s_waitcnt lgkmcnt(0)\n\t"
      "s_add_u32 m0, %[st], 0\n\t"
      "s_mul_i32 s43, s48, 40960\n\t"
      "s_add_u32 s43, s43, %[cb]\n\t"
      "s_add_u32 s43, s43, 0\n\t"
      "buffer_load_dwordx4 v16, %[rw], s43 offen lds\n\t"
      "buffer_load_dwordx4 v17, %[rw], s43 offen offset:1024 lds\n\t"
      "s_add_u32 m0, %[st], 2048\n\t"
      "s_mul_i32 s43, s48, 40960\n\t"
      "s_add_u32 s43, s43, %[cb]\n\t"
      "s_add_u32 s43, s43, 64\n\t"
      "buffer_load_dwordx4 v16, %[rw], s43 offen lds\n\t"
      "buffer_load_dwordx4 v17, %[rw], s43 offen offset:1024 lds\n\t"
      "s_waitcnt vmcnt(12)\n\t"
      "ds_read_b64_tr_b16 a[16:17], v20 offset:4096\n\t"
      "ds_read_b64_tr_b16 a[18:19], v20 offset:5120\n\t"
      "ds_read_b64_tr_b16 a[20:21], v20 offset:4104\n\t"
      "ds_read_b64_tr_b16 a[22:23], v20 offset:5128\n\t"
      "ds_read_b64_tr_b16 a[24:25], v20 offset:6144\n\t"
      "ds_read_b64_tr_b16 a[26:27], v20 offset:7168\n\t"
      "ds_read_b64_tr_b16 a[28:29], v20 offset:6152\n\t"
      "ds_read_b64_tr_b16 a[30:31], v20 offset:7176\n\t"
      "s_waitcnt lgkmcnt(0)\n\t"
      "s_add_u32 m0, %[st], 4096\n\t"
      "s_mul_i32 s43, s49, 40960\n\t"
      "s_add_u32 s43, s43, %[cb]\n\t"
      "s_add_u32 s43, s43, 0\n\t"
      "buffer_load_dwordx4 v16, %[rw], s43 offen lds\n\t"
      "buffer_load_dwordx4 v17, %[rw], s43 offen offset:1024 lds\n\t"
      "s_add_u32 m0, %[st], 6144\n\t"
      "s_mul_i32 s43, s49, 40960\n\t"
      "s_add_u32 s43, s43, %[cb]\n\t"
      "s_add_u32 s43, s43, 64\n\t"
      "buffer_load_dwordx4 v16, %[rw], s43 offen lds\n\t"
      "buffer_load_dwordx4 v17, %[rw], s43 offen offset:1024 lds\n\t"
      "s_waitcnt vmcnt(12)\n\t"
      "ds_read_b64_tr_b16 a[32:33], v20 offset:8192\n\t"
      "ds_read_b64_tr_b16 a[34:35], v20 offset:9216\n\t"
      "ds_read_b64_tr_b16 a[36:37], v20 offset:8200\n\t"
      "ds_read_b64_tr_b16 a[38:39], v20 offset:9224\n\t"
      "ds_read_b64_tr_b16 a[40:41], v20 offset:10240\n\t"
      "ds_read_b64_tr_b16 a[42:43], v20 offset:11264\n\t"
      "ds_read_b64_tr_b16 a[44:45], v20 offset:10248\n\t"
      "ds_read_b64_tr_b16 a[46:47], v20 offset:11272\n\t"
      "s_waitcnt lgkmcnt(0)\n\t"
      "s_add_u32 m0, %[st], 8192\n\t"
      "s_mul_i32 s43, s50, 40960\n\t"
      "s_add_u32 s43, s43, %[cb]\n\t"
      "s_add_u32 s43, s43, 0\n\t"
      "buffer_load_dwordx4 v16, %[rw], s43 offen lds\n\t"
      "buffer_load_dwordx4 v17, %[rw], s43 offen offset:1024 lds\n\t"
      "s_add_u32 m0, %[st], 10240\n\t"
      "s_mul_i32 s43, s50, 40960\n\t"
      "s_add_u32 s43, s43, %[cb]\n\t"
      "s_add_u32 s43, s43, 64\n\t"
      "buffer_load_dwordx4 v16, %[rw], s43 offen lds\n\t"
      "buffer_load_dwordx4 v17, %[rw], s43 offen offset:1024 lds\n\t"
      "s_waitcnt vmcnt(12)\n\t"
      "ds_read_b64_tr_b16 a[48:49], v20 offset:12288\n\t"
      "ds_read_b64_tr_b16 a[50:51], v20 offset:13312\n\t"
      "ds_read_b64_tr_b16 a[52:53], v20 offset:12296\n\t"
      "ds_read_b64_tr_b16 a[54:55], v20 offset:13320\n\t"
      "ds_read_b64_tr_b16 a[56:57], v20 offset:14336\n\t"
      "ds_read_b64_tr_b16 a[58:59], v20 offset:15360\n\t"
      "ds_read_b64_tr_b16 a[60:61], v20 offset:14344\n\t"
      "ds_read_b64_tr_b16 a[62:63], v20 offset:15368\n\t"
      "s_waitcnt lgkmcnt(0)\n\t"
      "s_add_u32 m0, %[st], 12288\n\t"
      "s_mul_i32 s43, s51, 40960\n\t"
      "s_add_u32 s43, s43, %[cb]\n\t"
      "s_add_u32 s43, s43, 0\n\t"
      "buffer_load_dwordx4 v16, %[rw], s43 offen lds\n\t"
      "buffer_load_dwordx4 v17, %[rw], s43 offen offset:1024 lds\n\t"
      "s_add_u32 m0, %[st], 14336\n\t"
      "s_mul_i32 s43, s51, 40960\n\t"
      "s_add_u32 s43, s43, %[cb]\n\t"
      "s_add_u32 s43, s43, 64\n\t"
      "buffer_load_dwordx4 v16, %[rw], s43 offen lds\n\t"
      "buffer_load_dwordx4 v17, %[rw], s43 offen offset:1024 lds\n\t"
      "s_waitcnt vmcnt(12)\n\t"
      "ds_read_b64_tr_b16 a[64:65], v20 offset:0\n\t"
      "ds_read_b64_tr_b16 a[66:67], v20 offset:1024\n\t"
      "ds_read_b64_tr_b16 a[68:69], v20 offset:8\n\t"
      "ds_read_b64_tr_b16 a[70:71], v20 offset:1032\n\t"
      "ds_read_b64_tr_b16 a[72:73], v20 offset:2048\n\t"
      "ds_read_b64_tr_b16 a[74:75], v20 offset:3072\n\t"
      "ds_read_b64_tr_b16 a[76:77], v20 offset:2056\n\t"
      "ds_read_b64_tr_b16 a[78:79], v20 offset:3080\n\t"
      "s_waitcnt lgkmcnt(0)\n\t"
      "s_add_u32 m0, %[st], 0\n\t"
      "s_mul_i32 s43, s52, 40960\n\t"
      "s_add_u32 s43, s43, %[cb]\n\t"
      "s_add_u32 s43, s43, 0\n\t"
      "buffer_load_dwordx4 v16, %[rw], s43 offen lds\n\t"
      "buffer_load_dwordx4 v17, %[rw], s43 offen offset:1024 lds\n\t"
      "s_add_u32 m0, %[st], 2048\n\t"
      "s_mul_i32 s43, s52, 40960\n\t"
      "s_add_u32 s43, s43, %[cb]\n\t"
      "s_add_u32 s43, s43, 64\n\t"
      "buffer_load_dwordx4 v16, %[rw], s43 offen lds\n\t"
      "buffer_load_dwordx4 v17, %[rw], s43 offen offset:1024 lds\n\t"
      "s_waitcnt vmcnt(12)\n\t"
      "ds_read_b64_tr_b16 a[80:81], v20 offset:4096\n\t"
      "ds_read_b64_tr_b16 a[82:83], v20 offset:5120\n\t"
      "ds_read_b64_tr_b16 a[84:85], v20 offset:4104\n\t"
      "ds_read_b64_tr_b16 a[86:87], v20 offset:5128\n\t"
      "ds_read_b64_tr_b16 a[88:89], v20 offset:6144\n\t"
      "ds_read_b64_tr_b16 a[90:91], v20 offset:7168\n\t"
      "ds_read_b64_tr_b16 a[92:93], v20 offset:6152\n\t"
      "ds_read_b64_tr_b16 a[94:95], v20 offset:7176\n\t"
      "s_waitcnt lgkmcnt(0)\n\t"
      "s_add_u32 m0, %[st], 4096\n\t"
      "s_mul_i32 s43, s53, 40960\n\t"
      "s_add_u32 s43, s43, %[cb]\n\t"
      "s_add_u32 s43, s43, 0\n\t"
      "buffer_load_dwordx4 v16, %[rw], s43 offen lds\n\t"
      "buffer_load_dwordx4 v17, %[rw], s43 offen offset:1024 lds\n\t"
      "s_add_u32 m0, %[st], 6144\n\t"
      "s_mul_i32 s43, s53, 40960\n\t"
      "s_add_u32 s43, s43, %[cb]\n\t"
      "s_add_u32 s43, s43, 64\n\t"
      "buffer_load_dwordx4 v16, %[rw], s43 offen lds\n\t"
      "buffer_load_dwordx4 v17, %[rw], s43 offen offset:1024 lds\n\t"
      "s_waitcnt vmcnt(12)\n\t"
      "ds_read_b64_tr_b16 a[96:97], v20 offset:8192\n\t"
      "ds_read_b64_tr_b16 a[98:99], v20 offset:9216\n\t"
      "ds_read_b64_tr_b16 a[100:101], v20 offset:8200\n\t"
      "ds_read_b64_tr_b16 a[102:103], v20 offset:9224\n\t"
      "ds_read_b64_tr_b16 a[104:105], v20 offset:10240\n\t"
      "ds_read_b64_tr_b16 a[106:107], v20 offset:11264\n\t"
      "ds_read_b64_tr_b16 a[108:109], v20 offset:10248\n\t"
      "ds_read_b64_tr_b16 a[110:111], v20 offset:11272\n\t"
      "s_waitcnt lgkmcnt(0)\n\t"
      "s_add_u32 m0, %[st], 8192\n\t"
      "s_mul_i32 s43, s54, 40960\n\t"
      "s_add_u32 s43, s43, %[cb]\n\t"
      "s_add_u32 s43, s43, 0\n\t"
      "buffer_load_dwordx4 v16, %[rw], s43 offen lds\n\t"
      "buffer_load_dwordx4 v17, %[rw], s43 offen offset:1024 lds\n\t"
      "s_add_u32 m0, %[st], 10240\n\t"
      "s_mul_i32 s43, s54, 40960\n\t"
      "s_add_u32 s43, s43, %[cb]\n\t"
      "s_add_u32 s43, s43, 64\n\t"
      "buffer_load_dwordx4 v16, %[rw], s43 offen lds\n\t"
      "buffer_load_dwordx4 v17, %[rw], s43 offen offset:1024 lds\n\t"
      "s_waitcnt vmcnt(12)\n\t"
      "ds_read_b64_tr_b16 a[112:113], v20 offset:12288\n\t"
      "ds_read_b64_tr_b16 a[114:115], v20 offset:13312\n\t"
      "ds_read_b64_tr_b16 a[116:117], v20 offset:12296\n\t"
      "ds_read_b64_tr_b16 a[118:119], v20 offset:13320\n\t"
      "ds_read_b64_tr_b16 a[120:121], v20 offset:14336\n\t"
      "ds_read_b64_tr_b16 a[122:123], v20 offset:15360\n\t"
      "ds_read_b64_tr_b16 a[124:125], v20 offset:14344\n\t"
      "ds_read_b64_tr_b16 a[126:127], v20 offset:15368\n\t"
      "s_waitcnt lgkmcnt(0)\n\t"
      "s_add_u32 m0, %[st], 12288\n\t"
      "s_mul_i32 s43, s55, 40960\n\t"
      "s_add_u32 s43, s43, %[cb]\n\t"
      "s_add_u32 s43, s43, 0\n\t"
      "buffer_load_dwordx4 v16, %[rw], s43 offen lds\n\t"
      "buffer_load_dwordx4 v17, %[rw], s43 offen offset:1024 lds\n\t"
      "s_add_u32 m0, %[st], 14336\n\t"
      "s_mul_i32 s43, s55, 40960\n\t"
      "s_add_u32 s43, s43, %[cb]\n\t"
      "s_add_u32 s43, s43, 64\n\t"
      "buffer_load_dwordx4 v16, %[rw], s43 offen lds\n\t"
      "buffer_load_dwordx4 v17, %[rw], s43 offen offset:1024 lds\n\t"
      "s_waitcnt vmcnt(12)\n\t"
      "ds_read_b64_tr_b16 a[128:129], v20 offset:0\n\t"
      "ds_read_b64_tr_b16 a[130:131], v20 offset:1024\n\t"
      "ds_read_b64_tr_b16 a[132:133], v20 offset:8\n\t"
      "ds_read_b64_tr_b16 a[134:135], v20 offset:1032\n\t"
      "ds_read_b64_tr_b16 a[136:137], v20 offset:2048\n\t"
      "ds_read_b64_tr_b16 a[138:139], v20 offset:3072\n\t"
      "ds_read_b64_tr_b16 a[140:141], v20 offset:2056\n\t"
      "ds_read_b64_tr_b16 a[142:143], v20 offset:3080\n\t"
      "s_waitcnt lgkmcnt(0)\n\t"
      "s_add_u32 m0, %[st], 0\n\t"
      "s_mul_i32 s43, s56, 40960\n\t"
      "s_add_u32 s43, s43, %[cb]\n\t"
      "s_add_u32 s43, s43, 0\n\t"
      "buffer_load_dwordx4 v16, %[rw], s43 offen lds\n\t"
      "buffer_load_dwordx4 v17, %[rw], s43 offen offset:1024 lds\n\t"
      "s_add_u32 m0, %[st], 2048\n\t"
      "s_mul_i32 s43, s56, 40960\n\t"
      "s_add_u32 s43, s43, %[cb]\n\t"
      "s_add_u32 s43, s43, 64\n\t"
      "buffer_load_dwordx4 v16, %[rw], s43 offen lds\n\t"
      "buffer_load_dwordx4 v17, %[rw], s43 offen offset:1024 lds\n\t"
      "s_waitcnt vmcnt(12)\n\t"
      "ds_read_b64_tr_b16 a[144:145], v20 offset:4096\n\t"
      "ds_read_b64_tr_b16 a[146:147], v20 offset:5120\n\t"
      "ds_read_b64_tr_b16 a[148:149], v20 offset:4104\n\t"
      "ds_read_b64_tr_b16 a[150:151], v20 offset:5128\n\t"
      "ds_read_b64_tr_b16 a[152:153], v20 offset:6144\n\t"
      "ds_read_b64_tr_b16 a[154:155], v20 offset:7168\n\t"
      "ds_read_b64_tr_b16 a[156:157], v20 offset:6152\n\t"
      "ds_read_b64_tr_b16 a[158:159], v20 offset:7176\n\t"
      "s_waitcnt lgkmcnt(0)\n\t"
      "s_add_u32 m0, %[st], 4096\n\t"
      "s_mul_i32 s43, s57, 40960\n\t"
      "s_add_u32 s43, s43, %[cb]\n\t"
      "s_add_u32 s43, s43, 0\n\t"
      "buffer_load_dwordx4 v16, %[rw], s43 offen lds\n\t"
      "buffer_load_dwordx4 v17, %[rw], s43 offen offset:1024 lds\n\t"
      "s_add_u32 m0, %[st], 6144\n\t"
      "s_mul_i32 s43, s57, 40960\n\t"
      "s_add_u32 s43, s43, %[cb]\n\t"
      "s_add_u32 s43, s43, 64\n\t"
      "buffer_load_dwordx4 v16, %[rw], s43 offen lds\n\t"
      "buffer_load_dwordx4 v17, %[rw], s43 offen offset:1024 lds\n\t"
      "s_waitcnt vmcnt(12)\n\t"
      "ds_read_b64_tr_b16 a[160:161], v20 offset:8192\n\t"
      "ds_read_b64_tr_b16 a[162:163], v20 offset:9216\n\t"
      "ds_read_b64_tr_b16 a[164:165], v20 offset:8200\n\t"
      "ds_read_b64_tr_b16 a[166:167], v20 offset:9224\n\t"
      "ds_read_b64_tr_b16 a[168:169], v20 offset:10240\n\t"
      "ds_read_b64_tr_b16 a[170:171], v20 offset:11264\n\t"
      "ds_read_b64_tr_b16 a[172:173], v20 offset:10248\n\t"
      "ds_read_b64_tr_b16 a[174:175], v20 offset:11272\n\t"
      "s_waitcnt lgkmcnt(0)\n\t"
      "s_waitcnt vmcnt(8)\n\t"
      "ds_read_b64_tr_b16 a[176:177], v20 offset:12288\n\t"
      "ds_read_b64_tr_b16 a[178:179], v20 offset:13312\n\t"
      "ds_read_b64_tr_b16 a[180:181], v20 offset:12296\n\t"
      "ds_read_b64_tr_b16 a[182:183], v20 offset:13320\n\t"
      "ds_read_b64_tr_b16 a[184:185], v20 offset:14336\n\t"
      "ds_read_b64_tr_b16 a[186:187], v20 offset:15360\n\t"
      "ds_read_b64_tr_b16 a[188:189], v20 offset:14344\n\t"
      "ds_read_b64_tr_b16 a[190:191], v20 offset:15368\n\t"
      "s_waitcnt lgkmcnt(0)\n\t"
      "s_waitcnt vmcnt(4)\n\t"
      "ds_read_b64_tr_b16 a[192:193], v20 offset:0\n\t"
      "ds_read_b64_tr_b16 a[194:195], v20 offset:1024\n\t"
      "ds_read_b64_tr_b16 a[196:197], v20 offset:8\n\t"
      "ds_read_b64_tr_b16 a[198:199], v20 offset:1032\n\t"
      "ds_read_b64_tr_b16 a[200:201], v20 offset:2048\n\t"
      "ds_read_b64_tr_b16 a[202:203], v20 offset:3072\n\t"
      "ds_read_b64_tr_b16 a[204:205], v20 offset:2056\n\t"
      "ds_read_b64_tr_b16 a[206:207], v20 offset:3080\n\t"
      "s_waitcnt lgkmcnt(0)\n\t"
      "s_waitcnt vmcnt(0)\n\t"
      "ds_read_b64_tr_b16 a[208:209], v20 offset:4096\n\t"
      "ds_read_b64_tr_b16 a[210:211], v20 offset:5120\n\t"
      "ds_read_b64_tr_b16 a[212:213], v20 offset:4104\n\t"
      "ds_read_b64_tr_b16 a[214:215], v20 offset:5128\n\t"
      "ds_read_b64_tr_b16 a[216:217], v20 offset:6144\n\t"
      "ds_read_b64_tr_b16 a[218:219], v20 offset:7168\n\t"
      "ds_read_b64_tr_b16 a[220:221], v20 offset:6152\n\t"
      "ds_read_b64_tr_b16 a[222:223], v20 offset:7176\n\t"
      "s_waitcnt lgkmcnt(0)\n\t"
      "s_waitcnt lgkmcnt(0)\n\t"
      "s_barrier\n\t"
      "s_cmp_lt_u32 %[w], 2\n\t"
      "s_cbranch_scc1 LZ%=\n\t"
      "s_cmp_eq_u32 %[rot], 0\n\t"
      "s_cbranch_scc1 LZ0%=\n\t"
      "v_accvgpr_write_b32 a112, 0\n\t"
      "v_accvgpr_write_b32 a113, 0\n\t"
      "v_accvgpr_write_b32 a114, 0\n\t"
      "v_accvgpr_write_b32 a115, 0\n\t"
      "v_accvgpr_write_b32 a116, 0\n\t"
      "v_accvgpr_write_b32 a117, 0\n\t"
      "v_accvgpr_write_b32 a118, 0\n\t"
      "v_accvgpr_write_b32 a119, 0\n\t"
      "v_accvgpr_write_b32 a120, 0\n\t"
      "v_accvgpr_write_b32 a121, 0\n\t"
      "v_accvgpr_write_b32 a122, 0\n\t"
      "v_accvgpr_write_b32 a123, 0\n\t"
      "v_accvgpr_write_b32 a124, 0\n\t"
      "v_accvgpr_write_b32 a125, 0\n\t"
      "v_accvgpr_write_b32 a126, 0\n\t"
      "v_accvgpr_write_b32 a127, 0\n\t"
      "s_branch LZ%=\n\t"
      "LZ0%=:\n\t"
      "v_accvgpr_write_b32 a0, 0\n\t"
      "v_accvgpr_write_b32 a1, 0\n\t"
      "v_accvgpr_write_b32 a2, 0\n\t"
      "v_accvgpr_write_b32 a3, 0\n\t"
      "v_accvgpr_write_b32 a4, 0\n\t"
      "v_accvgpr_write_b32 a5, 0\n\t"
      "v_accvgpr_write_b32 a6, 0\n\t"
      "v_accvgpr_write_b32 a7, 0\n\t"
      "v_accvgpr_write_b32 a8, 0\n\t"
      "v_accvgpr_write_b32 a9, 0\n\t"
      "v_accvgpr_write_b32 a10, 0\n\t"
      "v_accvgpr_write_b32 a11, 0\n\t"
      "v_accvgpr_write_b32 a12, 0\n\t"
      "v_accvgpr_write_b32 a13, 0\n\t"
      "v_accvgpr_write_b32 a14, 0\n\t"
      "v_accvgpr_write_b32 a15, 0\n\t"
      "LZ%=:\n\t"
      "s_lshl_b32 s44, s44, 10\n\t"
      "s_lshl_b32 s45, s45, 10\n\t"
      "s_lshl_b32 s46, s46, 10\n\t"
      "s_lshl_b32 s47, s47, 10\n\t"
      "s_lshl_b32 s48, s48, 10\n\t"
      "s_lshl_b32 s49, s49, 10\n\t"
      "s_lshl_b32 s50, s50, 10\n\t"
      "s_lshl_b32 s51, s51, 10\n\t"
      "s_lshl_b32 s52, s52, 10\n\t"
      "s_lshl_b32 s53, s53, 10\n\t"
      "s_lshl_b32 s54, s54, 10\n\t"
      "s_lshl_b32 s55, s55, 10\n\t"
      "s_lshl_b32 s56, s56, 10\n\t"
      "s_lshl_b32 s57, s57, 10\n\t"
      "s_mov_b32 s40, 0\n\t"
      "s_mul_i32 s36, %[sw], 20480\n\t"
      "s_sub_u32 s37, 20480, s36\n\t"
      "s_add_u32 s60, %[hb], s36\n\t"
      "s_add_u32 s61, %[hb], s37\n\t"
      "s_mov_b32 s41, 0\n\t"
      "s_mov_b32 s36, 0\n\t"
      "LOOP%=:\n\t"
      "s_cmp_lg_u32 s40, 63\n\t"
      "s_cbranch_scc1 LQW%=\n\t"
      "s_cmp_lg_u32 %[w], 0\n\t"
      "s_cbranch_scc1 LQB%=\n\t"
      "s_mov_b32 s42, 0\n\t"
      "s_mov_b64 exec, 1\n\t"
      "LQL%=:\n\t"
      "s_sleep 32\n\t"
      "buffer_load_dword v8, off, %[rs], %[qw] sc1\n\t"
      "s_add_u32 s42, s42, 1\n\t"
      "s_cmp_gt_u32 s42, 50000\n\t"
      "s_cbranch_scc1 LFAIL%=\n\t"
      "s_waitcnt vmcnt(0)\n\t"
      "v_cmp_gt_i32 vcc, 61, v8\n\t"
      "s_cbranch_vccnz LQL%=\n\t"
      "s_mov_b64 exec, -1\n\t"
      "LQB%=:\n\t"
      "s_barrier\n\t"
      "LQW%=:\n\t"
      "s_mov_b32 s42, 0\n\t"
      "s_mov_b64 exec, 0xff\n\t"
      "buffer_load_dwordx4 v[64:67], v1, %[rs], 0 offen sc1\n\t"
      "s_nop 11\n\t"
      "buffer_load_dwordx4 v[68:71], v1, %[rs], 0 offen sc1\n\t"
      "s_nop 11\n\t"
      "buffer_load_dwordx4 v[72:75], v1, %[rs], 0 offen sc1\n\t"
      "s_nop 11\n\t"
      "buffer_load_dwordx4 v[76:79], v1, %[rs], 0 offen sc1\n\t"
      "s_nop 11\n\t"
      "LEW%=:\n\t"
      "s_waitcnt vmcnt(3)\n\t"
      "v_min3_i32 v8, v64, v65, v66\n\t"
      "v_min_i32 v8, v8, v67\n\t"
      "v_cmp_gt_i32 vcc, s40, v8\n\t"
      "s_cbranch_vccz LEWD%=\n\t"
      "buffer_load_dwordx4 v[64:67], v1, %[rs], 0 offen sc1\n\t"
      "s_nop 11\n\t"
      "s_waitcnt vmcnt(3)\n\t"
      "v_min3_i32 v8, v68, v69, v70\n\t"
      "v_min_i32 v8, v8, v71\n\t"
      "v_cmp_gt_i32 vcc, s40, v8\n\t"
      "s_cbranch_vccz LEWD%=\n\t"
      "buffer_load_dwordx4 v[68:71], v1, %[rs], 0 offen sc1\n\t"
      "s_nop 11\n\t"
      "s_waitcnt vmcnt(3)\n\t"
      "v_min3_i32 v8, v72, v73, v74\n\t"
      "v_min_i32 v8, v8, v75\n\t"
      "v_cmp_gt_i32 vcc, s40, v8\n\t"
      "s_cbranch_vccz LEWD%=\n\t"
      "buffer_load_dwordx4 v[72:75], v1, %[rs], 0 offen sc1\n\t"
      "s_nop 11\n\t"
      "s_waitcnt vmcnt(3)\n\t"
      "v_min3_i32 v8, v76, v77, v78\n\t"
      "v_min_i32 v8, v8, v79\n\t"
      "v_cmp_gt_i32 vcc, s40, v8\n\t"
      "s_cbranch_vccz LEWD%=\n\t"
      "buffer_load_dwordx4 v[76:79], v1, %[rs], 0 offen sc1\n\t"
      "s_nop 11\n\t"
      "s_add_u32 s42, s42, 1\n\t"
      "s_cmp_lt_u32 s42, 400000\n\t"
      "s_cbranch_scc1 LEW%=\n\t"
      "s_branch LFAIL%=\n\t"
      "LEWD%=:\n\t"
      "s_mov_b64 exec, -1\n\t"
      "s_mov_b32 s42, 0\n\t"
      "LPOLL%=:\n\t"
      "buffer_load_dwordx4 v[48:51], v2, %[rs], s60 offen offset:0 sc1\n\t"
      "buffer_load_dwordx4 v[52:55], v2, %[rs], s60 offen offset:1024 sc1\n\t"
      "buffer_load_dwordx4 v[64:67], v0, %[rs], s44 offen sc1\n\t"
      "s_nop 10\n\t"
      "buffer_load_dwordx4 v[68:71], v0, %[rs], s45 offen sc1\n\t"
      "s_nop 10\n\t"
      "buffer_load_dwordx4 v[72:75], v0, %[rs], s46 offen sc1\n\t"
      "s_nop 10\n\t"
      "buffer_load_dwordx4 v[76:79], v0, %[rs], s47 offen sc1\n\t"
      "s_nop 10\n\t"
      "buffer_load_dwordx4 v[80:83], v0, %[rs], s48 offen sc1\n\t"
      "s_nop 10\n\t"
      "buffer_load_dwordx4 v[84:87], v0, %[rs], s49 offen sc1\n\t"
      "s_nop 10\n\t"
      "buffer_load_dwordx4 v[88:91], v0, %[rs], s50 offen sc1\n\t"
      "s_nop 10\n\t"
      "buffer_load_dwordx4 v[92:95], v0, %[rs], s51 offen sc1\n\t"
      "s_nop 10\n\t"
      "v_mov_b32 v8, 0\n\t"
      "s_add_u32 s42, s42, 1\n\t"
      "s_cmp_gt_u32 s42, 400000\n\t"
      "s_cbranch_scc1 LFAIL%=\n\t"
      "s_waitcnt vmcnt(8)\n\t"
      "v_lshlrev_b32 v16, 16, v48\n\t"
      "v_and_b32 v17, 0xffff0000, v48\n\t"
      "v_lshlrev_b32 v18, 16, v49\n\t"
      "v_and_b32 v19, 0xffff0000, v49\n\t"
      "v_lshlrev_b32 v20, 16, v50\n\t"
      "v_and_b32 v21, 0xffff0000, v50\n\t"
      "v_lshlrev_b32 v22, 16, v51\n\t"
      "v_and_b32 v23, 0xffff0000, v51\n\t"
      "v_lshlrev_b32 v24, 16, v52\n\t"
      "v_and_b32 v25, 0xffff0000, v52\n\t"
      "v_lshlrev_b32 v26, 16, v53\n\t"
      "v_and_b32 v27, 0xffff0000, v53\n\t"
      "v_lshlrev_b32 v28, 16, v54\n\t"
      "v_and_b32 v29, 0xffff0000, v54\n\t"
      "v_lshlrev_b32 v30, 16, v55\n\t"
      "v_and_b32 v31, 0xffff0000, v55\n\t"
      "buffer_load_dwordx4 v[96:99], v0, %[rs], s52 offen sc1\n\t"
      "s_waitcnt vmcnt(8)\n\t"
      "v_mfma_f32_16x16x32_bf16 v[16:19], a[0:3], v[64:67], v[16:19]\n\t"
      "v_max3_u32 v8, v8, v64, v65\n\t"
      "v_mfma_f32_16x16x32_bf16 v[20:23], a[4:7], v[64:67], v[20:23]\n\t"
      "v_max3_u32 v8, v8, v66, v67\n\t"
      "v_mfma_f32_16x16x32_bf16 v[24:27], a[8:11], v[64:67], v[24:27]\n\t"
      "v_mfma_f32_16x16x32_bf16 v[28:31], a[12:15], v[64:67], v[28:31]\n\t"
      "buffer_load_dwordx4 v[100:103], v0, %[rs], s53 offen sc1\n\t"
      "s_waitcnt vmcnt(8)\n\t"
      "v_mfma_f32_16x16x32_bf16 v[16:19], a[16:19], v[68:71], v[16:19]\n\t"
      "v_max3_u32 v8, v8, v68, v69\n\t"
      "v_mfma_f32_16x16x32_bf16 v[20:23], a[20:23], v[68:71], v[20:23]\n\t"
      "v_max3_u32 v8, v8, v70, v71\n\t"
      "v_mfma_f32_16x16x32_bf16 v[24:27], a[24:27], v[68:71], v[24:27]\n\t"
      "v_mfma_f32_16x16x32_bf16 v[28:31], a[28:31], v[68:71], v[28:31]\n\t"
      "buffer_load_dwordx4 v[104:107], v0, %[rs], s54 offen sc1\n\t"
      "s_waitcnt vmcnt(8)\n\t"
      "v_mfma_f32_16x16x32_bf16 v[16:19], a[32:35], v[72:75], v[16:19]\n\t"
      "v_max3_u32 v8, v8, v72, v73\n\t"
      "v_mfma_f32_16x16x32_bf16 v[20:23], a[36:39], v[72:75], v[20:23]\n\t"
      "v_max3_u32 v8, v8, v74, v75\n\t"
      "v_mfma_f32_16x16x32_bf16 v[24:27], a[40:43], v[72:75], v[24:27]\n\t"
      "v_mfma_f32_16x16x32_bf16 v[28:31], a[44:47], v[72:75], v[28:31]\n\t"
      "buffer_load_dwordx4 v[108:111], v0, %[rs], s55 offen sc1\n\t"
      "s_waitcnt vmcnt(8)\n\t"
      "v_mfma_f32_16x16x32_bf16 v[16:19], a[48:51], v[76:79], v[16:19]\n\t"
      "v_max3_u32 v8, v8, v76, v77\n\t"
      "v_mfma_f32_16x16x32_bf16 v[20:23], a[52:55], v[76:79], v[20:23]\n\t"
      "v_max3_u32 v8, v8, v78, v79\n\t"
      "v_mfma_f32_16x16x32_bf16 v[24:27], a[56:59], v[76:79], v[24:27]\n\t"
      "v_mfma_f32_16x16x32_bf16 v[28:31], a[60:63], v[76:79], v[28:31]\n\t"
      "buffer_load_dwordx4 v[112:115], v0, %[rs], s56 offen sc1\n\t"
      "s_waitcnt vmcnt(8)\n\t"
      "v_mfma_f32_16x16x32_bf16 v[16:19], a[64:67], v[80:83], v[16:19]\n\t"
      "v_max3_u32 v8, v8, v80, v81\n\t"
      "v_mfma_f32_16x16x32_bf16 v[20:23], a[68:71], v[80:83], v[20:23]\n\t"
      "v_max3_u32 v8, v8, v82, v83\n\t"
      "v_mfma_f32_16x16x32_bf16 v[24:27], a[72:75], v[80:83], v[24:27]\n\t"
      "v_mfma_f32_16x16x32_bf16 v[28:31], a[76:79], v[80:83], v[28:31]\n\t"
      "buffer_load_dwordx4 v[116:119], v0, %[rs], s57 offen sc1\n\t"
      "s_waitcnt vmcnt(8)\n\t"
      "v_mfma_f32_16x16x32_bf16 v[16:19], a[80:83], v[84:87], v[16:19]\n\t"
      "v_max3_u32 v8, v8, v84, v85\n\t"
      "v_mfma_f32_16x16x32_bf16 v[20:23], a[84:87], v[84:87], v[20:23]\n\t"
      "v_max3_u32 v8, v8, v86, v87\n\t"
      "v_mfma_f32_16x16x32_bf16 v[24:27], a[88:91], v[84:87], v[24:27]\n\t"
      "v_mfma_f32_16x16x32_bf16 v[28:31], a[92:95], v[84:87], v[28:31]\n\t"
      "s_waitcnt vmcnt(7)\n\t"
      "v_mfma_f32_16x16x32_bf16 v[16:19], a[96:99], v[88:91], v[16:19]\n\t"
      "v_max3_u32 v8, v8, v88, v89\n\t"
      "v_mfma_f32_16x16x32_bf16 v[20:23], a[100:103], v[88:91], v[20:23]\n\t"
      "v_max3_u32 v8, v8, v90, v91\n\t"
      "v_mfma_f32_16x16x32_bf16 v[24:27], a[104:107], v[88:91], v[24:27]\n\t"
      "v_mfma_f32_16x16x32_bf16 v[28:31], a[108:111], v[88:91], v[28:31]\n\t"
      "s_waitcnt vmcnt(6)\n\t"
      "v_mfma_f32_16x16x32_bf16 v[16:19], a[112:115], v[92:95], v[16:19]\n\t"
      "v_max3_u32 v8, v8, v92, v93\n\t"
      "v_mfma_f32_16x16x32_bf16 v[20:23], a[116:119], v[92:95], v[20:23]\n\t"
      "v_max3_u32 v8, v8, v94, v95\n\t"
      "v_mfma_f32_16x16x32_bf16 v[24:27], a[120:123], v[92:95], v[24:27]\n\t"
      "v_mfma_f32_16x16x32_bf16 v[28:31], a[124:127], v[92:95], v[28:31]\n\t"
      "s_waitcnt vmcnt(5)\n\t"
      "v_mfma_f32_16x16x32_bf16 v[16:19], a[128:131], v[96:99], v[16:19]\n\t"
      "v_max3_u32 v8, v8, v96, v97\n\t"
      "v_mfma_f32_16x16x32_bf16 v[20:23], a[132:135], v[96:99], v[20:23]\n\t"
      "v_max3_u32 v8, v8, v98, v99\n\t"
      "v_mfma_f32_16x16x32_bf16 v[24:27], a[136:139], v[96:99], v[24:27]\n\t"
      "v_mfma_f32_16x16x32_bf16 v[28:31], a[140:143], v[96:99], v[28:31]\n\t"
      "s_waitcnt vmcnt(4)\n\t"
      "v_mfma_f32_16x16x32_bf16 v[16:19], a[144:147], v[100:103], v[16:19]\n\t"
      "v_max3_u32 v8, v8, v100, v101\n\t"
      "v_mfma_f32_16x16x32_bf16 v[20:23], a[148:151], v[100:103], v[20:23]\n\t"
      "v_max3_u32 v8, v8, v102, v103\n\t"
      "v_mfma_f32_16x16x32_bf16 v[24:27], a[152:155], v[100:103], v[24:27]\n\t"
      "v_mfma_f32_16x16x32_bf16 v[28:31], a[156:159], v[100:103], v[28:31]\n\t"
      "s_waitcnt vmcnt(3)\n\t"
      "v_mfma_f32_16x16x32_bf16 v[16:19], a[160:163], v[104:107], v[16:19]\n\t"
      "v_max3_u32 v8, v8, v104, v105\n\t"
      "v_mfma_f32_16x16x32_bf16 v[20:23], a[164:167], v[104:107], v[20:23]\n\t"
      "v_max3_u32 v8, v8, v106, v107\n\t"
      "v_mfma_f32_16x16x32_bf16 v[24:27], a[168:171], v[104:107], v[24:27]\n\t"
      "v_mfma_f32_16x16x32_bf16 v[28:31], a[172:175], v[104:107], v[28:31]\n\t"
      "s_waitcnt vmcnt(2)\n\t"
      "v_mfma_f32_16x16x32_bf16 v[16:19], a[176:179], v[108:111], v[16:19]\n\t"
      "v_max3_u32 v8, v8, v108, v109\n\t"
      "v_mfma_f32_16x16x32_bf16 v[20:23], a[180:183], v[108:111], v[20:23]\n\t"
      "v_max3_u32 v8, v8, v110, v111\n\t"
      "v_mfma_f32_16x16x32_bf16 v[24:27], a[184:187], v[108:111], v[24:27]\n\t"
      "v_mfma_f32_16x16x32_bf16 v[28:31], a[188:191], v[108:111], v[28:31]\n\t"
      "s_waitcnt vmcnt(1)\n\t"
      "v_mfma_f32_16x16x32_bf16 v[16:19], a[192:195], v[112:115], v[16:19]\n\t"
      "v_max3_u32 v8, v8, v112, v113\n\t"
      "v_mfma_f32_16x16x32_bf16 v[20:23], a[196:199], v[112:115], v[20:23]\n\t"
      "v_max3_u32 v8, v8, v114, v115\n\t"
      "v_mfma_f32_16x16x32_bf16 v[24:27], a[200:203], v[112:115], v[24:27]\n\t"
      "v_mfma_f32_16x16x32_bf16 v[28:31], a[204:207], v[112:115], v[28:31]\n\t"
      "s_waitcnt vmcnt(0)\n\t"
      "v_mfma_f32_16x16x32_bf16 v[16:19], a[208:211], v[116:119], v[16:19]\n\t"
      "v_max3_u32 v8, v8, v116, v117\n\t"
      "v_mfma_f32_16x16x32_bf16 v[20:23], a[212:215], v[116:119], v[20:23]\n\t"
      "v_max3_u32 v8, v8, v118, v119\n\t"
      "v_mfma_f32_16x16x32_bf16 v[24:27], a[216:219], v[116:119], v[24:27]\n\t"
      "v_cmp_eq_u32 vcc, -1, v8\n\t"
      "v_mfma_f32_16x16x32_bf16 v[28:31], a[220:223], v[116:119], v[28:31]\n\t"
      "s_cbranch_vccnz LRETRY%=\n\t"
      "ds_write_b128 v3, v[16:19] offset:0\n\t"
      "ds_write_b128 v3, v[20:23] offset:1024\n\t"
      "s_nop 0\n\t"
      "ds_write_b128 v7, v[24:27] offset:2048\n\t"
      "s_nop 2\n\t"
      "ds_write_b128 v7, v[28:31] offset:3072\n\t"
      "s_waitcnt lgkmcnt(0)\n\t"
      "s_barrier\n\t"
      "ds_read_b128 v[16:19], v4 offset:0\n\t"
      "ds_read_b128 v[20:23], v4 offset:4096\n\t"
      "ds_read_b128 v[24:27], v4 offset:8192\n\t"
      "ds_read_b128 v[28:31], v4 offset:12288\n\t"
      "s_waitcnt lgkmcnt(2)\n\t"
      "v_pk_add_f32 v[16:17], v[16:17], v[20:21]\n\t"
      "v_pk_add_f32 v[18:19], v[18:19], v[22:23]\n\t"
      "s_waitcnt lgkmcnt(0)\n\t"
      "v_pk_add_f32 v[24:25], v[24:25], v[28:29]\n\t"
      "v_pk_add_f32 v[26:27], v[26:27], v[30:31]\n\t"
      "v_pk_add_f32 v[16:17], v[16:17], v[24:25]\n\t"
      "v_pk_add_f32 v[18:19], v[18:19], v[26:27]\n\t"
      "v_cvt_pk_bf16_f32 v12, v16, v17\n\t"
      "v_cvt_pk_bf16_f32 v13, v18, v19\n\t"
      "buffer_store_dwordx2 v[12:13], v5, %[ro], 0 offen\n\t"
      "v_and_b32 v5, 0x7fffffff, v5\n\t"
      "v_add_u32 v0, 6967296, v0\n\t"
      "s_add_u32 s60, s60, 2903040\n\t"
      "s_add_u32 s40, s40, 63\n\t"
      "s_cmp_lt_u32 s40, 64\n\t"
      "s_cbranch_scc1 LOOP%=\n\t"
      "s_branch LEND%=\n\t"
      "LRETRY%=:\n\t"
      "s_nop 7\n\t"
      "s_branch LPOLL%=\n\t"
      "LFAIL%=:\n\t"
      "v_mov_b32 v8, 1\n\t"
      "s_mov_b32 s43, 688128\n\t"
      "buffer_store_dword v8, off, %[rs], s43 sc1\n\t"
      "LEND%=:\n\t"
      "s_waitcnt vmcnt(0) lgkmcnt(0)\n\t"
      "s_mov_b64 exec, -1\n\t"
      "s_mov_b32 m0, s39\n\t"
      : "+{v[0:7]}"(ad), "+{v[16:23]}"(ld)
      : [rs] "s"(rs), [ro] "s"(ro), [rw] "s"(rw), [w] "s"(w), [st] "s"(uni(stage)), [cb] "s"(uni(k0 * 32 * (DM * 2) + cg * 128)),
        [hb] "s"(uni(WS_H + cg * 2048)), [sw] "s"(mt), [rot] "s"(7 * (cg & 1)), [qw] "s"(uni(ring_phys(EW_ACT + cg * 128, flip ^ 1)))
      : "memory", "vcc", "scc", "v8", "v9", "v10", "v11", "v12", "v13", "v14", "v15", "v24", "v25", "v26", "v27", "v28", "v29", "v30", "v31", "v32", "v33",
        "v34", "v35", "v36", "v37", "v38", "v39", "v40", "v41", "v42", "v43", "v44", "v45", "v46", "v47", "v48", "v49", "v50", "v51", "v52", "v53", "v54",
        "v55", "v56", "v57", "v58", "v59", "v60", "v61", "v62", "v63", "v64", "v65", "v66", "v67", "v68", "v69", "v70", "v71", "v72", "v73", "v74", "v75",
        "v76", "v77", "v78", "v79", "v80", "v81", "v82", "v83", "v84", "v85", "v86", "v87", "v88", "v89", "v90", "v91", "v92", "v93", "v94", "v95", "v96",
        "v97", "v98", "v99", "v100", "v101", "v102", "v103", "v104", "v105", "v106", "v107", "v108", "v109", "v110", "v111", "v112", "v113", "v114", "v115",
        "v116", "v117", "v118", "v119", "v120", "v121", "v122", "v123", "v124", "v125", "v126", "v127", "v128", "v129", "v130", "v131", "v132", "v133",
        "v134", "v135", "v136", "v137", "v138", "v139", "v140", "v141", "v142", "v143", "v144", "v145", "v146", "v147", "v148", "v149", "v150", "v151",
        "v152", "v153", "v154", "v155", "v156", "v157", "v158", "v159", "v160", "v161", "v162", "v163", "v164", "v165", "v166", "v167", "v168", "v169",
        "v170", "v171", "v172", "v173", "v174", "v175", "a0", "a1", "a2", "a3", "a4", "a5", "a6", "a7", "a8", "a9", "a10", "a11", "a12", "a13", "a14", "a15",
        "a16", "a17", "a18", "a19", "a20", "a21", "a22", "a23", "a24", "a25", "a26", "a27", "a28", "a29", "a30", "a31", "a32", "a33", "a34", "a35", "a36",
        "a37", "a38", "a39", "a40", "a41", "a42", "a43", "a44", "a45", "a46", "a47", "a48", "a49", "a50", "a51", "a52", "a53", "a54", "a55", "a56", "a57",
        "a58", "a59", "a60", "a61", "a62", "a63", "a64", "a65", "a66", "a67", "a68", "a69", "a70", "a71", "a72", "a73", "a74", "a75", "a76", "a77", "a78",
        "a79", "a80", "a81", "a82", "a83", "a84", "a85", "a86", "a87", "a88", "a89", "a90", "a91", "a92", "a93", "a94", "a95", "a96", "a97", "a98", "a99",
        "a100", "a101", "a102", "a103", "a104", "a105", "a106", "a107", "a108", "a109", "a110", "a111", "a112", "a113", "a114", "a115", "a116", "a117",
        "a118", "a119", "a120", "a121", "a122", "a123", "a124", "a125", "a126", "a127", "a128", "a129", "a130", "a131", "a132", "a133", "a134", "a135",
        "a136", "a137", "a138", "a139", "a140", "a141", "a142", "a143", "a144", "a145", "a146", "a147", "a148", "a149", "a150", "a151", "a152", "a153",
        "a154", "a155", "a156", "a157", "a158", "a159", "a160", "a161", "a162", "a163", "a164", "a165", "a166", "a167", "a168", "a169", "a170", "a171",
        "a172", "a173", "a174", "a175", "a176", "a177", "a178", "a179", "a180", "a181", "a182", "a183", "a184", "a185", "a186", "a187", "a188", "a189",
        "a190", "a191", "a192", "a193", "a194", "a195", "a196", "a197", "a198", "a199", "a200", "a201", "a202", "a203", "a204", "a205", "a206", "a207",
        "a208", "a209", "a210", "a211", "a212", "a213", "a214", "a215", "a216", "a217", "a218", "a219", "a220", "a221", "a222", "a223", "s33", "s34", "s35",
        "s36", "s37", "s38", "s39", "s40", "s41", "s42", "s43", "s44", "s45", "s46", "s47", "s48", "s49", "s50", "s51", "s52", "s53", "s54", "s55", "s56",
        "s57", "s58", "s59", "s60", "s61", "s62", "s63", "s64", "s65", "s66", "s67"
  );
}

DEV void role_cal(const Params& p, LAS char* lds, u2 tag) {
  const int lane = threadIdx.x & 63, w = uni(threadIdx.x >> 6);
  const rsrc_t ws = mk_rsrc(p.ws, WS_END);
  unsigned res = 0xFFFFFFFFu, first[10];
#pragma unroll
  for (int n = 0; n < 10; ++n) {
    const GAS unsigned short* m0 = (const GAS unsigned short*)p.mem0 + (4 * (n / 5) + (lane >> 4)) * DM + 32 * (4 * (n % 5) + w) + (lane & 15);
    first[n] = min((unsigned)m0[0] | ((unsigned)m0[16] << 16), 0xFFFFFFFEu);
  }
  if (w == 0) {
    res = 0;
#pragma nounroll
    for (int bit = 13; bit < 15; ++bit) {
      unsigned best[2] = {0xFFFFFFFFu, 0xFFFFFFFFu};
#pragma nounroll
      for (int it = 0; it < ((bit == 13) ? 6 : 4); ++it) {
#pragma unroll
        for (int f = 0; f < 2; ++f) {
          drain();
          const unsigned long long t0 = __builtin_amdgcn_s_memtime();
          if (lane == 0) st4c(ws, WS_CAL + res + (f << bit) + (it & 1) * 4096, 0, (unsigned)it);
          drain();
          const unsigned long long t1 = __builtin_amdgcn_s_memtime();
          best[f] = min(best[f], (unsigned)(t1 - t0));
        }
      }
      res |= (best[1] < best[0] ? 1u : 0u) << bit;
    }
  } else if (w == 2) {
    for (unsigned spins = 0; spins < SPIN_CAP; ++spins) {
      cfence();
      const u4 a = ld16c(ws, ST_ARRIVE + lane * 32, 0), b = ld16c(ws, ST_ARRIVE + lane * 32 + 16, 0);
      if (__all(((a.x ^ tag.x) | (a.y ^ tag.y) | (a.z ^ tag.x) | (a.w ^ tag.y) | (b.x ^ tag.x) | (b.y ^ tag.y) | (b.z ^ tag.x) | (b.w ^ tag.y)) == 0)) {
        res = 0;
        if (lane == 0) { st8c(ws, ST_GO, 0, tag); st8c(ws, ST_EPOCH, 0, tag); }
        break;
      }
      __builtin_amdgcn_s_sleep(4);
    }
  }
  if (lane == 0) *(LAS unsigned*)(lds + w * 4) = res;
  bar();
  const u4 r = *(LAS u4*)lds;
  if (r.z == 0) {
    if (threadIdx.x == 0) st8c(ws, ST_FLIP, 0, (u2){tag.x, tag.y ^ (r.x << 17)});
    const int c = lane & 15, q = lane >> 4;
#pragma unroll
    for (int n = 0; n < 10; ++n)
      st4c(ws, (m_slot(n / 5, 4 * (n % 5) + w) ^ r.x) + ((c >> 3) * 8 + (q >> 1) * 4 + ((c >> 2) & 1) * 2 + (q & 1)) * 16 + (c & 3) * 4, 0, first[n]);
  }
}

DEV void role_red(const Params& p, LAS char* lds, u2 tag, int rg, int cg) {
  const int lane = threadIdx.x & 63, w = uni(threadIdx.x >> 6), c = lane & 15, r = lane >> 4;
  const int rot = 4 * rg + (cg & 1);
  const unsigned lb = (unsigned)(size_t)lds, stage = lb + w * 32768;
  u8v ad, ld = loader_regs<RM_RED>(lane, DM * 2, stage, 3);
  ad[0] = o_slot(8 * w, rg) + lane * 16;
  ad[1] = m_slot(rg, cg) + ((c >> 3) * 8 + (r >> 1) * 4 + ((c >> 2) & 1) * 2 + (r & 1)) * 16 + (c & 3) * 4;
  ad[2] = (lane & 1) ? 0xEEEEu : 0x4444u;
  ad[3] = lb + w * 512 + lane * 8;
  ad[4] = lb + lane * 8;
  ad[5] = ewa_word(lane & 31);
  ad[6] = ((w & 1) && lane == 0) ? ewb_word(rg, cg) + (w >> 1) * 4 : DROP;
  ad[7] = 0;
  const u4 rs = mk_rs(p.ws, WS_END), rw = mk_rs(p.wo, 4096 * DM * 2);
  asm volatile(
      "s_sleep 100\n\t"
      "s_nop 4\n\t"
      "s_mov_b32 s39, m0\n\t"
      "s_cmp_eq_u32 %[w], 0\n\t"
      "s_cselect_b64 s[66:67], 1, 0\n\t"
      "s_add_u32 m0, %[st], 0\n\t"
      "s_xor_b32 s44, %[rot], 0\n\t"
      "s_mul_i32 s44, s44, 163840\n\t"
      "s_add_u32 s44, s44, %[cb]\n\t"
      "s_add_u32 s43, s44, 0\n\t"
      "buffer_load_dwordx4 v16, %[rw], s43 offen lds\n\t"
      "buffer_load_dwordx4 v17, %[rw], s43 offen offset:1024 lds\n\t"
      "s_add_u32 m0, %[st], 2048\n\t"
      "s_add_u32 s43, s44, 81920\n\t"
      "buffer_load_dwordx4 v16, %[rw], s43 offen lds\n\t"
      "buffer_load_dwordx4 v17, %[rw], s43 offen offset:1024 lds\n\t"
      "s_add_u32 m0, %[st], 4096\n\t"
      "s_add_u32 s43, s44, 20480\n\t"
      "buffer_load_dwordx4 v16, %[rw], s43 offen lds\n\t"
      "buffer_load_dwordx4 v17, %[rw], s43 offen offset:1024 lds\n\t"
      "s_add_u32 m0, %[st], 6144\n\t"
      "s_add_u32 s43, s44, 102400\n\t"
      "buffer_load_dwordx4 v16, %[rw], s43 offen lds\n\t"
      "buffer_load_dwordx4 v17, %[rw], s43 offen offset:1024 lds\n\t"
      "s_add_u32 m0, %[st], 8192\n\t"
      "s_xor_b32 s44, %[rot], 1\n\t"
      "s_mul_i32 s44, s44, 163840\n\t"
      "s_add_u32 s44, s44, %[cb]\n\t"
      "s_add_u32 s43, s44, 0\n\t"
      "buffer_load_dwordx4 v16, %[rw], s43 offen lds\n\t"
      "buffer_load_dwordx4 v17, %[rw], s43 offen offset:1024 lds\n\t"
      "s_add_u32 m0, %[st], 10240\n\t"
      "s_add_u32 s43, s44, 81920\n\t"
      "buffer_load_dwordx4 v16, %[rw], s43 offen lds\n\t"
      "buffer_load_dwordx4 v17, %[rw], s43 offen offset:1024 lds\n\t"
      "s_add_u32 m0, %[st], 12288\n\t"
      "s_add_u32 s43, s44, 20480\n\t"
      "buffer_load_dwordx4 v16, %[rw], s43 offen lds\n\t"
      "buffer_load_dwordx4 v17, %[rw], s43 offen offset:1024 lds\n\t"
      "s_add_u32 m0, %[st], 14336\n\t"
      "s_add_u32 s43, s44, 102400\n\t"
      "buffer_load_dwordx4 v16, %[rw], s43 offen lds\n\t"
      "buffer_load_dwordx4 v17, %[rw], s43 offen offset:1024 lds\n\t"
      "s_waitcnt vmcnt(12)\n\t"
      "ds_read_b64_tr_b16 a[0:1], v18 offset:0\n\t"
      "ds_read_b64_tr_b16 a[2:3], v18 offset:1024\n\t"
      "ds_read_b64_tr_b16 a[8:9], v19 offset:0\n\t"
      "ds_read_b64_tr_b16 a[10:11], v19 offset:1024\n\t"
      "ds_read_b64_tr_b16 a[4:5], v18 offset:2048\n\t"
      "ds_read_b64_tr_b16 a[6:7], v18 offset:3072\n\t"
      "ds_read_b64_tr_b16 a[12:13], v19 offset:2048\n\t"
      "ds_read_b64_tr_b16 a[14:15], v19 offset:3072\n\t"
      "s_waitcnt lgkmcnt(0)\n\t"
      "s_add_u32 m0, %[st], 0\n\t"
      "s_xor_b32 s44, %[rot], 2\n\t"
      "s_mul_i32 s44, s44, 163840\n\t"
      "s_add_u32 s44, s44, %[cb]\n\t"
      "s_add_u32 s43, s44, 0\n\t"
      "buffer_load_dwordx4 v16, %[rw], s43 offen lds\n\t"
      "buffer_load_dwordx4 v17, %[rw], s43 offen offset:1024 lds\n\t"
      "s_add_u32 m0, %[st], 2048\n\t"
      "s_add_u32 s43, s44, 81920\n\t"
      "buffer_load_dwordx4 v16, %[rw], s43 offen lds\n\t"
      "buffer_load_dwordx4 v17, %[rw], s43 offen offset:1024 lds\n\t"
      "s_waitcnt vmcnt(12)\n\t"
      "ds_read_b64_tr_b16 a[16:17], v18 offset:4096\n\t"
      "ds_read_b64_tr_b16 a[18:19], v18 offset:5120\n\t"
      "ds_read_b64_tr_b16 a[24:25], v19 offset:4096\n\t"
      "ds_read_b64_tr_b16 a[26:27], v19 offset:5120\n\t"
      "ds_read_b64_tr_b16 a[20:21], v18 offset:6144\n\t"
      "ds_read_b64_tr_b16 a[22:23], v18 offset:7168\n\t"
      "ds_read_b64_tr_b16 a[28:29], v19 offset:6144\n\t"
      "ds_read_b64_tr_b16 a[30:31], v19 offset:7168\n\t"
      "s_waitcnt lgkmcnt(0)\n\t"
      "s_add_u32 m0, %[st], 4096\n\t"
      "s_add_u32 s43, s44, 20480\n\t"
      "buffer_load_dwordx4 v16, %[rw], s43 offen lds\n\t"
      "buffer_load_dwordx4 v17, %[rw], s43 offen offset:1024 lds\n\t"
      "s_add_u32 m0, %[st], 6144\n\t"
      "s_add_u32 s43, s44, 102400\n\t"
      "buffer_load_dwordx4 v16, %[rw], s43 offen lds\n\t"
      "buffer_load_dwordx4 v17, %[rw], s43 offen offset:1024 lds\n\t"
      "s_waitcnt vmcnt(12)\n\t"
      "ds_read_b64_tr_b16 a[32:33], v18 offset:8192\n\t"
      "ds_read_b64_tr_b16 a[34:35], v18 offset:9216\n\t"
      "ds_read_b64_tr_b16 a[40:41], v19 offset:8192\n\t"
      "ds_read_b64_tr_b16 a[42:43], v19 offset:9216\n\t"
      "ds_read_b64_tr_b16 a[36:37], v18 offset:10240\n\t"
      "ds_read_b64_tr_b16 a[38:39], v18 offset:11264\n\t"
      "ds_read_b64_tr_b16 a[44:45], v19 offset:10240\n\t"
      "ds_read_b64_tr_b16 a[46:47], v19 offset:11264\n\t"
      "s_waitcnt lgkmcnt(0)\n\t"
      "s_add_u32 m0, %[st], 8192\n\t"
      "s_xor_b32 s44, %[rot], 3\n\t"
      "s_mul_i32 s44, s44, 163840\n\t"
      "s_add_u32 s44, s44, %[cb]\n\t"
      "s_add_u32 s43, s44, 0\n\t"
      "buffer_load_dwordx4 v16, %[rw], s43 offen lds\n\t"
      "buffer_load_dwordx4 v17, %[rw], s43 offen offset:1024 lds\n\t"
      "s_add_u32 m0, %[st], 10240\n\t"
      "s_add_u32 s43, s44, 81920\n\t"
      "buffer_load_dwordx4 v16, %[rw], s43 offen lds\n\t"
      "buffer_load_dwordx4 v17, %[rw], s43 offen offset:1024 lds\n\t"
      "s_waitcnt vmcnt(12)\n\t"
      "ds_read_b64_tr_b16 a[48:49], v18 offset:12288\n\t"
      "ds_read_b64_tr_b16 a[50:51], v18 offset:13312\n\t"
      "ds_read_b64_tr_b16 a[56:57], v19 offset:12288\n\t"
      "ds_read_b64_tr_b16 a[58:59], v19 offset:13312\n\t"
      "ds_read_b64_tr_b16 a[52:53], v18 offset:14336\n\t"
      "ds_read_b64_tr_b16 a[54:55], v18 offset:15360\n\t"
      "ds_read_b64_tr_b16 a[60:61], v19 offset:14336\n\t"
      "ds_read_b64_tr_b16 a[62:63], v19 offset:15360\n\t"
      "s_waitcnt lgkmcnt(0)\n\t"
      "s_add_u32 m0, %[st], 12288\n\t"
      "s_add_u32 s43, s44, 20480\n\t"
      "buffer_load_dwordx4 v16, %[rw], s43 offen lds\n\t"
      "buffer_load_dwordx4 v17, %[rw], s43 offen offset:1024 lds\n\t"
      "s_add_u32 m0, %[st], 14336\n\t"
      "s_add_u32 s43, s44, 102400\n\t"
      "buffer_load_dwordx4 v16, %[rw], s43 offen lds\n\t"
      "buffer_load_dwordx4 v17, %[rw], s43 offen offset:1024 lds\n\t"
      "s_waitcnt vmcnt(12)\n\t"
      "ds_read_b64_tr_b16 a[64:65], v18 offset:0\n\t"
      "ds_read_b64_tr_b16 a[66:67], v18 offset:1024\n\t"
      "ds_read_b64_tr_b16 a[72:73], v19 offset:0\n\t"
      "ds_read_b64_tr_b16 a[74:75], v19 offset:1024\n\t"
      "ds_read_b64_tr_b16 a[68:69], v18 offset:2048\n\t"
      "ds_read_b64_tr_b16 a[70:71], v18 offset:3072\n\t"
      "ds_read_b64_tr_b16 a[76:77], v19 offset:2048\n\t"
      "ds_read_b64_tr_b16 a[78:79], v19 offset:3072\n\t"
      "s_waitcnt lgkmcnt(0)\n\t"
      "s_add_u32 m0, %[st], 0\n\t"
      "s_xor_b32 s44, %[rot], 4\n\t"
      "s_mul_i32 s44, s44, 163840\n\t"
      "s_add_u32 s44, s44, %[cb]\n\t"
      "s_add_u32 s43, s44, 0\n\t"
      "buffer_load_dwordx4 v16, %[rw], s43 offen lds\n\t"
      "buffer_load_dwordx4 v17, %[rw], s43 offen offset:1024 lds\n\t"
      "s_add_u32 m0, %[st], 2048\n\t"
      "s_add_u32 s43, s44, 81920\n\t"
      "buffer_load_dwordx4 v16, %[rw], s43 offen lds\n\t"
      "buffer_load_dwordx4 v17, %[rw], s43 offen offset:1024 lds\n\t"
      "s_waitcnt vmcnt(12)\n\t"
      "ds_read_b64_tr_b16 a[80:81], v18 offset:4096\n\t"
      "ds_read_b64_tr_b16 a[82:83], v18 offset:5120\n\t"
      "ds_read_b64_tr_b16 a[88:89], v19 offset:4096\n\t"
      "ds_read_b64_tr_b16 a[90:91], v19 offset:5120\n\t"
      "ds_read_b64_tr_b16 a[84:85], v18 offset:6144\n\t"
      "ds_read_b64_tr_b16 a[86:87], v18 offset:7168\n\t"
      "ds_read_b64_tr_b16 a[92:93], v19 offset:6144\n\t"
      "ds_read_b64_tr_b16 a[94:95], v19 offset:7168\n\t"
      "s_waitcnt lgkmcnt(0)\n\t"
      "s_add_u32 m0, %[st], 4096\n\t"
      "s_add_u32 s43, s44, 20480\n\t"
      "buffer_load_dwordx4 v16, %[rw], s43 offen lds\n\t"
      "buffer_load_dwordx4 v17, %[rw], s43 offen offset:1024 lds\n\t"
      "s_add_u32 m0, %[st], 6144\n\t"
      "s_add_u32 s43, s44, 102400\n\t"
      "buffer_load_dwordx4 v16, %[rw], s43 offen lds\n\t"
      "buffer_load_dwordx4 v17, %[rw], s43 offen offset:1024 lds\n\t"
      "s_waitcnt vmcnt(12)\n\t"
      "ds_read_b64_tr_b16 a[96:97], v18 offset:8192\n\t"
      "ds_read_b64_tr_b16 a[98:99], v18 offset:9216\n\t"
      "ds_read_b64_tr_b16 a[104:105], v19 offset:8192\n\t"
      "ds_read_b64_tr_b16 a[106:107], v19 offset:9216\n\t"
      "ds_read_b64_tr_b16 a[100:101], v18 offset:10240\n\t"
      "ds_read_b64_tr_b16 a[102:103], v18 offset:11264\n\t"
      "ds_read_b64_tr_b16 a[108:109], v19 offset:10240\n\t"
      "ds_read_b64_tr_b16 a[110:111], v19 offset:11264\n\t"
      "s_waitcnt lgkmcnt(0)\n\t"
      "s_add_u32 m0, %[st], 8192\n\t"
      "s_xor_b32 s44, %[rot], 5\n\t"
      "s_mul_i32 s44, s44, 163840\n\t"
      "s_add_u32 s44, s44, %[cb]\n\t"
      "s_add_u32 s43, s44, 0\n\t"
      "buffer_load_dwordx4 v16, %[rw], s43 offen lds\n\t"
      "buffer_load_dwordx4 v17, %[rw], s43 offen offset:1024 lds\n\t"
      "s_add_u32 m0, %[st], 10240\n\t"
      "s_add_u32 s43, s44, 81920\n\t"
      "buffer_load_dwordx4 v16, %[rw], s43 offen lds\n\t"
      "buffer_load_dwordx4 v17, %[rw], s43 offen offset:1024 lds\n\t"
      "s_waitcnt vmcnt(12)\n\t"
      "ds_read_b64_tr_b16 a[112:113], v18 offset:12288\n\t"
      "ds_read_b64_tr_b16 a[114:115], v18 offset:13312\n\t"
      "ds_read_b64_tr_b16 a[120:121], v19 offset:12288\n\t"
      "ds_read_b64_tr_b16 a[122:123], v19 offset:13312\n\t"
      "ds_read_b64_tr_b16 a[116:117], v18 offset:14336\n\t"
      "ds_read_b64_tr_b16 a[118:119], v18 offset:15360\n\t"
      "ds_read_b64_tr_b16 a[124:125], v19 offset:14336\n\t"
      "ds_read_b64_tr_b16 a[126:127], v19 offset:15360\n\t"
      "s_waitcnt lgkmcnt(0)\n\t"
      "s_add_u32 m0, %[st], 12288\n\t"
      "s_add_u32 s43, s44, 20480\n\t"
      "buffer_load_dwordx4 v16, %[rw], s43 offen lds\n\t"
      "buffer_load_dwordx4 v17, %[rw], s43 offen offset:1024 lds\n\t"
      "s_add_u32 m0, %[st], 14336\n\t"
      "s_add_u32 s43, s44, 102400\n\t"
      "buffer_load_dwordx4 v16, %[rw], s43 offen lds\n\t"
      "buffer_load_dwordx4 v17, %[rw], s43 offen offset:1024 lds\n\t"
      "s_waitcnt vmcnt(12)\n\t"
      "ds_read_b64_tr_b16 a[128:129], v18 offset:0\n\t"
      "ds_read_b64_tr_b16 a[130:131], v18 offset:1024\n\t"
      "ds_read_b64_tr_b16 a[136:137], v19 offset:0\n\t"
      "ds_read_b64_tr_b16 a[138:139], v19 offset:1024\n\t"
      "ds_read_b64_tr_b16 a[132:133], v18 offset:2048\n\t"
      "ds_read_b64_tr_b16 a[134:135], v18 offset:3072\n\t"
      "ds_read_b64_tr_b16 a[140:141], v19 offset:2048\n\t"
      "ds_read_b64_tr_b16 a[142:143], v19 offset:3072\n\t"
      "s_waitcnt lgkmcnt(0)\n\t"
      "s_add_u32 m0, %[st], 0\n\t"
      "s_xor_b32 s44, %[rot], 6\n\t"
      "s_mul_i32 s44, s44, 163840\n\t"
      "s_add_u32 s44, s44, %[cb]\n\t"
      "s_add_u32 s43, s44, 0\n\t"
      "buffer_load_dwordx4 v16, %[rw], s43 offen lds\n\t"
      "buffer_load_dwordx4 v17, %[rw], s43 offen offset:1024 lds\n\t"
      "s_add_u32 m0, %[st], 2048\n\t"
      "s_add_u32 s43, s44, 81920\n\t"
      "buffer_load_dwordx4 v16, %[rw], s43 offen lds\n\t"
      "buffer_load_dwordx4 v17, %[rw], s43 offen offset:1024 lds\n\t"
      "s_waitcnt vmcnt(12)\n\t"
      "ds_read_b64_tr_b16 a[144:145], v18 offset:4096\n\t"
      "ds_read_b64_tr_b16 a[146:147], v18 offset:5120\n\t"
      "ds_read_b64_tr_b16 a[152:153], v19 offset:4096\n\t"
      "ds_read_b64_tr_b16 a[154:155], v19 offset:5120\n\t"
      "ds_read_b64_tr_b16 a[148:149], v18 offset:6144\n\t"
      "ds_read_b64_tr_b16 a[150:151], v18 offset:7168\n\t"
      "ds_read_b64_tr_b16 a[156:157], v19 offset:6144\n\t"
      "ds_read_b64_tr_b16 a[158:159], v19 offset:7168\n\t"
      "s_waitcnt lgkmcnt(0)\n\t"
      "s_add_u32 m0, %[st], 4096\n\t"
      "s_add_u32 s43, s44, 20480\n\t"
      "buffer_load_dwordx4 v16, %[rw], s43 offen lds\n\t"
      "buffer_load_dwordx4 v17, %[rw], s43 offen offset:1024 lds\n\t"
      "s_add_u32 m0, %[st], 6144\n\t"
      "s_add_u32 s43, s44, 102400\n\t"
      "buffer_load_dwordx4 v16, %[rw], s43 offen lds\n\t"
      "buffer_load_dwordx4 v17, %[rw], s43 offen offset:1024 lds\n\t"
      "s_waitcnt vmcnt(12)\n\t"
      "ds_read_b64_tr_b16 a[160:161], v18 offset:8192\n\t"
      "ds_read_b64_tr_b16 a[162:163], v18 offset:9216\n\t"
      "ds_read_b64_tr_b16 a[168:169], v19 offset:8192\n\t"
      "ds_read_b64_tr_b16 a[170:171], v19 offset:9216\n\t"
      "ds_read_b64_tr_b16 a[164:165], v18 offset:10240\n\t"
      "ds_read_b64_tr_b16 a[166:167], v18 offset:11264\n\t"
      "ds_read_b64_tr_b16 a[172:173], v19 offset:10240\n\t"
      "ds_read_b64_tr_b16 a[174:175], v19 offset:11264\n\t"
      "s_waitcnt lgkmcnt(0)\n\t"
      "s_add_u32 m0, %[st], 8192\n\t"
      "s_xor_b32 s44, %[rot], 7\n\t"
      "s_mul_i32 s44, s44, 163840\n\t"
      "s_add_u32 s44, s44, %[cb]\n\t"
      "s_add_u32 s43, s44, 0\n\t"
      "buffer_load_dwordx4 v16, %[rw], s43 offen lds\n\t"
      "buffer_load_dwordx4 v17, %[rw], s43 offen offset:1024 lds\n\t"
      "s_add_u32 m0, %[st], 10240\n\t"
      "s_add_u32 s43, s44, 81920\n\t"
      "buffer_load_dwordx4 v16, %[rw], s43 offen lds\n\t"
      "buffer_load_dwordx4 v17, %[rw], s43 offen offset:1024 lds\n\t"
      "s_waitcnt vmcnt(12)\n\t"
      "ds_read_b64_tr_b16 a[176:177], v18 offset:12288\n\t"
      "ds_read_b64_tr_b16 a[178:179], v18 offset:13312\n\t"
      "ds_read_b64_tr_b16 a[184:185], v19 offset:12288\n\t"
      "ds_read_b64_tr_b16 a[186:187], v19 offset:13312\n\t"
      "ds_read_b64_tr_b16 a[180:181], v18 offset:14336\n\t"
      "ds_read_b64_tr_b16 a[182:183], v18 offset:15360\n\t"
      "ds_read_b64_tr_b16 a[188:189], v19 offset:14336\n\t"
      "ds_read_b64_tr_b16 a[190:191], v19 offset:15360\n\t"
      "s_waitcnt lgkmcnt(0)\n\t"
      "s_add_u32 m0, %[st], 12288\n\t"
      "s_add_u32 s43, s44, 20480\n\t"
      "buffer_load_dwordx4 v16, %[rw], s43 offen lds\n\t"
      "buffer_load_dwordx4 v17, %[rw], s43 offen offset:1024 lds\n\t"
      "s_add_u32 m0, %[st], 14336\n\t"
      "s_add_u32 s43, s44, 102400\n\t"
      "buffer_load_dwordx4 v16, %[rw], s43 offen lds\n\t"
      "buffer_load_dwordx4 v17, %[rw], s43 offen offset:1024 lds\n\t"
      "s_waitcnt vmcnt(12)\n\t"
      "ds_read_b64_tr_b16 a[192:193], v18 offset:0\n\t"
      "ds_read_b64_tr_b16 a[194:195], v18 offset:1024\n\t"
      "ds_read_b64_tr_b16 a[200:201], v19 offset:0\n\t"
      "ds_read_b64_tr_b16 a[202:203], v19 offset:1024\n\t"
      "ds_read_b64_tr_b16 a[196:197], v18 offset:2048\n\t"
      "ds_read_b64_tr_b16 a[198:199], v18 offset:3072\n\t"
      "ds_read_b64_tr_b16 a[204:205], v19 offset:2048\n\t"
      "ds_read_b64_tr_b16 a[206:207], v19 offset:3072\n\t"
      "s_waitcnt lgkmcnt(0)\n\t"
      "s_waitcnt vmcnt(8)\n\t"
      "ds_read_b64_tr_b16 a[208:209], v18 offset:4096\n\t"
      "ds_read_b64_tr_b16 a[210:211], v18 offset:5120\n\t"
      "ds_read_b64_tr_b16 a[216:217], v19 offset:4096\n\t"
      "ds_read_b64_tr_b16 a[218:219], v19 offset:5120\n\t"
      "ds_read_b64_tr_b16 a[212:213], v18 offset:6144\n\t"
      "ds_read_b64_tr_b16 a[214:215], v18 offset:7168\n\t"
      "ds_read_b64_tr_b16 a[220:221], v19 offset:6144\n\t"
      "ds_read_b64_tr_b16 a[222:223], v19 offset:7168\n\t"
      "s_waitcnt lgkmcnt(0)\n\t"
      "s_waitcnt vmcnt(4)\n\t"
      "ds_read_b64_tr_b16 a[224:225], v18 offset:8192\n\t"
      "ds_read_b64_tr_b16 a[226:227], v18 offset:9216\n\t"
      "ds_read_b64_tr_b16 a[232:233], v19 offset:8192\n\t"
      "ds_read_b64_tr_b16 a[234:235], v19 offset:9216\n\t"
      "ds_read_b64_tr_b16 a[228:229], v18 offset:10240\n\t"
      "ds_read_b64_tr_b16 a[230:231], v18 offset:11264\n\t"
      "ds_read_b64_tr_b16 a[236:237], v19 offset:10240\n\t"
      "ds_read_b64_tr_b16 a[238:239], v19 offset:11264\n\t"
      "s_waitcnt lgkmcnt(0)\n\t"
      "s_waitcnt vmcnt(0)\n\t"
      "ds_read_b64_tr_b16 a[240:241], v18 offset:12288\n\t"
      "ds_read_b64_tr_b16 a[242:243], v18 offset:13312\n\t"
      "ds_read_b64_tr_b16 a[248:249], v19 offset:12288\n\t"
      "ds_read_b64_tr_b16 a[250:251], v19 offset:13312\n\t"
      "ds_read_b64_tr_b16 a[244:245], v18 offset:14336\n\t"
      "ds_read_b64_tr_b16 a[246:247], v18 offset:15360\n\t"
      "ds_read_b64_tr_b16 a[252:253], v19 offset:14336\n\t"
      "ds_read_b64_tr_b16 a[254:255], v19 offset:15360\n\t"
      "s_waitcnt lgkmcnt(0)\n\t"
      "s_waitcnt lgkmcnt(0)\n\t"
      "s_barrier\n\t"
      "s_mov_b32 s42, 0\n\t"
      "s_mov_b32 s59, 1179776\n\t"
      "LGW%=:\n\t"
      "buffer_load_dwordx2 v[24:25], off, %[rs], s59 sc1\n\t"
      "s_add_u32 s42, s42, 1\n\t"
      "s_cmp_gt_u32 s42, 400000\n\t"
      "s_cbranch_scc1 LFAIL%=\n\t"
      "s_waitcnt vmcnt(0)\n\t"
      "v_readfirstlane_b32 s60, v24\n\t"
      "v_readfirstlane_b32 s43, v25\n\t"
      "s_cmp_lg_u32 s60, %[t0]\n\t"
      "s_cbranch_scc1 LGW%=\n\t"
      "s_xor_b32 s43, s43, %[t1]\n\t"
      "s_lshl_b32 s60, s43, 2\n\t"
      "s_cbranch_scc1 LGW%=\n\t"
      "s_lshr_b32 s43, s43, 17\n\t"
      "v_xor_b32 v0, s43, v0\n\t"
      "v_xor_b32 v1, s43, v1\n\t"
      "v_xor_b32 v5, s43, v5\n\t"
      "v_xor_b32 v6, s43, v6\n\t"
      "s_mov_b32 s40, 0\n\t"
      "v_mov_b32 v7, -1\n\t"
      "v_mov_b32 v9, v6\n\t"
      "s_mov_b32 s35, 0\n\t"
      "s_mov_b32 s33, 64\n\t"
      "v_mov_b32 v10, 8192\n\t"
      "v_mov_b32 v11, 0\n\t"
      "ds_write_b32 v10, v11\n\t"
      "s_xor_b32 s43, %[rot], 0\n\t"
      "s_lshl_b32 s44, s43, 10\n\t"
      "s_xor_b32 s43, %[rot], 1\n\t"
      "s_lshl_b32 s45, s43, 10\n\t"
      "s_xor_b32 s43, %[rot], 2\n\t"
      "s_lshl_b32 s46, s43, 10\n\t"
      "s_xor_b32 s43, %[rot], 3\n\t"
      "s_lshl_b32 s47, s43, 10\n\t"
      "s_xor_b32 s43, %[rot], 4\n\t"
      "s_lshl_b32 s48, s43, 10\n\t"
      "s_xor_b32 s43, %[rot], 5\n\t"
      "s_lshl_b32 s49, s43, 10\n\t"
      "s_xor_b32 s43, %[rot], 6\n\t"
      "s_lshl_b32 s50, s43, 10\n\t"
      "s_xor_b32 s43, %[rot], 7\n\t"
      "s_lshl_b32 s51, s43, 10\n\t"
      "s_branch LOOP%=\n\t"
      ".p2align 6\n\t"
      "s_nop 0\n\t"
      "s_nop 0\n\t"
      "s_nop 0\n\t"
      "s_nop 0\n\t"
      "LOOP%=:\n\t"
      "s_and_b32 s43, s40, 3\n\t"
      "s_lshl_b32 s52, s43, 17\n\t"
      "v_add_u32 v8, s52, v0\n\t"
      "s_add_u32 s43, s40, 1\n\t"
      "s_and_b32 s43, s43, 3\n\t"
      "s_lshl_b32 s54, s43, 15\n\t"
      "s_xor_b32 s55, s54, 0x10000\n\t"
      "s_mov_b32 s42, 0\n\t"
      "s_add_u32 s43, s40, 1\n\t"
      "v_mov_b32 v14, s43\n\t"
      "s_cmp_lg_u32 %[w], 0\n\t"
      "s_cbranch_scc1 LEWB%=\n\t"
      "s_mov_b32 exec_lo, -1\n\t"
      "s_mov_b32 exec_hi, 0\n\t"
      "v_mov_b32 v11, 0\n\t"
      "buffer_load_dword v16, v5, %[rs], 0 offen sc1\n\t"
      "ds_wrxchg_rtn_b32 v11, v10, v11\n\t"
      "s_nop 8\n\t"
      "buffer_load_dword v17, v5, %[rs], 0 offen sc1\n\t"
      "s_nop 9\n\t"
      "buffer_load_dword v18, v5, %[rs], 0 offen sc1\n\t"
      "s_waitcnt lgkmcnt(0)\n\t"
      "v_cmp_ne_u32 vcc, 0, v11\n\t"
      "s_cbranch_vccz LEWN%=\n\t"
      "s_cmp_lt_u32 s40, 2\n\t"
      "s_cbranch_scc1 LEWN%=\n\t"
      "s_cmp_le_i32 s40, s33\n\t"
      "s_addc_u32 s35, s35, 0\n\t"
      "s_add_u32 s33, s40, 64\n\t"
      "LEWN%=:\n\t"
      "s_mov_b32 s34, s35\n\t"
      "s_nop 4\n\t"
      "buffer_load_dword v19, v5, %[rs], 0 offen sc1\n\t"
      "s_nop 9\n\t"
      "buffer_load_dword v20, v5, %[rs], 0 offen sc1\n\t"
      "s_nop 9\n\t"
      "buffer_load_dword v21, v5, %[rs], 0 offen sc1\n\t"
      "s_nop 9\n\t"
      "buffer_load_dword v22, v5, %[rs], 0 offen sc1\n\t"
      "s_nop 9\n\t"
      "buffer_load_dword v23, v5, %[rs], 0 offen sc1\n\t"
      "s_nop 9\n\t"
      "buffer_load_dword v24, v5, %[rs], 0 offen sc1\n\t"
      "s_nop 9\n\t"
      "buffer_load_dword v25, v5, %[rs], 0 offen sc1\n\t"
      "s_nop 9\n\t"
      "buffer_load_dword v26, v5, %[rs], 0 offen sc1\n\t"
      "s_nop 9\n\t"
      "buffer_load_dword v27, v5, %[rs], 0 offen sc1\n\t"
      "s_nop 9\n\t"
      "LEW%=:\n\t"
      "s_waitcnt vmcnt(11)\n\t"
      "v_cmp_gt_i32 vcc, s40, v16\n\t"
      "s_cbranch_vccz LEWD%=\n\t"
      "buffer_load_dword v16, v5, %[rs], 0 offen sc1\n\t"
      "s_nop 6\n\t"
      "s_waitcnt vmcnt(11)\n\t"
      "v_cmp_gt_i32 vcc, s40, v17\n\t"
      "s_cbranch_vccz LEWD%=\n\t"
      "buffer_load_dword v17, v5, %[rs], 0 offen sc1\n\t"
      "s_nop 6\n\t"
      "s_waitcnt vmcnt(11)\n\t"
      "v_cmp_gt_i32 vcc, s40, v18\n\t"
      "s_cbranch_vccz LEWD%=\n\t"
      "buffer_load_dword v18, v5, %[rs], 0 offen sc1\n\t"
      "s_nop 6\n\t"
      "s_waitcnt vmcnt(11)\n\t"
      "v_cmp_gt_i32 vcc, s40, v19\n\t"
      "s_cbranch_vccz LEWD%=\n\t"
      "buffer_load_dword v19, v5, %[rs], 0 offen sc1\n\t"
      "s_nop 6\n\t"
      "s_waitcnt vmcnt(11)\n\t"
      "v_cmp_gt_i32 vcc, s40, v20\n\t"
      "s_cbranch_vccz LEWD%=\n\t"
      "buffer_load_dword v20, v5, %[rs], 0 offen sc1\n\t"
      "s_nop 6\n\t"
      "s_waitcnt vmcnt(11)\n\t"
      "v_cmp_gt_i32 vcc, s40, v21\n\t"
      "s_cbranch_vccz LEWD%=\n\t"
      "buffer_load_dword v21, v5, %[rs], 0 offen sc1\n\t"
      "s_nop 6\n\t"
      "s_waitcnt vmcnt(11)\n\t"
      "v_cmp_gt_i32 vcc, s40, v22\n\t"
      "s_cbranch_vccz LEWD%=\n\t"
      "buffer_load_dword v22, v5, %[rs], 0 offen sc1\n\t"
      "s_nop 6\n\t"
      "s_waitcnt vmcnt(11)\n\t"
      "v_cmp_gt_i32 vcc, s40, v23\n\t"
      "s_cbranch_vccz LEWD%=\n\t"
      "buffer_load_dword v23, v5, %[rs], 0 offen sc1\n\t"
      "s_nop 6\n\t"
      "s_waitcnt vmcnt(11)\n\t"
      "v_cmp_gt_i32 vcc, s40, v24\n\t"
      "s_cbranch_vccz LEWD%=\n\t"
      "buffer_load_dword v24, v5, %[rs], 0 offen sc1\n\t"
      "s_nop 6\n\t"
      "s_waitcnt vmcnt(11)\n\t"
      "v_cmp_gt_i32 vcc, s40, v25\n\t"
      "s_cbranch_vccz LEWD%=\n\t"
      "buffer_load_dword v25, v5, %[rs], 0 offen sc1\n\t"
      "s_nop 6\n\t"
      "s_waitcnt vmcnt(11)\n\t"
      "v_cmp_gt_i32 vcc, s40, v26\n\t"
      "s_cbranch_vccz LEWD%=\n\t"
      "buffer_load_dword v26, v5, %[rs], 0 offen sc1\n\t"
      "s_nop 6\n\t"
      "s_waitcnt vmcnt(11)\n\t"
      "v_cmp_gt_i32 vcc, s40, v27\n\t"
      "s_cbranch_vccz LEWD%=\n\t"
      "buffer_load_dword v27, v5, %[rs], 0 offen sc1\n\t"
      "s_nop 6\n\t"
      "s_add_u32 s42, s42, 1\n\t"
      "s_cmp_lt_u32 s42, 100000\n\t"
      "s_cbranch_scc1 LEW%=\n\t"
      "s_branch LFAIL%=\n\t"
      "LEWE%=:\n\t"
      "s_nop 0\n\t"
      "LEWD%=:\n\t"
      "s_sub_u32 s34, s34, 1\n\t"
      "s_cbranch_scc0 LEWE%=\n\t"
      "s_mov_b64 exec, -1\n\t"
      "LEWB%=:\n\t"
      "s_barrier\n\t"
      "s_mov_b32 s42, 0\n\t"
      "LPOLL%=:\n\t"
      "buffer_load_dwordx4 v[16:19], v8, %[rs], s44 offen sc1\n\t"
      "v_mov_b64 v[48:49], 0\n\t"
      "s_nop 8\n\t"
      "buffer_load_dwordx4 v[20:23], v8, %[rs], s45 offen sc1\n\t"
      "v_mov_b64 v[50:51], 0\n\t"
      "s_nop 8\n\t"
      "buffer_load_dwordx4 v[24:27], v8, %[rs], s46 offen sc1\n\t"
      "v_mov_b64 v[52:53], 0\n\t"
      "s_nop 8\n\t"
      "buffer_load_dwordx4 v[28:31], v8, %[rs], s47 offen sc1\n\t"
      "v_mov_b64 v[54:55], 0\n\t"
      "s_nop 8\n\t"
      "buffer_load_dwordx4 v[32:35], v8, %[rs], s48 offen sc1\n\t"
      "v_mov_b64 v[56:57], 0\n\t"
      "s_nop 8\n\t"
      "buffer_load_dwordx4 v[36:39], v8, %[rs], s49 offen sc1\n\t"
      "v_mov_b64 v[58:59], 0\n\t"
      "s_nop 8\n\t"
      "buffer_load_dwordx4 v[40:43], v8, %[rs], s50 offen sc1\n\t"
      "v_mov_b64 v[60:61], 0\n\t"
      "s_nop 8\n\t"
      "buffer_load_dwordx4 v[44:47], v8, %[rs], s51 offen sc1\n\t"
      "v_mov_b64 v[62:63], 0\n\t"
      "v_mov_b32 v6, 0\n\t"
      "s_add_u32 s42, s42, 1\n\t"
      "s_cmp_gt_u32 s42, 400000\n\t"
      "s_cbranch_scc1 LFAIL%=\n\t"
      "s_waitcnt vmcnt(7)\n\t"
      "v_max3_u32 v6, v6, v16, v17\n\t"
      "v_smfmac_f32_16x16x64_bf16 v[48:51], v[16:19], a[0:7], v2\n\t"
      "v_max3_u32 v6, v6, v18, v19\n\t"
      "v_cmp_eq_u32 vcc, -1, v6\n\t"
      "v_smfmac_f32_16x16x64_bf16 v[52:55], v[16:19], a[16:23], v2\n\t"
      "s_cbranch_vccnz LRETRY%=\n\t"
      "buffer_store_dword v14, v9, %[rs], 0 offen sc1\n\t"
      "v_smfmac_f32_16x16x64_bf16 v[56:59], v[16:19], a[8:15], v2\n\t"
      "v_smfmac_f32_16x16x64_bf16 v[60:63], v[16:19], a[24:31], v2\n\t"
      "s_waitcnt vmcnt(7)\n\t"
      "v_smfmac_f32_16x16x64_bf16 v[48:51], v[20:23], a[32:39], v2\n\t"
      "v_max3_u32 v6, v6, v20, v21\n\t"
      "v_smfmac_f32_16x16x64_bf16 v[52:55], v[20:23], a[48:55], v2\n\t"
      "v_max3_u32 v6, v6, v22, v23\n\t"
      "v_smfmac_f32_16x16x64_bf16 v[56:59], v[20:23], a[40:47], v2\n\t"
      "v_smfmac_f32_16x16x64_bf16 v[60:63], v[20:23], a[56:63], v2\n\t"
      "s_waitcnt vmcnt(6)\n\t"
      "v_smfmac_f32_16x16x64_bf16 v[48:51], v[24:27], a[64:71], v2\n\t"
      "v_max3_u32 v6, v6, v24, v25\n\t"
      "v_smfmac_f32_16x16x64_bf16 v[52:55], v[24:27], a[80:87], v2\n\t"
      "v_max3_u32 v6, v6, v26, v27\n\t"
      "v_smfmac_f32_16x16x64_bf16 v[56:59], v[24:27], a[72:79], v2\n\t"
      "v_smfmac_f32_16x16x64_bf16 v[60:63], v[24:27], a[88:95], v2\n\t"
      "s_waitcnt vmcnt(5)\n\t"
      "v_smfmac_f32_16x16x64_bf16 v[48:51], v[28:31], a[96:103], v2\n\t"
      "v_max3_u32 v6, v6, v28, v29\n\t"
      "v_smfmac_f32_16x16x64_bf16 v[52:55], v[28:31], a[112:119], v2\n\t"
      "v_max3_u32 v6, v6, v30, v31\n\t"
      "v_smfmac_f32_16x16x64_bf16 v[56:59], v[28:31], a[104:111], v2\n\t"
      "v_smfmac_f32_16x16x64_bf16 v[60:63], v[28:31], a[120:127], v2\n\t"
      "s_waitcnt vmcnt(4)\n\t"
      "v_smfmac_f32_16x16x64_bf16 v[48:51], v[32:35], a[128:135], v2\n\t"
      "v_max3_u32 v6, v6, v32, v33\n\t"
      "v_smfmac_f32_16x16x64_bf16 v[52:55], v[32:35], a[144:151], v2\n\t"
      "v_max3_u32 v6, v6, v34, v35\n\t"
      "v_smfmac_f32_16x16x64_bf16 v[56:59], v[32:35], a[136:143], v2\n\t"
      "v_smfmac_f32_16x16x64_bf16 v[60:63], v[32:35], a[152:159], v2\n\t"
      "s_waitcnt vmcnt(3)\n\t"
      "v_smfmac_f32_16x16x64_bf16 v[48:51], v[36:39], a[160:167], v2\n\t"
      "v_max3_u32 v6, v6, v36, v37\n\t"
      "v_smfmac_f32_16x16x64_bf16 v[52:55], v[36:39], a[176:183], v2\n\t"
      "v_max3_u32 v6, v6, v38, v39\n\t"
      "v_smfmac_f32_16x16x64_bf16 v[56:59], v[36:39], a[168:175], v2\n\t"
      "v_smfmac_f32_16x16x64_bf16 v[60:63], v[36:39], a[184:191], v2\n\t"
      "s_waitcnt vmcnt(2)\n\t"
      "v_smfmac_f32_16x16x64_bf16 v[48:51], v[40:43], a[192:199], v2\n\t"
      "v_max3_u32 v6, v6, v40, v41\n\t"
      "v_smfmac_f32_16x16x64_bf16 v[52:55], v[40:43], a[208:215], v2\n\t"
      "v_max3_u32 v6, v6, v42, v43\n\t"
      "v_smfmac_f32_16x16x64_bf16 v[56:59], v[40:43], a[200:207], v2\n\t"
      "v_smfmac_f32_16x16x64_bf16 v[60:63], v[40:43], a[216:223], v2\n\t"
      "s_waitcnt vmcnt(1)\n\t"
      "v_smfmac_f32_16x16x64_bf16 v[48:51], v[44:47], a[224:231], v2\n\t"
      "v_max3_u32 v6, v6, v44, v45\n\t"
      "v_smfmac_f32_16x16x64_bf16 v[52:55], v[44:47], a[240:247], v2\n\t"
      "v_max3_u32 v6, v6, v46, v47\n\t"
      "v_smfmac_f32_16x16x64_bf16 v[56:59], v[44:47], a[232:239], v2\n\t"
      "v_cmp_eq_u32 vcc, -1, v6\n\t"
      "v_smfmac_f32_16x16x64_bf16 v[60:63], v[44:47], a[248:255], v2\n\t"
      "s_cbranch_vccnz LRETRY%=\n\t"
      "v_add_f32 v48, v48, v49\n\t"
      "v_add_f32 v54, v54, v55\n\t"
      "v_add_f32 v48, v48, v54\n\t"
      "s_nop 0\n\t"
      "v_add_f32 v56, v56, v57\n\t"
      "s_nop 1\n\t"
      "v_add_f32 v62, v62, v63\n\t"
      "v_add_f32 v49, v56, v62\n\t"
      "s_cmp_eq_u32 %[w], 0\n\t"
      "s_cbranch_scc1 LW0%=\n\t"
      "ds_write_b64 v3, v[48:49]\n\t"
      "s_waitcnt lgkmcnt(0)\n\t"
      "s_barrier\n\t"
      "s_branch LNEXT%=\n\t"
      "LW0%=:\n\t"
      "s_barrier\n\t"
      "ds_read_b64 v[50:51], v4 offset:512\n\t"
      "ds_read_b64 v[52:53], v4 offset:1024\n\t"
      "ds_read_b64 v[54:55], v4 offset:1536\n\t"
      "s_waitcnt lgkmcnt(2)\n\t"
      "v_pk_add_f32 v[48:49], v[48:49], v[50:51]\n\t"
      "s_waitcnt lgkmcnt(0)\n\t"
      "v_pk_add_f32 v[52:53], v[52:53], v[54:55]\n\t"
      "v_pk_add_f32 v[48:49], v[48:49], v[52:53]\n\t"
      "v_cvt_pk_bf16_f32 v48, v48, v49\n\t"
      "buffer_store_dword v48, v1, %[rs], s54 offen sc1\n\t"
      "buffer_store_dword v7, v1, %[rs], s55 offen sc1\n\t"
      "LNEXT%=:\n\t"
      "v_xor_b32 v3, 2048, v3\n\t"
      "v_xor_b32 v4, 2048, v4\n\t"
      "s_add_u32 s40, s40, 1\n\t"
      "s_cmp_lt_u32 s40, 63\n\t"
      "s_cbranch_scc1 LOOP%=\n\t"
      "s_branch LEND%=\n\t"
      "LRETRY%=:\n\t"
      "ds_write_b32 v10, v10\n\t"
      "s_nop 6\n\t"
      "s_branch LPOLL%=\n\t"
      "LFAIL%=:\n\t"
      "v_mov_b32 v6, 1\n\t"
      "s_mov_b32 s43, 688128\n\t"
      "buffer_store_dword v6, off, %[rs], s43 sc1\n\t"
      "LEND%=:\n\t"
      "s_waitcnt vmcnt(0) lgkmcnt(0)\n\t"
      "s_mov_b64 exec, -1\n\t"
      "s_mov_b32 m0, s39\n\t"
      : "+{v[0:7]}"(ad), "+{v[16:23]}"(ld)
      : [rs] "s"(rs), [rw] "s"(rw), [w] "s"(w), [st] "s"(uni(stage)), [cb] "s"(uni(w * 1024 * (DM * 2) + cg * 64)), [rot] "s"(rot),
        [t0] "s"(tag.x), [t1] "s"(tag.y)
      : "memory", "vcc", "scc", "v8", "v9", "v10", "v11", "v12", "v13", "v14", "v15", "v24", "v25", "v26", "v27", "v28", "v29", "v30", "v31", "v32", "v33",
        "v34", "v35", "v36", "v37", "v38", "v39", "v40", "v41", "v42", "v43", "v44", "v45", "v46", "v47", "v48", "v49", "v50", "v51", "v52", "v53", "v54",
        "v55", "v56", "v57", "v58", "v59", "v60", "v61", "v62", "v63", "a0", "a1", "a2", "a3", "a4", "a5", "a6", "a7", "a8", "a9", "a10", "a11", "a12", "a13",
        "a14", "a15", "a16", "a17", "a18", "a19", "a20", "a21", "a22", "a23", "a24", "a25", "a26", "a27", "a28", "a29", "a30", "a31", "a32", "a33", "a34",
        "a35", "a36", "a37", "a38", "a39", "a40", "a41", "a42", "a43", "a44", "a45", "a46", "a47", "a48", "a49", "a50", "a51", "a52", "a53", "a54", "a55",
        "a56", "a57", "a58", "a59", "a60", "a61", "a62", "a63", "a64", "a65", "a66", "a67", "a68", "a69", "a70", "a71", "a72", "a73", "a74", "a75", "a76",
        "a77", "a78", "a79", "a80", "a81", "a82", "a83", "a84", "a85", "a86", "a87", "a88", "a89", "a90", "a91", "a92", "a93", "a94", "a95", "a96", "a97",
        "a98", "a99", "a100", "a101", "a102", "a103", "a104", "a105", "a106", "a107", "a108", "a109", "a110", "a111", "a112", "a113", "a114", "a115", "a116",
        "a117", "a118", "a119", "a120", "a121", "a122", "a123", "a124", "a125", "a126", "a127", "a128", "a129", "a130", "a131", "a132", "a133", "a134",
        "a135", "a136", "a137", "a138", "a139", "a140", "a141", "a142", "a143", "a144", "a145", "a146", "a147", "a148", "a149", "a150", "a151", "a152",
        "a153", "a154", "a155", "a156", "a157", "a158", "a159", "a160", "a161", "a162", "a163", "a164", "a165", "a166", "a167", "a168", "a169", "a170",
        "a171", "a172", "a173", "a174", "a175", "a176", "a177", "a178", "a179", "a180", "a181", "a182", "a183", "a184", "a185", "a186", "a187", "a188",
        "a189", "a190", "a191", "a192", "a193", "a194", "a195", "a196", "a197", "a198", "a199", "a200", "a201", "a202", "a203", "a204", "a205", "a206",
        "a207", "a208", "a209", "a210", "a211", "a212", "a213", "a214", "a215", "a216", "a217", "a218", "a219", "a220", "a221", "a222", "a223", "a224",
        "a225", "a226", "a227", "a228", "a229", "a230", "a231", "a232", "a233", "a234", "a235", "a236", "a237", "a238", "a239", "a240", "a241", "a242",
        "a243", "a244", "a245", "a246", "a247", "a248", "a249", "a250", "a251", "a252", "a253", "a254", "a255", "s33", "s34", "s35", "s36", "s37", "s38",
        "s39", "s40", "s41", "s42", "s43", "s44", "s45", "s46", "s47", "s48", "s49", "s50", "s51", "s52", "s53", "s54", "s55", "s56", "s57", "s58", "s59",
        "s60", "s61", "s62", "s63", "s64", "s65", "s66", "s67"
  );
}

constexpr unsigned L_MEM = 131072, L_QE = 157696;

DEV void role_head(const Params& p, LAS char* lds, u2 tag, int h) {
  const int lane = threadIdx.x & 63, w = uni(threadIdx.x >> 6), g = lane >> 4, c = lane & 15;
  const unsigned lb = (unsigned)(size_t)lds, l16 = lane * 16, stage = lb + w * 32768;
  u8v ld = loader_regs<RM_HEAD>(lane, 8192, stage, 3);
  u16v ad;
  ad[0] = lb + L_MEM + l16;
  ad[1] = stage + l16;
  ad[2] = lb + L_QE + 2 * g * 544 + w * 64 + c * 4;
  ad[3] = lb + L_QE + (2 * (c >> 2) + (c & 1)) * 544 + g * 16;
  ad[4] = o_slot(h, g >> 1) + w * 256 + (g & 1) * 128 + c * 4;
  ad[5] = (lane & 2) ? 0xEEEEu : 0x4444u;
#pragma unroll
  for (int n = 0; n < 3; ++n) {
    const int f = (n == 0) ? 6 + w : (n == 1) ? w : 4;
    ad[6 + n] = m_slot(c >> 3, 2 * f + (g >> 1)) + ((g & 1) * 8 + (c & 7)) * 16;
  }
  ad[9] = l16;
  ad[10] = CTL_QKV + ((lane & 4) ? (32 + (h >> 1)) * 128 + (2 * (h & 1) + (lane & 1)) * 4 : (((lane & 2) ? h - 1 : h) & 31) * 128 + (lane & 3) * 4);
  ad[11] = lb + L_MEM + l16 + w * 1024;
  ad[12] = 0;
  ad[13] = (w == 0 && lane == 0) ? 0u : DROP;
  ad[14] = 0;
  ad[15] = (w == 0 && lane < 40) ? ewb_word(lane / 20, lane % 20) : DROP;
  const u4 rs = mk_rs(p.ws, WS_END), rq = mk_rs(p.wq, DM * 8192), rk = mk_rs(p.wk, DM * 8192), rv = mk_rs(p.wv, DM * 8192);
  const unsigned long long m0 = __ballot(4 * g + 0 <= c), m1 = __ballot(4 * g + 1 <= c), m2 = __ballot(4 * g + 2 <= c), m3 = __ballot(4 * g + 3 <= c);
  asm volatile(
      "s_sleep 0\n\t"
      "s_nop 4\n\t"
      "s_mov_b32 s39, m0\n\t"
      "s_cmp_eq_u32 %[w], 0\n\t"
      "s_cselect_b64 s[66:67], 1, 0\n\t"
      "s_add_u32 m0, %[st], 0\n\t"
      "s_add_u32 s43, %[cb], 0\n\t"
      "buffer_load_dwordx4 v16, %[rq], s43 offen lds\n\t"
      "buffer_load_dwordx4 v17, %[rq], s43 offen offset:1024 lds\n\t"
      "s_add_u32 m0, %[st], 2048\n\t"
      "s_add_u32 s43, %[cb], 262144\n\t"
      "buffer_load_dwordx4 v16, %[rq], s43 offen lds\n\t"
      "buffer_load_dwordx4 v17, %[rq], s43 offen offset:1024 lds\n\t"
      "s_add_u32 m0, %[st], 4096\n\t"
      "s_add_u32 s43, %[cb], 524288\n\t"
      "buffer_load_dwordx4 v16, %[rq], s43 offen lds\n\t"
      "buffer_load_dwordx4 v17, %[rq], s43 offen offset:1024 lds\n\t"
      "s_add_u32 m0, %[st], 6144\n\t"
      "s_add_u32 s43, %[cb], 786432\n\t"
      "buffer_load_dwordx4 v16, %[rq], s43 offen lds\n\t"
      "buffer_load_dwordx4 v17, %[rq], s43 offen offset:1024 lds\n\t"
      "s_add_u32 m0, %[st], 8192\n\t"
      "s_add_u32 s43, %[cb], 0\n\t"
      "buffer_load_dwordx4 v16, %[rk], s43 offen lds\n\t"
      "buffer_load_dwordx4 v17, %[rk], s43 offen offset:1024 lds\n\t"
      "s_add_u32 m0, %[st], 10240\n\t"
      "s_add_u32 s43, %[cb], 262144\n\t"
      "buffer_load_dwordx4 v16, %[rk], s43 offen lds\n\t"
      "buffer_load_dwordx4 v17, %[rk], s43 offen offset:1024 lds\n\t"
      "s_add_u32 m0, %[st], 12288\n\t"
      "s_add_u32 s43, %[cb], 524288\n\t"
      "buffer_load_dwordx4 v16, %[rk], s43 offen lds\n\t"
      "buffer_load_dwordx4 v17, %[rk], s43 offen offset:1024 lds\n\t"
      "s_add_u32 m0, %[st], 14336\n\t"
      "s_add_u32 s43, %[cb], 786432\n\t"
      "buffer_load_dwordx4 v16, %[rk], s43 offen lds\n\t"
      "buffer_load_dwordx4 v17, %[rk], s43 offen offset:1024 lds\n\t"
      "s_waitcnt vmcnt(12)\n\t"
      "ds_read_b64_tr_b16 a[0:1], v18 offset:0\n\t"
      "ds_read_b64_tr_b16 a[2:3], v18 offset:1024\n\t"
      "ds_read_b64_tr_b16 a[8:9], v19 offset:0\n\t"
      "ds_read_b64_tr_b16 a[10:11], v19 offset:1024\n\t"
      "ds_read_b64_tr_b16 a[4:5], v18 offset:2048\n\t"
      "ds_read_b64_tr_b16 a[6:7], v18 offset:3072\n\t"
      "ds_read_b64_tr_b16 a[12:13], v19 offset:2048\n\t"
      "ds_read_b64_tr_b16 a[14:15], v19 offset:3072\n\t"
      "s_waitcnt lgkmcnt(0)\n\t"
      "s_add_u32 m0, %[st], 0\n\t"
      "s_add_u32 s43, %[cb], 0\n\t"
      "buffer_load_dwordx4 v16, %[rv], s43 offen lds\n\t"
      "buffer_load_dwordx4 v17, %[rv], s43 offen offset:1024 lds\n\t"
      "s_add_u32 m0, %[st], 2048\n\t"
      "s_add_u32 s43, %[cb], 262144\n\t"
      "buffer_load_dwordx4 v16, %[rv], s43 offen lds\n\t"
      "buffer_load_dwordx4 v17, %[rv], s43 offen offset:1024 lds\n\t"
      "s_waitcnt vmcnt(12)\n\t"
      "ds_read_b64_tr_b16 a[16:17], v18 offset:4096\n\t"
      "ds_read_b64_tr_b16 a[18:19], v18 offset:5120\n\t"
      "ds_read_b64_tr_b16 a[24:25], v19 offset:4096\n\t"
      "ds_read_b64_tr_b16 a[26:27], v19 offset:5120\n\t"
      "ds_read_b64_tr_b16 a[20:21], v18 offset:6144\n\t"
      "ds_read_b64_tr_b16 a[22:23], v18 offset:7168\n\t"
      "ds_read_b64_tr_b16 a[28:29], v19 offset:6144\n\t"
      "ds_read_b64_tr_b16 a[30:31], v19 offset:7168\n\t"
      "s_waitcnt lgkmcnt(0)\n\t"
      "s_add_u32 m0, %[st], 4096\n\t"
      "s_add_u32 s43, %[cb], 524288\n\t"
      "buffer_load_dwordx4 v16, %[rv], s43 offen lds\n\t"
      "buffer_load_dwordx4 v17, %[rv], s43 offen offset:1024 lds\n\t"
      "s_add_u32 m0, %[st], 6144\n\t"
      "s_add_u32 s43, %[cb], 786432\n\t"
      "buffer_load_dwordx4 v16, %[rv], s43 offen lds\n\t"
      "buffer_load_dwordx4 v17, %[rv], s43 offen offset:1024 lds\n\t"
      "s_waitcnt vmcnt(12)\n\t"
      "ds_read_b64_tr_b16 v[24:25], v18 offset:8192\n\t"
      "ds_read_b64_tr_b16 v[26:27], v18 offset:9216\n\t"
      "ds_read_b64_tr_b16 v[28:29], v19 offset:8192\n\t"
      "ds_read_b64_tr_b16 v[30:31], v19 offset:9216\n\t"
      "ds_read_b64_tr_b16 v[32:33], v18 offset:10240\n\t"
      "ds_read_b64_tr_b16 v[34:35], v18 offset:11264\n\t"
      "ds_read_b64_tr_b16 v[36:37], v19 offset:10240\n\t"
      "ds_read_b64_tr_b16 v[38:39], v19 offset:11264\n\t"
      "s_waitcnt lgkmcnt(0)\n\t"
      "ds_write_b128 v1, v[24:27] offset:16384\n\t"
      "ds_write_b128 v1, v[28:31] offset:17408\n\t"
      "s_add_u32 m0, %[st], 8192\n\t"
      "s_add_u32 s43, %[cb], 1048576\n\t"
      "buffer_load_dwordx4 v16, %[rq], s43 offen lds\n\t"
      "buffer_load_dwordx4 v17, %[rq], s43 offen offset:1024 lds\n\t"
      "ds_write_b128 v1, v[32:35] offset:18432\n\t"
      "ds_write_b128 v1, v[36:39] offset:19456\n\t"
      "s_waitcnt lgkmcnt(0)\n\t"
      "s_add_u32 m0, %[st], 10240\n\t"
      "s_add_u32 s43, %[cb], 1310720\n\t"
      "buffer_load_dwordx4 v16, %[rq], s43 offen lds\n\t"
      "buffer_load_dwordx4 v17, %[rq], s43 offen offset:1024 lds\n\t"
      "s_waitcnt vmcnt(12)\n\t"
      "ds_read_b64_tr_b16 v[24:25], v18 offset:12288\n\t"
      "ds_read_b64_tr_b16 v[26:27], v18 offset:13312\n\t"
      "ds_read_b64_tr_b16 v[28:29], v19 offset:12288\n\t"
      "ds_read_b64_tr_b16 v[30:31], v19 offset:13312\n\t"
      "ds_read_b64_tr_b16 v[32:33], v18 offset:14336\n\t"
      "ds_read_b64_tr_b16 v[34:35], v18 offset:15360\n\t"
      "ds_read_b64_tr_b16 v[36:37], v19 offset:14336\n\t"
      "ds_read_b64_tr_b16 v[38:39], v19 offset:15360\n\t"
      "s_waitcnt lgkmcnt(0)\n\t"
      "ds_write_b128 v1, v[24:27] offset:20480\n\t"
      "ds_write_b128 v1, v[28:31] offset:21504\n\t"
      "s_add_u32 m0, %[st], 12288\n\t"
      "s_add_u32 s43, %[cb], 1572864\n\t"
      "buffer_load_dwordx4 v16, %[rq], s43 offen lds\n\t"
      "buffer_load_dwordx4 v17, %[rq], s43 offen offset:1024 lds\n\t"
      "ds_write_b128 v1, v[32:35] offset:22528\n\t"
      "ds_write_b128 v1, v[36:39] offset:23552\n\t"
      "s_waitcnt lgkmcnt(0)\n\t"
      "s_add_u32 m0, %[st], 14336\n\t"
      "s_add_u32 s43, %[cb], 1835008\n\t"
      "buffer_load_dwordx4 v16, %[rq], s43 offen lds\n\t"
      "buffer_load_dwordx4 v17, %[rq], s43 offen offset:1024 lds\n\t"
      "s_waitcnt vmcnt(12)\n\t"
      "ds_read_b64_tr_b16 a[192:193], v20 offset:0\n\t"
      "ds_read_b64_tr_b16 a[194:195], v20 offset:1024\n\t"
      "ds_read_b64_tr_b16 a[200:201], v20 offset:8\n\t"
      "ds_read_b64_tr_b16 a[202:203], v20 offset:1032\n\t"
      "ds_read_b64_tr_b16 a[196:197], v20 offset:2048\n\t"
      "ds_read_b64_tr_b16 a[198:199], v20 offset:3072\n\t"
      "ds_read_b64_tr_b16 a[204:205], v20 offset:2056\n\t"
      "ds_read_b64_tr_b16 a[206:207], v20 offset:3080\n\t"
      "s_waitcnt lgkmcnt(0)\n\t"
      "s_add_u32 m0, %[st], 0\n\t"
      "s_add_u32 s43, %[cb], 1048576\n\t"
      "buffer_load_dwordx4 v16, %[rk], s43 offen lds\n\t"
      "buffer_load_dwordx4 v17, %[rk], s43 offen offset:1024 lds\n\t"
      "s_add_u32 m0, %[st], 2048\n\t"
      "s_add_u32 s43, %[cb], 1310720\n\t"
      "buffer_load_dwordx4 v16, %[rk], s43 offen lds\n\t"
      "buffer_load_dwordx4 v17, %[rk], s43 offen offset:1024 lds\n\t"
      "s_waitcnt vmcnt(12)\n\t"
      "ds_read_b64_tr_b16 a[208:209], v20 offset:4096\n\t"
      "ds_read_b64_tr_b16 a[210:211], v20 offset:5120\n\t"
      "ds_read_b64_tr_b16 a[216:217], v20 offset:4104\n\t"
      "ds_read_b64_tr_b16 a[218:219], v20 offset:5128\n\t"
      "ds_read_b64_tr_b16 a[212:213], v20 offset:6144\n\t"
      "ds_read_b64_tr_b16 a[214:215], v20 offset:7168\n\t"
      "ds_read_b64_tr_b16 a[220:221], v20 offset:6152\n\t"
      "ds_read_b64_tr_b16 a[222:223], v20 offset:7176\n\t"
      "s_waitcnt lgkmcnt(0)\n\t"
      "s_add_u32 m0, %[st], 4096\n\t"
      "s_add_u32 s43, %[cb], 1572864\n\t"
      "buffer_load_dwordx4 v16, %[rk], s43 offen lds\n\t"
      "buffer_load_dwordx4 v17, %[rk], s43 offen offset:1024 lds\n\t"
      "s_add_u32 m0, %[st], 6144\n\t"
      "s_add_u32 s43, %[cb], 1835008\n\t"
      "buffer_load_dwordx4 v16, %[rk], s43 offen lds\n\t"
      "buffer_load_dwordx4 v17, %[rk], s43 offen offset:1024 lds\n\t"
      "s_waitcnt vmcnt(12)\n\t"
      "ds_read_b64_tr_b16 a[32:33], v18 offset:8192\n\t"
      "ds_read_b64_tr_b16 a[34:35], v18 offset:9216\n\t"
      "ds_read_b64_tr_b16 a[40:41], v19 offset:8192\n\t"
      "ds_read_b64_tr_b16 a[42:43], v19 offset:9216\n\t"
      "ds_read_b64_tr_b16 a[36:37], v18 offset:10240\n\t"
      "ds_read_b64_tr_b16 a[38:39], v18 offset:11264\n\t"
      "ds_read_b64_tr_b16 a[44:45], v19 offset:10240\n\t"
      "ds_read_b64_tr_b16 a[46:47], v19 offset:11264\n\t"
      "s_waitcnt lgkmcnt(0)\n\t"
      "s_add_u32 m0, %[st], 8192\n\t"
      "s_add_u32 s43, %[cb], 1048576\n\t"
      "buffer_load_dwordx4 v16, %[rv], s43 offen lds\n\t"
      "buffer_load_dwordx4 v17, %[rv], s43 offen offset:1024 lds\n\t"
      "s_add_u32 m0, %[st], 10240\n\t"
      "s_add_u32 s43, %[cb], 1310720\n\t"
      "buffer_load_dwordx4 v16, %[rv], s43 offen lds\n\t"
      "buffer_load_dwordx4 v17, %[rv], s43 offen offset:1024 lds\n\t"
      "s_waitcnt vmcnt(12)\n\t"
      "ds_read_b64_tr_b16 a[48:49], v18 offset:12288\n\t"
      "ds_read_b64_tr_b16 a[50:51], v18 offset:13312\n\t"
      "ds_read_b64_tr_b16 a[56:57], v19 offset:12288\n\t"
      "ds_read_b64_tr_b16 a[58:59], v19 offset:13312\n\t"
      "ds_read_b64_tr_b16 a[52:53], v18 offset:14336\n\t"
      "ds_read_b64_tr_b16 a[54:55], v18 offset:15360\n\t"
      "ds_read_b64_tr_b16 a[60:61], v19 offset:14336\n\t"
      "ds_read_b64_tr_b16 a[62:63], v19 offset:15360\n\t"
      "s_waitcnt lgkmcnt(0)\n\t"
      "s_add_u32 m0, %[st], 12288\n\t"
      "s_add_u32 s43, %[cb], 1572864\n\t"
      "buffer_load_dwordx4 v16, %[rv], s43 offen lds\n\t"
      "buffer_load_dwordx4 v17, %[rv], s43 offen offset:1024 lds\n\t"
      "s_add_u32 m0, %[st], 14336\n\t"
      "s_add_u32 s43, %[cb], 1835008\n\t"
      "buffer_load_dwordx4 v16, %[rv], s43 offen lds\n\t"
      "buffer_load_dwordx4 v17, %[rv], s43 offen offset:1024 lds\n\t"
      "s_waitcnt vmcnt(12)\n\t"
      "ds_read_b64_tr_b16 v[24:25], v18 offset:0\n\t"
      "ds_read_b64_tr_b16 v[26:27], v18 offset:1024\n\t"
      "ds_read_b64_tr_b16 v[28:29], v19 offset:0\n\t"
      "ds_read_b64_tr_b16 v[30:31], v19 offset:1024\n\t"
      "ds_read_b64_tr_b16 v[32:33], v18 offset:2048\n\t"
      "ds_read_b64_tr_b16 v[34:35], v18 offset:3072\n\t"
      "ds_read_b64_tr_b16 v[36:37], v19 offset:2048\n\t"
      "ds_read_b64_tr_b16 v[38:39], v19 offset:3072\n\t"
      "s_waitcnt lgkmcnt(0)\n\t"
      "ds_write_b128 v1, v[24:27] offset:24576\n\t"
      "ds_write_b128 v1, v[28:31] offset:25600\n\t"
      "s_add_u32 m0, %[st], 0\n\t"
      "s_add_u32 s43, %[cb], 2097152\n\t"
      "buffer_load_dwordx4 v16, %[rq], s43 offen lds\n\t"
      "buffer_load_dwordx4 v17, %[rq], s43 offen offset:1024 lds\n\t"
      "ds_write_b128 v1, v[32:35] offset:26624\n\t"
      "ds_write_b128 v1, v[36:39] offset:27648\n\t"
      "s_waitcnt lgkmcnt(0)\n\t"
      "s_add_u32 m0, %[st], 2048\n\t"
      "s_add_u32 s43, %[cb], 2359296\n\t"
      "buffer_load_dwordx4 v16, %[rq], s43 offen lds\n\t"
      "buffer_load_dwordx4 v17, %[rq], s43 offen offset:1024 lds\n\t"
      "s_waitcnt vmcnt(12)\n\t"
      "ds_read_b64_tr_b16 v[24:25], v18 offset:4096\n\t"
      "ds_read_b64_tr_b16 v[26:27], v18 offset:5120\n\t"
      "ds_read_b64_tr_b16 v[28:29], v19 offset:4096\n\t"
      "ds_read_b64_tr_b16 v[30:31], v19 offset:5120\n\t"
      "ds_read_b64_tr_b16 v[32:33], v18 offset:6144\n\t"
      "ds_read_b64_tr_b16 v[34:35], v18 offset:7168\n\t"
      "ds_read_b64_tr_b16 v[36:37], v19 offset:6144\n\t"
      "ds_read_b64_tr_b16 v[38:39], v19 offset:7168\n\t"
      "s_waitcnt lgkmcnt(0)\n\t"
      "ds_write_b128 v1, v[24:27] offset:28672\n\t"
      "ds_write_b128 v1, v[28:31] offset:29696\n\t"
      "s_add_u32 m0, %[st], 4096\n\t"
      "s_add_u32 s43, %[cb], 2621440\n\t"
      "buffer_load_dwordx4 v16, %[rq], s43 offen lds\n\t"
      "buffer_load_dwordx4 v17, %[rq], s43 offen offset:1024 lds\n\t"
      "ds_write_b128 v1, v[32:35] offset:30720\n\t"
      "ds_write_b128 v1, v[36:39] offset:31744\n\t"
      "s_waitcnt lgkmcnt(0)\n\t"
      "s_add_u32 m0, %[st], 6144\n\t"
      "s_add_u32 s43, %[cb], 2883584\n\t"
      "buffer_load_dwordx4 v16, %[rq], s43 offen lds\n\t"
      "buffer_load_dwordx4 v17, %[rq], s43 offen offset:1024 lds\n\t"
      "s_waitcnt vmcnt(12)\n\t"
      "ds_read_b64_tr_b16 a[224:225], v20 offset:8192\n\t"
      "ds_read_b64_tr_b16 a[226:227], v20 offset:9216\n\t"
      "ds_read_b64_tr_b16 a[232:233], v20 offset:8200\n\t"
      "ds_read_b64_tr_b16 a[234:235], v20 offset:9224\n\t"
      "ds_read_b64_tr_b16 a[228:229], v20 offset:10240\n\t"
      "ds_read_b64_tr_b16 a[230:231], v20 offset:11264\n\t"
      "ds_read_b64_tr_b16 a[236:237], v20 offset:10248\n\t"
      "ds_read_b64_tr_b16 a[238:239], v20 offset:11272\n\t"
      "s_waitcnt lgkmcnt(0)\n\t"
      "s_add_u32 m0, %[st], 8192\n\t"
      "s_add_u32 s43, %[cb], 2097152\n\t"
      "buffer_load_dwordx4 v16, %[rk], s43 offen lds\n\t"
      "buffer_load_dwordx4 v17, %[rk], s43 offen offset:1024 lds\n\t"
      "s_add_u32 m0, %[st], 10240\n\t"
      "s_add_u32 s43, %[cb], 2359296\n\t"
      "buffer_load_dwordx4 v16, %[rk], s43 offen lds\n\t"
      "buffer_load_dwordx4 v17, %[rk], s43 offen offset:1024 lds\n\t"
      "s_waitcnt vmcnt(12)\n\t"
      "ds_read_b64_tr_b16 a[240:241], v20 offset:12288\n\t"
      "ds_read_b64_tr_b16 a[242:243], v20 offset:13312\n\t"
      "ds_read_b64_tr_b16 a[248:249], v20 offset:12296\n\t"
      "ds_read_b64_tr_b16 a[250:251], v20 offset:13320\n\t"
      "ds_read_b64_tr_b16 a[244:245], v20 offset:14336\n\t"
      "ds_read_b64_tr_b16 a[246:247], v20 offset:15360\n\t"
      "ds_read_b64_tr_b16 a[252:253], v20 offset:14344\n\t"
      "ds_read_b64_tr_b16 a[254:255], v20 offset:15368\n\t"
      "s_waitcnt lgkmcnt(0)\n\t"
      "s_add_u32 m0, %[st], 12288\n\t"
      "s_add_u32 s43, %[cb], 2621440\n\t"
      "buffer_load_dwordx4 v16, %[rk], s43 offen lds\n\t"
      "buffer_load_dwordx4 v17, %[rk], s43 offen offset:1024 lds\n\t"
      "s_add_u32 m0, %[st], 14336\n\t"
      "s_add_u32 s43, %[cb], 2883584\n\t"
      "buffer_load_dwordx4 v16, %[rk], s43 offen lds\n\t"
      "buffer_load_dwordx4 v17, %[rk], s43 offen offset:1024 lds\n\t"
      "s_waitcnt vmcnt(12)\n\t"
      "ds_read_b64_tr_b16 a[64:65], v18 offset:0\n\t"
      "ds_read_b64_tr_b16 a[66:67], v18 offset:1024\n\t"
      "ds_read_b64_tr_b16 a[72:73], v19 offset:0\n\t"
      "ds_read_b64_tr_b16 a[74:75], v19 offset:1024\n\t"
      "ds_read_b64_tr_b16 a[68:69], v18 offset:2048\n\t"
      "ds_read_b64_tr_b16 a[70:71], v18 offset:3072\n\t"
      "ds_read_b64_tr_b16 a[76:77], v19 offset:2048\n\t"
      "ds_read_b64_tr_b16 a[78:79], v19 offset:3072\n\t"
      "s_waitcnt lgkmcnt(0)\n\t"
      "s_add_u32 m0, %[st], 0\n\t"
      "s_add_u32 s43, %[cb], 2097152\n\t"
      "buffer_load_dwordx4 v16, %[rv], s43 offen lds\n\t"
      "buffer_load_dwordx4 v17, %[rv], s43 offen offset:1024 lds\n\t"
      "s_add_u32 m0, %[st], 2048\n\t"
      "s_add_u32 s43, %[cb], 2359296\n\t"
      "buffer_load_dwordx4 v16, %[rv], s43 offen lds\n\t"
      "buffer_load_dwordx4 v17, %[rv], s43 offen offset:1024 lds\n\t"
      "s_waitcnt vmcnt(12)\n\t"
      "ds_read_b64_tr_b16 a[80:81], v18 offset:4096\n\t"
      "ds_read_b64_tr_b16 a[82:83], v18 offset:5120\n\t"
      "ds_read_b64_tr_b16 a[88:89], v19 offset:4096\n\t"
      "ds_read_b64_tr_b16 a[90:91], v19 offset:5120\n\t"
      "ds_read_b64_tr_b16 a[84:85], v18 offset:6144\n\t"
      "ds_read_b64_tr_b16 a[86:87], v18 offset:7168\n\t"
      "ds_read_b64_tr_b16 a[92:93], v19 offset:6144\n\t"
      "ds_read_b64_tr_b16 a[94:95], v19 offset:7168\n\t"
      "s_waitcnt lgkmcnt(0)\n\t"
      "s_add_u32 m0, %[st], 4096\n\t"
      "s_add_u32 s43, %[cb], 2621440\n\t"
      "buffer_load_dwordx4 v16, %[rv], s43 offen lds\n\t"
      "buffer_load_dwordx4 v17, %[rv], s43 offen offset:1024 lds\n\t"
      "s_add_u32 m0, %[st], 6144\n\t"
      "s_add_u32 s43, %[cb], 2883584\n\t"
      "buffer_load_dwordx4 v16, %[rv], s43 offen lds\n\t"
      "buffer_load_dwordx4 v17, %[rv], s43 offen offset:1024 lds\n\t"
      "s_waitcnt vmcnt(12)\n\t"
      "ds_read_b64_tr_b16 a[96:97], v18 offset:8192\n\t"
      "ds_read_b64_tr_b16 a[98:99], v18 offset:9216\n\t"
      "ds_read_b64_tr_b16 a[104:105], v19 offset:8192\n\t"
      "ds_read_b64_tr_b16 a[106:107], v19 offset:9216\n\t"
      "ds_read_b64_tr_b16 a[100:101], v18 offset:10240\n\t"
      "ds_read_b64_tr_b16 a[102:103], v18 offset:11264\n\t"
      "ds_read_b64_tr_b16 a[108:109], v19 offset:10240\n\t"
      "ds_read_b64_tr_b16 a[110:111], v19 offset:11264\n\t"
      "s_waitcnt lgkmcnt(0)\n\t"
      "s_add_u32 m0, %[st], 8192\n\t"
      "s_add_u32 s43, %[cb], 3145728\n\t"
      "buffer_load_dwordx4 v16, %[rk], s43 offen lds\n\t"
      "buffer_load_dwordx4 v17, %[rk], s43 offen offset:1024 lds\n\t"
      "s_add_u32 m0, %[st], 10240\n\t"
      "s_add_u32 s43, %[cb], 3407872\n\t"
      "buffer_load_dwordx4 v16, %[rk], s43 offen lds\n\t"
      "buffer_load_dwordx4 v17, %[rk], s43 offen offset:1024 lds\n\t"
      "s_waitcnt vmcnt(12)\n\t"
      "ds_read_b64_tr_b16 a[112:113], v18 offset:12288\n\t"
      "ds_read_b64_tr_b16 a[114:115], v18 offset:13312\n\t"
      "ds_read_b64_tr_b16 a[120:121], v19 offset:12288\n\t"
      "ds_read_b64_tr_b16 a[122:123], v19 offset:13312\n\t"
      "ds_read_b64_tr_b16 a[116:117], v18 offset:14336\n\t"
      "ds_read_b64_tr_b16 a[118:119], v18 offset:15360\n\t"
      "ds_read_b64_tr_b16 a[124:125], v19 offset:14336\n\t"
      "ds_read_b64_tr_b16 a[126:127], v19 offset:15360\n\t"
      "s_waitcnt lgkmcnt(0)\n\t"
      "s_add_u32 m0, %[st], 12288\n\t"
      "s_add_u32 s43, %[cb], 3670016\n\t"
      "buffer_load_dwordx4 v16, %[rk], s43 offen lds\n\t"
      "buffer_load_dwordx4 v17, %[rk], s43 offen offset:1024 lds\n\t"
      "s_add_u32 m0, %[st], 14336\n\t"
      "s_add_u32 s43, %[cb], 3932160\n\t"
      "buffer_load_dwordx4 v16, %[rk], s43 offen lds\n\t"
      "buffer_load_dwordx4 v17, %[rk], s43 offen offset:1024 lds\n\t"
      "s_waitcnt vmcnt(12)\n\t"
      "ds_read_b64_tr_b16 v[160:161], v20 offset:0\n\t"
      "ds_read_b64_tr_b16 v[162:163], v20 offset:1024\n\t"
      "ds_read_b64_tr_b16 v[168:169], v20 offset:8\n\t"
      "ds_read_b64_tr_b16 v[170:171], v20 offset:1032\n\t"
      "ds_read_b64_tr_b16 v[164:165], v20 offset:2048\n\t"
      "ds_read_b64_tr_b16 v[166:167], v20 offset:3072\n\t"
      "ds_read_b64_tr_b16 v[172:173], v20 offset:2056\n\t"
      "ds_read_b64_tr_b16 v[174:175], v20 offset:3080\n\t"
      "s_waitcnt lgkmcnt(0)\n\t"
      "s_add_u32 m0, %[st], 0\n\t"
      "s_add_u32 s43, %[cb], 3145728\n\t"
      "buffer_load_dwordx4 v16, %[rv], s43 offen lds\n\t"
      "buffer_load_dwordx4 v17, %[rv], s43 offen offset:1024 lds\n\t"
      "s_add_u32 m0, %[st], 2048\n\t"
      "s_add_u32 s43, %[cb], 3407872\n\t"
      "buffer_load_dwordx4 v16, %[rv], s43 offen lds\n\t"
      "buffer_load_dwordx4 v17, %[rv], s43 offen offset:1024 lds\n\t"
      "s_waitcnt vmcnt(12)\n\t"
      "ds_read_b64_tr_b16 v[176:177], v20 offset:4096\n\t"
      "ds_read_b64_tr_b16 v[178:179], v20 offset:5120\n\t"
      "ds_read_b64_tr_b16 v[184:185], v20 offset:4104\n\t"
      "ds_read_b64_tr_b16 v[186:187], v20 offset:5128\n\t"
      "ds_read_b64_tr_b16 v[180:181], v20 offset:6144\n\t"
      "ds_read_b64_tr_b16 v[182:183], v20 offset:7168\n\t"
      "ds_read_b64_tr_b16 v[188:189], v20 offset:6152\n\t"
      "ds_read_b64_tr_b16 v[190:191], v20 offset:7176\n\t"
      "s_waitcnt lgkmcnt(0)\n\t"
      "s_add_u32 m0, %[st], 4096\n\t"
      "s_add_u32 s43, %[cb], 3670016\n\t"
      "buffer_load_dwordx4 v16, %[rv], s43 offen lds\n\t"
      "buffer_load_dwordx4 v17, %[rv], s43 offen offset:1024 lds\n\t"
      "s_add_u32 m0, %[st], 6144\n\t"
      "s_add_u32 s43, %[cb], 3932160\n\t"
      "buffer_load_dwordx4 v16, %[rv], s43 offen lds\n\t"
      "buffer_load_dwordx4 v17, %[rv], s43 offen offset:1024 lds\n\t"
      "s_waitcnt vmcnt(12)\n\t"
      "ds_read_b64_tr_b16 a[128:129], v18 offset:8192\n\t"
      "ds_read_b64_tr_b16 a[130:131], v18 offset:9216\n\t"
      "ds_read_b64_tr_b16 a[136:137], v19 offset:8192\n\t"
      "ds_read_b64_tr_b16 a[138:139], v19 offset:9216\n\t"
      "ds_read_b64_tr_b16 a[132:133], v18 offset:10240\n\t"
      "ds_read_b64_tr_b16 a[134:135], v18 offset:11264\n\t"
      "ds_read_b64_tr_b16 a[140:141], v19 offset:10240\n\t"
      "ds_read_b64_tr_b16 a[142:143], v19 offset:11264\n\t"
      "s_waitcnt lgkmcnt(0)\n\t"
      "s_add_u32 m0, %[st], 8192\n\t"
      "s_add_u32 s43, %[cb], 4194304\n\t"
      "buffer_load_dwordx4 v16, %[rk], s43 offen lds\n\t"
      "buffer_load_dwordx4 v17, %[rk], s43 offen offset:1024 lds\n\t"
      "s_add_u32 m0, %[st], 10240\n\t"
      "s_add_u32 s43, %[cb], 4456448\n\t"
      "buffer_load_dwordx4 v16, %[rk], s43 offen lds\n\t"
      "buffer_load_dwordx4 v17, %[rk], s43 offen offset:1024 lds\n\t"
      "s_waitcnt vmcnt(12)\n\t"
      "ds_read_b64_tr_b16 a[144:145], v18 offset:12288\n\t"
      "ds_read_b64_tr_b16 a[146:147], v18 offset:13312\n\t"
      "ds_read_b64_tr_b16 a[152:153], v19 offset:12288\n\t"
      "ds_read_b64_tr_b16 a[154:155], v19 offset:13312\n\t"
      "ds_read_b64_tr_b16 a[148:149], v18 offset:14336\n\t"
      "ds_read_b64_tr_b16 a[150:151], v18 offset:15360\n\t"
      "ds_read_b64_tr_b16 a[156:157], v19 offset:14336\n\t"
      "ds_read_b64_tr_b16 a[158:159], v19 offset:15360\n\t"
      "s_waitcnt lgkmcnt(0)\n\t"
      "s_add_u32 m0, %[st], 12288\n\t"
      "s_add_u32 s43, %[cb], 4718592\n\t"
      "buffer_load_dwordx4 v16, %[rk], s43 offen lds\n\t"
      "buffer_load_dwordx4 v17, %[rk], s43 offen offset:1024 lds\n\t"
      "s_add_u32 m0, %[st], 14336\n\t"
      "s_add_u32 s43, %[cb], 4980736\n\t"
      "buffer_load_dwordx4 v16, %[rk], s43 offen lds\n\t"
      "buffer_load_dwordx4 v17, %[rk], s43 offen offset:1024 lds\n\t"
      "s_waitcnt vmcnt(12)\n\t"
      "ds_read_b64_tr_b16 v[192:193], v20 offset:0\n\t"
      "ds_read_b64_tr_b16 v[194:195], v20 offset:1024\n\t"
      "ds_read_b64_tr_b16 v[200:201], v20 offset:8\n\t"
      "ds_read_b64_tr_b16 v[202:203], v20 offset:1032\n\t"
      "ds_read_b64_tr_b16 v[196:197], v20 offset:2048\n\t"
      "ds_read_b64_tr_b16 v[198:199], v20 offset:3072\n\t"
      "ds_read_b64_tr_b16 v[204:205], v20 offset:2056\n\t"
      "ds_read_b64_tr_b16 v[206:207], v20 offset:3080\n\t"
      "s_waitcnt lgkmcnt(0)\n\t"
      "s_add_u32 m0, %[st], 0\n\t"
      "s_add_u32 s43, %[cb], 4194304\n\t"
      "buffer_load_dwordx4 v16, %[rv], s43 offen lds\n\t"
      "buffer_load_dwordx4 v17, %[rv], s43 offen offset:1024 lds\n\t"
      "s_add_u32 m0, %[st], 2048\n\t"
      "s_add_u32 s43, %[cb], 4456448\n\t"
      "buffer_load_dwordx4 v16, %[rv], s43 offen lds\n\t"
      "buffer_load_dwordx4 v17, %[rv], s43 offen offset:1024 lds\n\t"
      "s_waitcnt vmcnt(12)\n\t"
      "ds_read_b64_tr_b16 v[208:209], v20 offset:4096\n\t"
      "ds_read_b64_tr_b16 v[210:211], v20 offset:5120\n\t"
      "ds_read_b64_tr_b16 v[216:217], v20 offset:4104\n\t"
      "ds_read_b64_tr_b16 v[218:219], v20 offset:5128\n\t"
      "ds_read_b64_tr_b16 v[212:213], v20 offset:6144\n\t"
      "ds_read_b64_tr_b16 v[214:215], v20 offset:7168\n\t"
      "ds_read_b64_tr_b16 v[220:221], v20 offset:6152\n\t"
      "ds_read_b64_tr_b16 v[222:223], v20 offset:7176\n\t"
      "s_waitcnt lgkmcnt(0)\n\t"
      "s_add_u32 m0, %[st], 4096\n\t"
      "s_add_u32 s43, %[cb], 4718592\n\t"
      "buffer_load_dwordx4 v16, %[rv], s43 offen lds\n\t"
      "buffer_load_dwordx4 v17, %[rv], s43 offen offset:1024 lds\n\t"
      "s_add_u32 m0, %[st], 6144\n\t"
      "s_add_u32 s43, %[cb], 4980736\n\t"
      "buffer_load_dwordx4 v16, %[rv], s43 offen lds\n\t"
      "buffer_load_dwordx4 v17, %[rv], s43 offen offset:1024 lds\n\t"
      "s_waitcnt vmcnt(12)\n\t"
      "ds_read_b64_tr_b16 a[160:161], v18 offset:8192\n\t"
      "ds_read_b64_tr_b16 a[162:163], v18 offset:9216\n\t"
      "ds_read_b64_tr_b16 a[168:169], v19 offset:8192\n\t"
      "ds_read_b64_tr_b16 a[170:171], v19 offset:9216\n\t"
      "ds_read_b64_tr_b16 a[164:165], v18 offset:10240\n\t"
      "ds_read_b64_tr_b16 a[166:167], v18 offset:11264\n\t"
      "ds_read_b64_tr_b16 a[172:173], v19 offset:10240\n\t"
      "ds_read_b64_tr_b16 a[174:175], v19 offset:11264\n\t"
      "s_waitcnt lgkmcnt(0)\n\t"
      "s_add_u32 m0, %[st], 8192\n\t"
      "s_add_u32 s43, %[cb], 3145728\n\t"
      "buffer_load_dwordx4 v16, %[rq], s43 offen lds\n\t"
      "buffer_load_dwordx4 v17, %[rq], s43 offen offset:1024 lds\n\t"
      "s_add_u32 m0, %[st], 10240\n\t"
      "s_add_u32 s43, %[cb], 3407872\n\t"
      "buffer_load_dwordx4 v16, %[rq], s43 offen lds\n\t"
      "buffer_load_dwordx4 v17, %[rq], s43 offen offset:1024 lds\n\t"
      "s_waitcnt vmcnt(12)\n\t"
      "ds_read_b64_tr_b16 a[176:177], v18 offset:12288\n\t"
      "ds_read_b64_tr_b16 a[178:179], v18 offset:13312\n\t"
      "ds_read_b64_tr_b16 a[184:185], v19 offset:12288\n\t"
      "ds_read_b64_tr_b16 a[186:187], v19 offset:13312\n\t"
      "ds_read_b64_tr_b16 a[180:181], v18 offset:14336\n\t"
      "ds_read_b64_tr_b16 a[182:183], v18 offset:15360\n\t"
      "ds_read_b64_tr_b16 a[188:189], v19 offset:14336\n\t"
      "ds_read_b64_tr_b16 a[190:191], v19 offset:15360\n\t"
      "s_waitcnt lgkmcnt(0)\n\t"
      "s_add_u32 m0, %[st], 12288\n\t"
      "s_add_u32 s43, %[cb], 3670016\n\t"
      "buffer_load_dwordx4 v16, %[rq], s43 offen lds\n\t"
      "buffer_load_dwordx4 v17, %[rq], s43 offen offset:1024 lds\n\t"
      "s_add_u32 m0, %[st], 14336\n\t"
      "s_add_u32 s43, %[cb], 3932160\n\t"
      "buffer_load_dwordx4 v16, %[rq], s43 offen lds\n\t"
      "buffer_load_dwordx4 v17, %[rq], s43 offen offset:1024 lds\n\t"
      "s_waitcnt vmcnt(12)\n\t"
      "ds_read_b64_tr_b16 v[224:225], v20 offset:0\n\t"
      "ds_read_b64_tr_b16 v[226:227], v20 offset:1024\n\t"
      "ds_read_b64_tr_b16 v[232:233], v20 offset:8\n\t"
      "ds_read_b64_tr_b16 v[234:235], v20 offset:1032\n\t"
      "ds_read_b64_tr_b16 v[228:229], v20 offset:2048\n\t"
      "ds_read_b64_tr_b16 v[230:231], v20 offset:3072\n\t"
      "ds_read_b64_tr_b16 v[236:237], v20 offset:2056\n\t"
      "ds_read_b64_tr_b16 v[238:239], v20 offset:3080\n\t"
      "s_waitcnt lgkmcnt(0)\n\t"
      "s_add_u32 m0, %[st], 0\n\t"
      "s_add_u32 s43, %[cb], 4194304\n\t"
      "buffer_load_dwordx4 v16, %[rq], s43 offen lds\n\t"
      "buffer_load_dwordx4 v17, %[rq], s43 offen offset:1024 lds\n\t"
      "s_add_u32 m0, %[st], 2048\n\t"
      "s_add_u32 s43, %[cb], 4456448\n\t"
      "buffer_load_dwordx4 v16, %[rq], s43 offen lds\n\t"
      "buffer_load_dwordx4 v17, %[rq], s43 offen offset:1024 lds\n\t"
      "s_waitcnt vmcnt(12)\n\t"
      "ds_read_b64_tr_b16 v[240:241], v20 offset:4096\n\t"
      "ds_read_b64_tr_b16 v[242:243], v20 offset:5120\n\t"
      "ds_read_b64_tr_b16 v[248:249], v20 offset:4104\n\t"
      "ds_read_b64_tr_b16 v[250:251], v20 offset:5128\n\t"
      "ds_read_b64_tr_b16 v[244:245], v20 offset:6144\n\t"
      "ds_read_b64_tr_b16 v[246:247], v20 offset:7168\n\t"
      "ds_read_b64_tr_b16 v[252:253], v20 offset:6152\n\t"
      "ds_read_b64_tr_b16 v[254:255], v20 offset:7176\n\t"
      "s_waitcnt lgkmcnt(0)\n\t"
      "s_add_u32 m0, %[st], 4096\n\t"
      "s_add_u32 s43, %[cb], 4718592\n\t"
      "buffer_load_dwordx4 v16, %[rq], s43 offen lds\n\t"
      "buffer_load_dwordx4 v17, %[rq], s43 offen offset:1024 lds\n\t"
      "s_add_u32 m0, %[st], 6144\n\t"
      "s_add_u32 s43, %[cb], 4980736\n\t"
      "buffer_load_dwordx4 v16, %[rq], s43 offen lds\n\t"
      "buffer_load_dwordx4 v17, %[rq], s43 offen offset:1024 lds\n\t"
      "s_waitcnt vmcnt(12)\n\t"
      "ds_read_b64_tr_b16 v[24:25], v18 offset:8192\n\t"
      "ds_read_b64_tr_b16 v[26:27], v18 offset:9216\n\t"
      "ds_read_b64_tr_b16 v[28:29], v19 offset:8192\n\t"
      "ds_read_b64_tr_b16 v[30:31], v19 offset:9216\n\t"
      "ds_read_b64_tr_b16 v[32:33], v18 offset:10240\n\t"
      "ds_read_b64_tr_b16 v[34:35], v18 offset:11264\n\t"
      "ds_read_b64_tr_b16 v[36:37], v19 offset:10240\n\t"
      "ds_read_b64_tr_b16 v[38:39], v19 offset:11264\n\t"
      "s_waitcnt lgkmcnt(0)\n\t"
      "ds_write_b128 v1, v[24:27] offset:8192\n\t"
      "ds_write_b128 v1, v[28:31] offset:9216\n\t"
      "ds_write_b128 v1, v[32:35] offset:10240\n\t"
      "ds_write_b128 v1, v[36:39] offset:11264\n\t"
      "s_waitcnt lgkmcnt(0)\n\t"
      "s_waitcnt vmcnt(8)\n\t"
      "ds_read_b64_tr_b16 v[24:25], v18 offset:12288\n\t"
      "ds_read_b64_tr_b16 v[26:27], v18 offset:13312\n\t"
      "ds_read_b64_tr_b16 v[28:29], v19 offset:12288\n\t"
      "ds_read_b64_tr_b16 v[30:31], v19 offset:13312\n\t"
      "ds_read_b64_tr_b16 v[32:33], v18 offset:14336\n\t"
      "ds_read_b64_tr_b16 v[34:35], v18 offset:15360\n\t"
      "ds_read_b64_tr_b16 v[36:37], v19 offset:14336\n\t"
      "ds_read_b64_tr_b16 v[38:39], v19 offset:15360\n\t"
      "s_waitcnt lgkmcnt(0)\n\t"
      "ds_write_b128 v1, v[24:27] offset:12288\n\t"
      "ds_write_b128 v1, v[28:31] offset:13312\n\t"
      "ds_write_b128 v1, v[32:35] offset:14336\n\t"
      "ds_write_b128 v1, v[36:39] offset:15360\n\t"
      "s_waitcnt lgkmcnt(0)\n\t"
      "s_waitcnt vmcnt(4)\n\t"
      "ds_read_b64_tr_b16 v[24:25], v18 offset:0\n\t"
      "ds_read_b64_tr_b16 v[26:27], v18 offset:1024\n\t"
      "ds_read_b64_tr_b16 v[28:29], v19 offset:0\n\t"
      "ds_read_b64_tr_b16 v[30:31], v19 offset:1024\n\t"
      "ds_read_b64_tr_b16 v[32:33], v18 offset:2048\n\t"
      "ds_read_b64_tr_b16 v[34:35], v18 offset:3072\n\t"
      "ds_read_b64_tr_b16 v[36:37], v19 offset:2048\n\t"
      "ds_read_b64_tr_b16 v[38:39], v19 offset:3072\n\t"
      "s_waitcnt lgkmcnt(0)\n\t"
      "ds_write_b128 v1, v[24:27] offset:0\n\t"
      "ds_write_b128 v1, v[28:31] offset:1024\n\t"
      "ds_write_b128 v1, v[32:35] offset:2048\n\t"
      "ds_write_b128 v1, v[36:39] offset:3072\n\t"
      "s_waitcnt lgkmcnt(0)\n\t"
      "s_waitcnt vmcnt(0)\n\t"
      "ds_read_b64_tr_b16 v[24:25], v18 offset:4096\n\t"
      "ds_read_b64_tr_b16 v[26:27], v18 offset:5120\n\t"
      "ds_read_b64_tr_b16 v[28:29], v19 offset:4096\n\t"
      "ds_read_b64_tr_b16 v[30:31], v19 offset:5120\n\t"
      "ds_read_b64_tr_b16 v[32:33], v18 offset:6144\n\t"
      "ds_read_b64_tr_b16 v[34:35], v18 offset:7168\n\t"
      "ds_read_b64_tr_b16 v[36:37], v19 offset:6144\n\t"
      "ds_read_b64_tr_b16 v[38:39], v19 offset:7168\n\t"
      "s_waitcnt lgkmcnt(0)\n\t"
      "ds_write_b128 v1, v[24:27] offset:4096\n\t"
      "ds_write_b128 v1, v[28:31] offset:5120\n\t"
      "ds_write_b128 v1, v[32:35] offset:6144\n\t"
      "ds_write_b128 v1, v[36:39] offset:7168\n\t"
      "s_waitcnt lgkmcnt(0)\n\t"
      "s_waitcnt lgkmcnt(0)\n\t"
      "s_barrier\n\t"
      "ds_read_b128 v[96:99], v1 offset:8192\n\t"
      "ds_read_b128 v[100:103], v1 offset:10240\n\t"
      "ds_read_b128 v[104:107], v1 offset:9216\n\t"
      "ds_read_b128 v[108:111], v1 offset:11264\n\t"
      "ds_read_b128 v[112:115], v1 offset:12288\n\t"
      "ds_read_b128 v[116:119], v1 offset:14336\n\t"
      "ds_read_b128 v[120:123], v1 offset:13312\n\t"
      "ds_read_b128 v[124:127], v1 offset:15360\n\t"
      "s_mov_b32 s42, 0\n\t"
      "s_mov_b32 s59, 1179776\n\t"
      "LGW%=:\n\t"
      "buffer_load_dwordx2 v[24:25], off, %[rs], s59 sc1\n\t"
      "s_add_u32 s42, s42, 1\n\t"
      "s_cmp_gt_u32 s42, 400000\n\t"
      "s_cbranch_scc1 LFAIL%=\n\t"
      "s_waitcnt vmcnt(0)\n\t"
      "v_readfirstlane_b32 s60, v24\n\t"
      "v_readfirstlane_b32 s43, v25\n\t"
      "s_cmp_lg_u32 s60, %[t0]\n\t"
      "s_cbranch_scc1 LGW%=\n\t"
      "s_xor_b32 s43, s43, %[t1]\n\t"
      "s_lshl_b32 s60, s43, 2\n\t"
      "s_cbranch_scc1 LGW%=\n\t"
      "s_lshr_b32 s43, s43, 17\n\t"
      "v_xor_b32 v4, s43, v4\n\t"
      "v_xor_b32 v6, s43, v6\n\t"
      "v_xor_b32 v7, s43, v7\n\t"
      "v_xor_b32 v8, s43, v8\n\t"
      "v_xor_b32 v15, s43, v15\n\t"
      "s_xor_b32 s37, %[ewa], s43\n\t"
      "s_and_b32 s43, s43, 8192\n\t"
      "s_xor_b32 s36, %[ewx], s43\n\t"
      "s_waitcnt lgkmcnt(0)\n\t"
      "ds_read_b128 v[144:147], v1 offset:4096\n\t"
      "ds_read_b128 v[148:151], v1 offset:6144\n\t"
      "ds_read_b128 v[152:155], v1 offset:5120\n\t"
      "ds_read_b128 v[156:159], v1 offset:7168\n\t"
      "ds_read_b128 v[128:131], v1 offset:0\n\t"
      "ds_read_b128 v[132:135], v1 offset:2048\n\t"
      "ds_read_b128 v[136:139], v1 offset:1024\n\t"
      "ds_read_b128 v[140:143], v1 offset:3072\n\t"
      "s_mov_b32 s40, 0\n\t"
      "s_mov_b32 s41, -1\n\t"
      "s_mov_b32 s49, 0\n\t"
      "s_mov_b32 s44, 0x3f803f80\n\t"
      "s_mov_b32 s46, 0x3f003f00\n\t"
      "s_mov_b32 s48, 0x3e0293ee\n\t"
      "s_mov_b64 s[50:51], -1\n\t"
      "s_mov_b32 s45, 0xff800000\n\t"
      "s_lshl_b32 s43, %[w], 11\n\t"
      "s_add_u32 s64, s43, 141312\n\t"
      "s_add_u32 s65, s43, 149504\n\t"
      "s_lshl_b32 s43, %[w], 8\n\t"
      "s_add_u32 s38, s43, 162304\n\t"
      "s_mov_b32 s35, 0\n\t"
      "s_mov_b32 s33, -1\n\t"
      "v_mov_b32 v12, 162048\n\t"
      "v_mov_b32 v14, -1\n\t"
      "ds_write_b32 v12, v14\n\t"
      "s_waitcnt lgkmcnt(0)\n\t"
      "s_mul_i32 s55, s49, 24576\n\t"
      "s_add_u32 s55, s55, %[qso]\n\t"
      "s_add_u32 s56, s55, 8192\n\t"
      "s_add_u32 s57, s55, 16384\n\t"
      "s_cmp_ge_i32 s41, s49\n\t"
      "s_cbranch_scc1 LQ2a%=\n\t"
      "s_mov_b32 s42, 0\n\t"
      "LQ1a%=:\n\t"
      "buffer_load_dword v14, v10, %[rs], 0 offen sc1\n\t"
      "s_add_u32 s42, s42, 1\n\t"
      "s_cmp_gt_u32 s42, 400000\n\t"
      "s_cbranch_scc1 LFAIL%=\n\t"
      "s_waitcnt vmcnt(0)\n\t"
      "v_cmp_gt_i32 vcc, s49, v14\n\t"
      "s_cbranch_vccnz LQ1a%=\n\t"
      "LQ2a%=:\n\t"
      "s_mov_b32 m0, s64\n\t"
      "s_nop 0\n\t"
      "buffer_load_dwordx4 v9, %[rs], s56 offen sc1 lds\n\t"
      "buffer_load_dwordx4 v9, %[rs], s56 offen offset:1024 sc1 lds\n\t"
      "s_mov_b32 m0, s65\n\t"
      "s_nop 0\n\t"
      "buffer_load_dwordx4 v9, %[rs], s55 offen sc1 lds\n\t"
      "buffer_load_dwordx4 v9, %[rs], s55 offen offset:1024 sc1 lds\n\t"
      "s_mov_b32 m0, s38\n\t"
      "buffer_load_dwordx4 v[16:19], v9, %[rs], s57 offen sc1\n\t"
      "buffer_load_dwordx4 v[20:23], v9, %[rs], s57 offen offset:1024 sc1\n\t"
      "s_add_u32 s60, %[oxo], 2097152\n\t"
      "s_lshl_b32 s47, %[w], 10\n\t"
      "s_add_u32 s47, s47, 137216\n\t"
      "s_mov_b32 m0, s47\n\t"
      "s_mov_b32 s54, 0\n\t"
      "s_branch LEWB%=\n\t"
      ".p2align 6\n\t"
      "s_nop 0\n\t"
      "s_nop 0\n\t"
      "s_nop 0\n\t"
      "s_nop 0\n\t"
      "s_nop 0\n\t"
      "s_nop 0\n\t"
      "s_nop 0\n\t"
      "s_nop 0\n\t"
      "s_nop 0\n\t"
      "s_nop 0\n\t"
      "s_nop 0\n\t"
      "s_nop 0\n\t"
      "LOOP%=:\n\t"
      "s_cmp_lg_u32 %[w], 0\n\t"
      "s_cbranch_scc1 LEWB%=\n\t"
      "s_mov_b32 exec_lo, -1\n\t"
      "s_mov_b32 exec_hi, 0xff\n\t"
      "LEW%=:\n\t"
      "s_waitcnt vmcnt(5)\n\t"
      "v_min_i32 v64, v64, v65\n\t"
      "v_cmp_gt_i32 vcc, s40, v64\n\t"
      "s_cbranch_vccz LEWD%=\n\t"
      "buffer_load_dwordx2 v[64:65], v15, %[rs], 0 offen sc1\n\t"
      "s_nop 5\n\t"
      "s_waitcnt vmcnt(5)\n\t"
      "v_min_i32 v66, v66, v67\n\t"
      "v_cmp_gt_i32 vcc, s40, v66\n\t"
      "s_cbranch_vccz LEWD%=\n\t"
      "buffer_load_dwordx2 v[66:67], v15, %[rs], 0 offen sc1\n\t"
      "s_nop 5\n\t"
      "s_waitcnt vmcnt(5)\n\t"
      "v_min_i32 v92, v92, v93\n\t"
      "v_cmp_gt_i32 vcc, s40, v92\n\t"
      "s_cbranch_vccz LEWD%=\n\t"
      "buffer_load_dwordx2 v[92:93], v15, %[rs], 0 offen sc1\n\t"
      "s_nop 5\n\t"
      "s_waitcnt vmcnt(5)\n\t"
      "v_min_i32 v94, v94, v95\n\t"
      "v_cmp_gt_i32 vcc, s40, v94\n\t"
      "s_cbranch_vccz LEWD%=\n\t"
      "buffer_load_dwordx2 v[94:95], v15, %[rs], 0 offen sc1\n\t"
      "s_nop 5\n\t"
      "s_waitcnt vmcnt(5)\n\t"
      "v_min_i32 v84, v84, v85\n\t"
      "v_cmp_gt_i32 vcc, s40, v84\n\t"
      "s_cbranch_vccz LEWD%=\n\t"
      "buffer_load_dwordx2 v[84:85], v15, %[rs], 0 offen sc1\n\t"
      "s_nop 5\n\t"
      "s_waitcnt vmcnt(5)\n\t"
      "v_min_i32 v86, v86, v87\n\t"
      "v_cmp_gt_i32 vcc, s40, v86\n\t"
      "s_cbranch_vccz LEWD%=\n\t"
      "buffer_load_dwordx2 v[86:87], v15, %[rs], 0 offen sc1\n\t"
      "s_nop 5\n\t"
      "s_add_u32 s42, s42, 1\n\t"
      "s_cmp_lt_u32 s42, 100000\n\t"
      "s_cbranch_scc1 LEW%=\n\t"
      "s_branch LFAIL%=\n\t"
      "LEWE%=:\n\t"
      "s_nop 0\n\t"
      "LEWD%=:\n\t"
      "s_sub_u32 s34, s34, 1\n\t"
      "s_cbranch_scc0 LEWE%=\n\t"
      "s_mov_b64 exec, -1\n\t"
      "LEWB%=:\n\t"
      "s_barrier\n\t"
      "buffer_load_dwordx4 v[72:75], v8, %[rs], s54 offen sc1\n\t"
      "buffer_load_dwordx4 v[76:79], v8, %[rs], s54 offen offset:512 sc1\n\t"
      "buffer_load_dwordx4 v6, %[rs], s54 offen sc1 lds\n\t"
      "s_sub_u32 m0, m0, 6144\n\t"
      "s_nop 0\n\t"
      "buffer_load_dwordx4 v7, %[rs], s54 offen sc1 lds\n\t"
      "s_mov_b32 m0, s38\n\t"
      "s_mov_b32 s42, 1\n\t"
      "s_sub_u32 s43, s40, 1\n\t"
      "s_add_u32 s61, s60, 2162688\n\t"
      "v_mov_b32 v70, s43\n\t"
      "v_lshrrev_b32 v71, 8, v9\n\t"
      "v_and_b32 v69, 0xf0, v9\n\t"
      "v_lshlrev_b32 v71, 13, v71\n\t"
      "v_lshl_or_b32 v71, v69, 3, v71\n\t"
      "s_lshl_b32 s52, s54, 2\n\t"
      "s_xor_b32 s53, s52, 0x40000\n\t"
      "s_lshl_b32 s58, s40, 15\n\t"
      "s_add_u32 s58, s58, %[oxo]\n\t"
      "s_add_u32 s49, s40, 1\n\t"
      "s_min_u32 s49, s49, 63\n\t"
      "s_cmp_eq_u32 s40, 63\n\t"
      "s_cselect_b64 s[50:51], 0, s[50:51]\n\t"
      "s_mul_i32 s55, s49, 24576\n\t"
      "s_add_u32 s55, s55, %[qso]\n\t"
      "s_add_u32 s56, s55, 8192\n\t"
      "s_add_u32 s57, s55, 16384\n\t"
      "v_mov_b64 v[24:25], 0\n\t"
      "v_mov_b64 v[26:27], 0\n\t"
      "v_mov_b64 v[28:29], 0\n\t"
      "v_mov_b64 v[30:31], 0\n\t"
      "v_mov_b64 v[32:33], 0\n\t"
      "v_mov_b64 v[34:35], 0\n\t"
      "v_mov_b64 v[36:37], 0\n\t"
      "v_mov_b64 v[38:39], 0\n\t"
      "v_mov_b64 v[40:41], 0\n\t"
      "v_mov_b64 v[42:43], 0\n\t"
      "v_mov_b64 v[44:45], 0\n\t"
      "v_mov_b64 v[46:47], 0\n\t"
      "buffer_store_dword v70, v13, %[rs], s36 offen sc1\n\t"
      "s_mov_b32 exec_hi, 0xffff\n\t"
      "buffer_load_dword v71, %[rs], s55 offen sc1 lds\n\t"
      "s_mov_b32 exec_hi, -1\n\t"
      "s_waitcnt lgkmcnt(0)\n\t"
      "s_waitcnt vmcnt(5)\n\t"
      "v_smfmac_f32_16x16x64_bf16 v[24:27], v[72:75], a[64:71], v5\n\t"
      "v_max3_u32 v14, v72, v73, v74\n\t"
      "v_smfmac_f32_16x16x64_bf16 v[28:31], v[72:75], a[72:79], v5\n\t"
      "v_max3_u32 v14, v14, v75, v75\n\t"
      "v_smfmac_f32_16x16x64_bf16 v[32:35], v[72:75], a[96:103], v5\n\t"
      "v_smfmac_f32_16x16x64_bf16 v[36:39], v[72:75], a[104:111], v5\n\t"
      "s_waitcnt vmcnt(4)\n\t"
      "v_smfmac_f32_16x16x64_bf16 v[24:27], v[76:79], a[80:87], v5\n\t"
      "v_max3_u32 v14, v14, v76, v77\n\t"
      "v_smfmac_f32_16x16x64_bf16 v[28:31], v[76:79], a[88:95], v5\n\t"
      "v_max3_u32 v14, v14, v78, v79\n\t"
      "v_smfmac_f32_16x16x64_bf16 v[32:35], v[76:79], a[112:119], v5\n\t"
      "v_cmp_eq_u32 vcc, -1, v14\n\t"
      "v_smfmac_f32_16x16x64_bf16 v[36:39], v[76:79], a[120:127], v5\n\t"
      "s_cbranch_vccnz LRD%=\n\t"
      "buffer_store_dwordx4 v[48:51], v9, %[rs], s60 offen sc1\n\t"
      "buffer_store_dwordx4 v[52:55], v9, %[rs], s61 offen sc1\n\t"
      "s_waitcnt vmcnt(4)\n\t"
      "LGO%=:\n\t"
      "s_barrier\n\t"
      "ds_read_b128 v[80:83], v0 offset:6144\n\t"
      "ds_read_b128 v[84:87], v0 offset:7168\n\t"
      "v_smfmac_f32_16x16x64_bf16 v[40:43], v[72:75], v[160:167], v5\n\t"
      "ds_read_b128 v[88:91], v0 offset:8192\n\t"
      "v_mov_b32 v14, s40\n\t"
      "v_smfmac_f32_16x16x64_bf16 v[44:47], v[72:75], v[168:175], v5\n\t"
      "ds_read_b128 v[92:95], v0 offset:9216\n\t"
      "v_smfmac_f32_16x16x64_bf16 v[40:43], v[76:79], v[176:183], v5\n\t"
      "ds_read_b128 v[56:59], v0 offset:0\n\t"
      "v_smfmac_f32_16x16x64_bf16 v[44:47], v[76:79], v[184:191], v5\n\t"
      "ds_read_b128 v[60:63], v0 offset:1024\n\t"
      "s_waitcnt lgkmcnt(5)\n\t"
      "v_smfmac_f32_16x16x64_bf16 v[24:27], v[80:83], v[96:103], v5\n\t"
      "ds_read_b128 v[64:67], v0 offset:2048\n\t"
      "v_max3_u32 v11, v80, v81, v82\n\t"
      "v_smfmac_f32_16x16x64_bf16 v[28:31], v[80:83], v[104:111], v5\n\t"
      "ds_read_b128 v[68:71], v0 offset:3072\n\t"
      "s_waitcnt lgkmcnt(6)\n\t"
      "v_max3_u32 v11, v11, v83, v84\n\t"
      "v_smfmac_f32_16x16x64_bf16 v[24:27], v[84:87], v[112:119], v5\n\t"
      "ds_read_b128 v[96:99], v1 offset:16384\n\t"
      "v_max3_u32 v11, v11, v85, v86\n\t"
      "v_smfmac_f32_16x16x64_bf16 v[28:31], v[84:87], v[120:127], v5\n\t"
      "ds_read_b128 v[100:103], v1 offset:18432\n\t"
      "s_waitcnt lgkmcnt(6)\n\t"
      "v_max3_u32 v11, v11, v87, v88\n\t"
      "v_smfmac_f32_16x16x64_bf16 v[24:27], v[88:91], v[128:135], v5\n\t"
      "ds_read_b128 v[104:107], v1 offset:17408\n\t"
      "v_max3_u32 v11, v11, v89, v90\n\t"
      "v_smfmac_f32_16x16x64_bf16 v[28:31], v[88:91], v[136:143], v5\n\t"
      "ds_read_b128 v[108:111], v1 offset:19456\n\t"
      "v_max3_u32 v11, v11, v91, v92\n\t"
      "v_smfmac_f32_16x16x64_bf16 v[24:27], v[92:95], v[144:151], v5\n\t"
      "ds_read_b128 v[112:115], v1 offset:20480\n\t"
      "s_waitcnt lgkmcnt(5)\n\t"
      "v_max3_u32 v11, v11, v93, v94\n\t"
      "v_smfmac_f32_16x16x64_bf16 v[28:31], v[92:95], v[152:159], v5\n\t"
      "ds_read_b128 v[116:119], v1 offset:22528\n\t"
      "v_max3_u32 v11, v11, v95, v56\n\t"
      "v_smfmac_f32_16x16x64_bf16 v[24:27], v[56:59], a[0:7], v5\n\t"
      "ds_read_b128 v[120:123], v1 offset:21504\n\t"
      "v_max3_u32 v11, v11, v57, v58\n\t"
      "v_smfmac_f32_16x16x64_bf16 v[28:31], v[56:59], a[8:15], v5\n\t"
      "ds_read_b128 v[124:127], v1 offset:23552\n\t"
      "v_max3_u32 v11, v11, v59, v60\n\t"
      "v_smfmac_f32_16x16x64_bf16 v[24:27], v[60:63], a[16:23], v5\n\t"
      "ds_read_b128 v[128:131], v1 offset:24576\n\t"
      "v_max3_u32 v11, v11, v61, v62\n\t"
      "v_smfmac_f32_16x16x64_bf16 v[24:27], v[64:67], a[32:39], v5\n\t"
      "ds_read_b128 v[132:135], v1 offset:26624\n\t"
      "v_max3_u32 v11, v11, v63, v64\n\t"
      "v_smfmac_f32_16x16x64_bf16 v[24:27], v[68:71], a[48:55], v5\n\t"
      "ds_read_b128 v[136:139], v1 offset:25600\n\t"
      "v_max3_u32 v11, v11, v65, v66\n\t"
      "v_smfmac_f32_16x16x64_bf16 v[28:31], v[60:63], a[24:31], v5\n\t"
      "ds_read_b128 v[140:143], v1 offset:27648\n\t"
      "v_max3_u32 v11, v11, v67, v68\n\t"
      "v_smfmac_f32_16x16x64_bf16 v[28:31], v[64:67], a[40:47], v5\n\t"
      "ds_read_b128 v[144:147], v1 offset:28672\n\t"
      "s_waitcnt lgkmcnt(9)\n\t"
      "v_max3_u32 v11, v11, v69, v70\n\t"
      "v_smfmac_f32_16x16x64_bf16 v[28:31], v[68:71], a[56:63], v5\n\t"
      "ds_read_b128 v[148:151], v1 offset:30720\n\t"
      "v_max3_u32 v11, v11, v71, v71\n\t"
      "v_smfmac_f32_16x16x64_bf16 v[32:35], v[80:83], a[128:135], v5\n\t"
      "ds_read_b128 v[152:155], v1 offset:29696\n\t"
      "v_cmp_eq_u32 vcc, -1, v11\n\t"
      "v_add_f32 v24, v24, v26\n\t"
      "v_smfmac_f32_16x16x64_bf16 v[36:39], v[80:83], a[136:143], v5\n\t"
      "s_cbranch_vccnz LREDO%=\n\t"
      "ds_read_b128 v[156:159], v1 offset:31744\n\t"
      "v_add_f32 v25, v25, v27\n\t"
      "v_add_f32 v28, v28, v30\n\t"
      "v_smfmac_f32_16x16x64_bf16 v[32:35], v[56:59], v[96:103], v5\n\t"
      "s_waitcnt lgkmcnt(8)\n\t"
      "v_add_f32 v29, v29, v31\n\t"
      "v_smfmac_f32_16x16x64_bf16 v[36:39], v[56:59], v[104:111], v5\n\t"
      "s_waitcnt lgkmcnt(4)\n\t"
      "v_cvt_pk_bf16_f32 v24, v24, v28\n\t"
      "v_smfmac_f32_16x16x64_bf16 v[32:35], v[60:63], v[112:119], v5\n\t"
      "v_cvt_pk_bf16_f32 v25, v25, v29\n\t"
      "v_smfmac_f32_16x16x64_bf16 v[36:39], v[60:63], v[120:127], v5\n\t"
      "ds_write2_b32 v2, v24, v25 offset1:136\n\t"
      "v_smfmac_f32_16x16x64_bf16 v[32:35], v[64:67], v[128:135], v5\n\t"
      "ds_read_b128 v[96:99], v0 offset:10240\n\t"
      "s_waitcnt lgkmcnt(2)\n\t"
      "v_smfmac_f32_16x16x64_bf16 v[36:39], v[64:67], v[136:143], v5\n\t"
      "ds_read_b128 v[112:115], v0 offset:14336\n\t"
      "v_smfmac_f32_16x16x64_bf16 v[32:35], v[68:71], v[144:151], v5\n\t"
      "ds_read_b128 v[100:103], v0 offset:11264\n\t"
      "v_smfmac_f32_16x16x64_bf16 v[36:39], v[68:71], v[152:159], v5\n\t"
      "s_waitcnt lgkmcnt(3)\n\t"
      "s_barrier\n\t"
      "ds_read_b128 v[128:131], v3 offset:0\n\t"
      "v_smfmac_f32_16x16x64_bf16 v[32:35], v[84:87], a[144:151], v5\n\t"
      "buffer_store_dword v14, v13, %[rs], s37 offen sc1\n\t"
      "ds_read_b128 v[132:135], v3 offset:64\n\t"
      "v_smfmac_f32_16x16x64_bf16 v[32:35], v[88:91], a[160:167], v5\n\t"
      "ds_read_b128 v[136:139], v3 offset:128\n\t"
      "v_smfmac_f32_16x16x64_bf16 v[32:35], v[92:95], a[176:183], v5\n\t"
      "ds_read_b128 v[140:143], v3 offset:192\n\t"
      "v_smfmac_f32_16x16x64_bf16 v[36:39], v[84:87], a[152:159], v5\n\t"
      "ds_read_b128 v[116:119], v0 offset:15360\n\t"
      "v_smfmac_f32_16x16x64_bf16 v[36:39], v[88:91], a[168:175], v5\n\t"
      "ds_read_b128 v[104:107], v0 offset:12288\n\t"
      "v_smfmac_f32_16x16x64_bf16 v[36:39], v[92:95], a[184:191], v5\n\t"
      "ds_read_b128 v[120:123], v0 offset:16384\n\t"
      "s_waitcnt lgkmcnt(5)\n\t"
      "v_add_f32 v32, v32, v34\n\t"
      "v_mfma_f32_16x16x32_bf16 v[48:51], v[96:99], v[128:131], 0\n\t"
      "ds_read_b128 v[108:111], v0 offset:13312\n\t"
      "v_add_f32 v33, v33, v35\n\t"
      "v_mfma_f32_16x16x32_bf16 v[52:55], v[112:115], v[128:131], 0\n\t"
      "ds_read_b128 v[124:127], v0 offset:17408\n\t"
      "v_add_f32 v36, v36, v38\n\t"
      "v_add_f32 v37, v37, v39\n\t"
      "v_cvt_pk_bf16_f32 v32, v32, v36\n\t"
      "v_cvt_pk_bf16_f32 v33, v33, v37\n\t"
      "ds_write2_b32 v2, v32, v33 offset0:64 offset1:200\n\t"
      "s_waitcnt lgkmcnt(3)\n\t"
      "v_mfma_f32_16x16x32_bf16 v[48:51], v[100:103], v[132:135], v[48:51]\n\t"
      "v_mfma_f32_16x16x32_bf16 v[52:55], v[116:119], v[132:135], v[52:55]\n\t"
      "v_mfma_f32_16x16x32_bf16 v[48:51], v[104:107], v[136:139], v[48:51]\n\t"
      "v_mfma_f32_16x16x32_bf16 v[52:55], v[120:123], v[136:139], v[52:55]\n\t"
      "s_waitcnt lgkmcnt(0)\n\t"
      "s_barrier\n\t"
      "ds_read_b128 v[144:147], v3 offset:256\n\t"
      "v_mfma_f32_16x16x32_bf16 v[48:51], v[108:111], v[140:143], v[48:51]\n\t"
      "ds_read_b128 v[148:151], v3 offset:320\n\t"
      "ds_read_b128 v[152:155], v3 offset:384\n\t"
      "ds_read_b128 v[156:159], v3 offset:448\n\t"
      "v_mfma_f32_16x16x32_bf16 v[52:55], v[124:127], v[140:143], v[52:55]\n\t"
      "v_smfmac_f32_16x16x64_bf16 v[40:43], v[56:59], a[192:199], v5\n\t"
      "v_smfmac_f32_16x16x64_bf16 v[44:47], v[56:59], a[200:207], v5\n\t"
      "v_smfmac_f32_16x16x64_bf16 v[40:43], v[60:63], a[208:215], v5\n\t"
      "s_waitcnt lgkmcnt(3)\n\t"
      "v_mfma_f32_16x16x32_bf16 v[24:27], v[144:147], v[128:131], 0\n\t"
      "s_waitcnt lgkmcnt(0)\n\t"
      "v_mov_b32 v56, s44\n\t"
      "v_mov_b32 v57, s44\n\t"
      "v_mfma_f32_16x16x32_bf16 v[24:27], v[148:151], v[132:135], v[24:27]\n\t"
      "v_max3_f32 v28, v48, v49, v50\n\t"
      "v_max3_f32 v29, v52, v53, v54\n\t"
      "v_mfma_f32_16x16x32_bf16 v[24:27], v[152:155], v[136:139], v[24:27]\n\t"
      "v_max3_f32 v28, v28, v51, v55\n\t"
      "v_mov_b32 v58, s44\n\t"
      "v_mfma_f32_16x16x32_bf16 v[24:27], v[156:159], v[140:143], v[24:27]\n\t"
      "v_max_f32 v28, v28, v29\n\t"
      "v_mov_b32 v59, s44\n\t"
      "v_smfmac_f32_16x16x64_bf16 v[44:47], v[60:63], a[216:223], v5\n\t"
      "v_smfmac_f32_16x16x64_bf16 v[40:43], v[64:67], a[224:231], v5\n\t"
      "v_max3_f32 v30, v28, v24, v25\n\t"
      "v_max3_f32 v31, v28, v24, v25\n\t"
      "v_smfmac_f32_16x16x64_bf16 v[44:47], v[64:67], a[232:239], v5\n\t"
      "s_nop 0\n\t"
      "v_permlane16_swap_b32 v30, v31\n\t"
      "v_max_f32 v28, v30, v31\n\t"
      "v_max_f32 v29, v30, v31\n\t"
      "v_smfmac_f32_16x16x64_bf16 v[40:43], v[68:71], a[240:247], v5\n\t"
      "s_nop 0\n\t"
      "v_permlane32_swap_b32 v28, v29\n\t"
      "v_max_f32 v28, v28, v29\n\t"
      "v_mul_f32 v28, s48, v28\n\t"
      "v_smfmac_f32_16x16x64_bf16 v[44:47], v[68:71], a[248:255], v5\n\t"
      "v_fma_f32 v48, v48, s48, -v28\n\t"
      "v_exp_f32 v48, v48\n\t"
      "v_smfmac_f32_16x16x64_bf16 v[40:43], v[80:83], v[192:199], v5\n\t"
      "v_fma_f32 v49, v49, s48, -v28\n\t"
      "v_exp_f32 v49, v49\n\t"
      "v_fma_f32 v50, v50, s48, -v28\n\t"
      "v_smfmac_f32_16x16x64_bf16 v[40:43], v[84:87], v[208:215], v5\n\t"
      "v_exp_f32 v50, v50\n\t"
      "v_fma_f32 v51, v51, s48, -v28\n\t"
      "v_smfmac_f32_16x16x64_bf16 v[40:43], v[88:91], v[224:231], v5\n\t"
      "v_exp_f32 v51, v51\n\t"
      "v_fma_f32 v52, v52, s48, -v28\n\t"
      "v_cvt_pk_bf16_f32 v64, v48, v49\n\t"
      "v_smfmac_f32_16x16x64_bf16 v[44:47], v[80:83], v[200:207], v5\n\t"
      "v_exp_f32 v52, v52\n\t"
      "v_fma_f32 v53, v53, s48, -v28\n\t"
      "v_smfmac_f32_16x16x64_bf16 v[40:43], v[92:95], v[240:247], v5\n\t"
      "v_cvt_pk_bf16_f32 v65, v50, v51\n\t"
      "v_exp_f32 v53, v53\n\t"
      "v_fma_f32 v54, v54, s48, -v28\n\t"
      "v_smfmac_f32_16x16x64_bf16 v[44:47], v[84:87], v[216:223], v5\n\t"
      "v_exp_f32 v54, v54\n\t"
      "v_fma_f32 v55, v55, s48, -v28\n\t"
      "v_smfmac_f32_16x16x64_bf16 v[44:47], v[88:91], v[232:239], v5\n\t"
      "v_cvt_pk_bf16_f32 v66, v52, v53\n\t"
      "v_exp_f32 v55, v55\n\t"
      "v_fma_f32 v24, v24, s48, -v28\n\t"
      "v_smfmac_f32_16x16x64_bf16 v[44:47], v[92:95], v[248:255], v5\n\t"
      "v_cvt_pk_bf16_f32 v67, v54, v55\n\t"
      "v_fma_f32 v25, v25, s48, -v28\n\t"
      "v_mov_b32 v60, s46\n\t"
      "v_mfma_f32_16x16x32_bf16 v[68:71], v[64:67], v[56:59], 0\n\t"
      "v_exp_f32 v24, v24\n\t"
      "v_mfma_f32_16x16x32_bf16 v[32:35], v[64:67], v[16:19], 0\n\t"
      "v_exp_f32 v25, v25\n\t"
      "v_mfma_f32_16x16x32_bf16 v[36:39], v[64:67], v[20:23], 0\n\t"
      "v_mov_b32 v61, s46\n\t"
      "v_cvt_pk_bf16_f32 v62, v24, v25\n\t"
      "v_cvt_pk_bf16_f32 v63, v24, v25\n\t"
      "v_cvt_pk_bf16_f32 v40, v40, v41\n\t"
      "v_cvt_pk_bf16_f32 v41, v42, v43\n\t"
      "v_mfma_f32_16x16x16_bf16 v[68:71], v[62:63], v[60:61], v[68:71]\n\t"
      "v_cvt_pk_bf16_f32 v42, v44, v45\n\t"
      "v_cvt_pk_bf16_f32 v43, v46, v47\n\t"
      "v_mfma_f32_16x16x16_bf16 v[32:35], v[62:63], v[40:41], v[32:35]\n\t"
      "s_nop 0\n\t"
      "v_mfma_f32_16x16x16_bf16 v[36:39], v[62:63], v[42:43], v[36:39]\n\t"
      "v_rcp_f32 v68, v68\n\t"
      "v_rcp_f32 v69, v69\n\t"
      "s_mov_b64 exec, s[50:51]\n\t"
      "s_nop 1\n\t"
      "v_mul_f32 v32, v32, v68\n\t"
      "v_mul_f32 v33, v33, v69\n\t"
      "s_nop 0\n\t"
      "v_mul_f32 v36, v36, v68\n\t"
      "v_mul_f32 v37, v37, v69\n\t"
      "v_cvt_pk_bf16_f32 v44, v32, v36\n\t"
      "v_cvt_pk_bf16_f32 v45, v33, v37\n\t"
      "buffer_store_dword v44, v4, %[rs], s52 offen sc1\n\t"
      "buffer_store_dword v45, v4, %[rs], s52 offen offset:64 sc1\n\t"
      "s_mov_b64 exec, -1\n\t"
      "ds_read_b128 v[64:67], v0 offset:18432\n\t"
      "ds_read_b128 v[68:71], v0 offset:19456\n\t"
      "ds_read_b128 v[48:51], v0 offset:20480\n\t"
      "ds_read_b128 v[52:55], v0 offset:21504\n\t"
      "ds_read_b128 v[80:83], v0 offset:22528\n\t"
      "ds_read_b128 v[84:87], v0 offset:23552\n\t"
      "ds_read_b128 v[88:91], v0 offset:24576\n\t"
      "ds_read_b128 v[92:95], v0 offset:25600\n\t"
      "s_cmp_ge_i32 s41, s49\n\t"
      "s_cbranch_scc1 LQ2b%=\n\t"
      "s_mov_b32 s42, 0\n\t"
      "LQ1b%=:\n\t"
      "buffer_load_dword v14, v10, %[rs], 0 offen sc1\n\t"
      "s_add_u32 s42, s42, 1\n\t"
      "s_cmp_gt_u32 s42, 400000\n\t"
      "s_cbranch_scc1 LFAIL%=\n\t"
      "s_waitcnt vmcnt(0)\n\t"
      "v_cmp_gt_i32 vcc, s49, v14\n\t"
      "s_cbranch_vccnz LQ1b%=\n\t"
      "LQ2b%=:\n\t"
      "v_mov_b32 v62, s45\n\t"
      "v_mov_b32 v63, -1\n\t"
      "buffer_load_dword v14, v10, %[rs], 0 offen sc1\n\t"
      "s_waitcnt lgkmcnt(7)\n\t"
      "v_mfma_f32_16x16x32_bf16 v[24:27], v[144:147], v[64:67], 0\n\t"
      "buffer_store_dword v63, v4, %[rs], s53 offen sc1\n\t"
      "v_mfma_f32_16x16x32_bf16 v[28:31], v[96:99], v[64:67], 0\n\t"
      "s_waitcnt lgkmcnt(6)\n\t"
      "v_mfma_f32_16x16x32_bf16 v[24:27], v[148:151], v[68:71], v[24:27]\n\t"
      "buffer_store_dword v63, v4, %[rs], s53 offen offset:64 sc1\n\t"
      "v_mfma_f32_16x16x32_bf16 v[28:31], v[100:103], v[68:71], v[28:31]\n\t"
      "s_waitcnt lgkmcnt(5)\n\t"
      "v_mfma_f32_16x16x32_bf16 v[24:27], v[152:155], v[48:51], v[24:27]\n\t"
      "buffer_load_dwordx4 v[72:75], v9, %[rs], s57 offen sc1\n\t"
      "v_mfma_f32_16x16x32_bf16 v[28:31], v[104:107], v[48:51], v[28:31]\n\t"
      "s_waitcnt lgkmcnt(4)\n\t"
      "v_mfma_f32_16x16x32_bf16 v[24:27], v[156:159], v[52:55], v[24:27]\n\t"
      "buffer_load_dwordx4 v[76:79], v9, %[rs], s57 offen offset:1024 sc1\n\t"
      "v_mfma_f32_16x16x32_bf16 v[28:31], v[108:111], v[52:55], v[28:31]\n\t"
      "s_waitcnt lgkmcnt(3)\n\t"
      "v_mfma_f32_16x16x32_bf16 v[32:35], v[144:147], v[80:83], 0\n\t"
      "s_waitcnt lgkmcnt(0)\n\t"
      "s_barrier\n\t"
      "v_mfma_f32_16x16x32_bf16 v[36:39], v[96:99], v[80:83], 0\n\t"
      "s_mov_b32 m0, s64\n\t"
      "s_nop 0\n\t"
      "buffer_load_dwordx4 v9, %[rs], s56 offen sc1 lds\n\t"
      "v_mfma_f32_16x16x32_bf16 v[44:47], v[112:115], v[80:83], 0\n\t"
      "ds_read_b128 v[128:131], v1 offset:0\n\t"
      "ds_read_b128 v[132:135], v1 offset:2048\n\t"
      "v_mfma_f32_16x16x32_bf16 v[32:35], v[148:151], v[84:87], v[32:35]\n\t"
      "buffer_load_dwordx4 v9, %[rs], s56 offen offset:1024 sc1 lds\n\t"
      "v_cndmask_b32 v28, v62, v28, %[m0]\n\t"
      "v_cndmask_b32 v29, v62, v29, %[m1]\n\t"
      "v_mfma_f32_16x16x32_bf16 v[36:39], v[100:103], v[84:87], v[36:39]\n\t"
      "v_cndmask_b32 v30, v62, v30, %[m2]\n\t"
      "v_cndmask_b32 v31, v62, v31, %[m3]\n\t"
      "v_mfma_f32_16x16x32_bf16 v[44:47], v[116:119], v[84:87], v[44:47]\n\t"
      "s_mov_b32 m0, s65\n\t"
      "s_nop 0\n\t"
      "buffer_load_dwordx4 v9, %[rs], s55 offen sc1 lds\n\t"
      "v_max3_f32 v64, v24, v25, v28\n\t"
      "v_max3_f32 v65, v29, v30, v31\n\t"
      "v_mfma_f32_16x16x32_bf16 v[32:35], v[152:155], v[88:91], v[32:35]\n\t"
      "ds_read_b128 v[136:139], v1 offset:1024\n\t"
      "v_max_f32 v66, v64, v65\n\t"
      "v_max_f32 v67, v64, v65\n\t"
      "v_mfma_f32_16x16x32_bf16 v[36:39], v[104:107], v[88:91], v[36:39]\n\t"
      "buffer_load_dwordx4 v9, %[rs], s55 offen offset:1024 sc1 lds\n\t"
      "s_mov_b32 m0, s38\n\t"
      "s_nop 0\n\t"
      "v_permlane16_swap_b32 v66, v67\n\t"
      "v_mfma_f32_16x16x32_bf16 v[44:47], v[120:123], v[88:91], v[44:47]\n\t"
      "ds_read_b128 v[140:143], v1 offset:3072\n\t"
      "v_max_f32 v64, v66, v67\n\t"
      "v_max_f32 v65, v66, v67\n\t"
      "v_mfma_f32_16x16x32_bf16 v[32:35], v[156:159], v[92:95], v[32:35]\n\t"
      "s_nop 0\n\t"
      "v_permlane32_swap_b32 v64, v65\n\t"
      "v_mfma_f32_16x16x32_bf16 v[36:39], v[108:111], v[92:95], v[36:39]\n\t"
      "v_max_f32 v64, v64, v65\n\t"
      "v_mul_f32 v64, s48, v64\n\t"
      "v_mfma_f32_16x16x32_bf16 v[44:47], v[124:127], v[92:95], v[44:47]\n\t"
      "ds_read_b128 v[96:99], v1 offset:8192\n\t"
      "ds_read_b128 v[100:103], v1 offset:10240\n\t"
      "ds_read_b128 v[104:107], v1 offset:9216\n\t"
      "ds_read_b128 v[108:111], v1 offset:11264\n\t"
      "v_fma_f32 v28, v28, s48, -v64\n\t"
      "v_exp_f32 v28, v28\n\t"
      "v_fma_f32 v29, v29, s48, -v64\n\t"
      "v_exp_f32 v29, v29\n\t"
      "v_fma_f32 v30, v30, s48, -v64\n\t"
      "v_exp_f32 v30, v30\n\t"
      "v_fma_f32 v31, v31, s48, -v64\n\t"
      "v_exp_f32 v31, v31\n\t"
      "v_fma_f32 v24, v24, s48, -v64\n\t"
      "v_exp_f32 v24, v24\n\t"
      "v_fma_f32 v25, v25, s48, -v64\n\t"
      "v_exp_f32 v25, v25\n\t"
      "v_cndmask_b32 v44, v62, v44, %[m0]\n\t"
      "v_cndmask_b32 v45, v62, v45, %[m1]\n\t"
      "v_cndmask_b32 v46, v62, v46, %[m2]\n\t"
      "v_cndmask_b32 v47, v62, v47, %[m3]\n\t"
      "v_cvt_pk_bf16_f32 v68, v28, v29\n\t"
      "v_cvt_pk_bf16_f32 v69, v30, v31\n\t"
      "v_cvt_pk_bf16_f32 v70, v24, v25\n\t"
      "v_cvt_pk_bf16_f32 v71, v24, v25\n\t"
      "v_max3_f32 v64, v32, v33, v36\n\t"
      "v_max3_f32 v65, v37, v38, v39\n\t"
      "v_mfma_f32_16x16x16_bf16 v[80:83], v[56:57], v[68:69], 0\n\t"
      "ds_read_b128 v[112:115], v1 offset:12288\n\t"
      "v_max3_f32 v64, v64, v44, v45\n\t"
      "v_max3_f32 v65, v65, v46, v47\n\t"
      "v_mfma_f32_16x16x16_bf16 v[84:87], v[16:17], v[68:69], 0\n\t"
      "ds_read_b128 v[116:119], v1 offset:14336\n\t"
      "v_max_f32 v66, v64, v65\n\t"
      "v_max_f32 v67, v64, v65\n\t"
      "v_mfma_f32_16x16x16_bf16 v[88:91], v[20:21], v[68:69], 0\n\t"
      "ds_read_b128 v[120:123], v1 offset:13312\n\t"
      "v_permlane16_swap_b32 v66, v67\n\t"
      "v_mfma_f32_16x16x16_bf16 v[80:83], v[60:61], v[70:71], v[80:83]\n\t"
      "ds_read_b128 v[124:127], v1 offset:15360\n\t"
      "v_max_f32 v64, v66, v67\n\t"
      "v_max_f32 v65, v66, v67\n\t"
      "v_mfma_f32_16x16x16_bf16 v[84:87], v[40:41], v[70:71], v[84:87]\n\t"
      "ds_read_b128 v[144:147], v1 offset:4096\n\t"
      "v_permlane32_swap_b32 v64, v65\n\t"
      "v_mfma_f32_16x16x16_bf16 v[88:91], v[42:43], v[70:71], v[88:91]\n\t"
      "ds_read_b128 v[148:151], v1 offset:6144\n\t"
      "v_max_f32 v64, v64, v65\n\t"
      "v_mul_f32 v64, s48, v64\n\t"
      "s_cmp_lg_u32 s40, 63\n\t"
      "s_cbranch_scc1 LNL%=\n\t"
      "v_mov_b32 v63, 63\n\t"
      "buffer_store_dword v63, v13, %[rs], s36 offen sc1\n\t"
      "LNL%=:\n\t"
      "s_cmp_lg_u32 %[w], 0\n\t"
      "v_fma_f32 v36, v36, s48, -v64\n\t"
      "v_exp_f32 v36, v36\n\t"
      "v_fma_f32 v37, v37, s48, -v64\n\t"
      "v_exp_f32 v37, v37\n\t"
      "v_fma_f32 v38, v38, s48, -v64\n\t"
      "v_exp_f32 v38, v38\n\t"
      "v_fma_f32 v39, v39, s48, -v64\n\t"
      "v_exp_f32 v39, v39\n\t"
      "v_fma_f32 v44, v44, s48, -v64\n\t"
      "v_exp_f32 v44, v44\n\t"
      "v_fma_f32 v45, v45, s48, -v64\n\t"
      "v_exp_f32 v45, v45\n\t"
      "v_fma_f32 v46, v46, s48, -v64\n\t"
      "v_exp_f32 v46, v46\n\t"
      "v_fma_f32 v47, v47, s48, -v64\n\t"
      "v_exp_f32 v47, v47\n\t"
      "v_fma_f32 v32, v32, s48, -v64\n\t"
      "v_exp_f32 v32, v32\n\t"
      "v_fma_f32 v33, v33, s48, -v64\n\t"
      "v_exp_f32 v33, v33\n\t"
      "v_rcp_f32 v80, v80\n\t"
      "v_cvt_pk_bf16_f32 v92, v36, v37\n\t"
      "v_cvt_pk_bf16_f32 v93, v38, v39\n\t"
      "v_cvt_pk_bf16_f32 v94, v44, v45\n\t"
      "v_cvt_pk_bf16_f32 v95, v46, v47\n\t"
      "v_cvt_pk_bf16_f32 v68, v32, v33\n\t"
      "v_cvt_pk_bf16_f32 v69, v32, v33\n\t"
      "v_mfma_f32_16x16x32_bf16 v[24:27], v[56:59], v[92:95], 0\n\t"
      "buffer_load_dwordx2 v[64:65], v15, %[rs], 0 offen sc1\n\t"
      "v_mul_f32 v84, v84, v80\n\t"
      "v_mul_f32 v85, v85, v80\n\t"
      "v_mfma_f32_16x16x32_bf16 v[28:31], v[16:19], v[92:95], 0\n\t"
      "s_waitcnt lgkmcnt(8)\n\t"
      "ds_read_b32 v83, v12\n\t"
      "v_mul_f32 v86, v86, v80\n\t"
      "v_mul_f32 v87, v87, v80\n\t"
      "v_mfma_f32_16x16x32_bf16 v[32:35], v[20:23], v[92:95], 0\n\t"
      "buffer_load_dwordx2 v[66:67], v15, %[rs], 0 offen sc1\n\t"
      "v_mul_f32 v88, v88, v80\n\t"
      "v_mul_f32 v89, v89, v80\n\t"
      "v_mfma_f32_16x16x16_bf16 v[24:27], v[60:61], v[68:69], v[24:27]\n\t"
      "ds_read_b128 v[152:155], v1 offset:5120\n\t"
      "v_mul_f32 v90, v90, v80\n\t"
      "v_mul_f32 v91, v91, v80\n\t"
      "v_mfma_f32_16x16x16_bf16 v[28:31], v[40:41], v[68:69], v[28:31]\n\t"
      "ds_read_b128 v[156:159], v1 offset:7168\n\t"
      "buffer_load_dwordx2 v[92:93], v15, %[rs], 0 offen sc1\n\t"
      "v_cvt_pk_bf16_f32 v48, v84, v85\n\t"
      "v_cvt_pk_bf16_f32 v49, v86, v87\n\t"
      "v_mfma_f32_16x16x16_bf16 v[32:35], v[42:43], v[68:69], v[32:35]\n\t"
      "s_waitcnt vmcnt(7)\n\t"
      "v_cvt_pk_bf16_f32 v50, v88, v89\n\t"
      "v_cvt_pk_bf16_f32 v51, v90, v91\n\t"
      "v_mov_b64 v[16:17], v[72:73]\n\t"
      "v_mov_b64 v[18:19], v[74:75]\n\t"
      "v_rcp_f32 v24, v24\n\t"
      "v_mov_b64 v[20:21], v[76:77]\n\t"
      "v_mov_b64 v[22:23], v[78:79]\n\t"
      "s_add_u32 s43, s49, 1\n\t"
      "s_min_u32 s43, s43, 63\n\t"
      "buffer_load_dwordx2 v[94:95], v15, %[rs], 0 offen sc1\n\t"
      "v_pk_mul_f32 v[28:29], v[28:29], v[24:25] op_sel_hi:[1,0]\n\t"
      "v_pk_mul_f32 v[30:31], v[30:31], v[24:25] op_sel_hi:[1,0]\n\t"
      "v_pk_mul_f32 v[32:33], v[32:33], v[24:25] op_sel_hi:[1,0]\n\t"
      "v_pk_mul_f32 v[34:35], v[34:35], v[24:25] op_sel_hi:[1,0]\n\t"
      "v_cvt_pk_bf16_f32 v52, v28, v29\n\t"
      "v_cvt_pk_bf16_f32 v53, v30, v31\n\t"
      "v_cvt_pk_bf16_f32 v54, v32, v33\n\t"
      "v_cvt_pk_bf16_f32 v55, v34, v35\n\t"
      "s_waitcnt lgkmcnt(2)\n\t"
      "v_readfirstlane_b32 s59, v83\n\t"
      "v_cmp_gt_i32 vcc, s43, v14\n\t"
      "s_cmp_eq_u32 s40, 63\n\t"
      "s_cbranch_scc1 LLAST%=\n\t"
      "buffer_load_dwordx2 v[84:85], v15, %[rs], 0 offen sc1\n\t"
      "s_cmp_eq_u64 vcc, 0\n\t"
      "s_cselect_b32 s41, s43, s41\n\t"
      "s_cmp_lg_u32 s59, s40\n\t"
      "s_cbranch_scc1 LNA%=\n\t"
      "s_cmp_lt_u32 s40, 2\n\t"
      "s_cbranch_scc1 LNA%=\n\t"
      "s_cmp_le_i32 s40, s33\n\t"
      "s_addc_u32 s35, s35, 0\n\t"
      "s_add_u32 s33, s40, 3\n\t"
      "LNA%=:\n\t"
      "s_mov_b32 s60, s58\n\t"
      "s_add_u32 s40, s40, 1\n\t"
      "s_mov_b32 s34, s35\n\t"
      "s_and_b32 s43, s40, 3\n\t"
      "s_lshl_b32 s54, s43, 15\n\t"
      "s_mov_b32 s42, 0\n\t"
      "s_mov_b32 m0, s47\n\t"
      "buffer_load_dwordx2 v[86:87], v15, %[rs], 0 offen sc1\n\t"
      "s_branch LOOP%=\n\t"
      "LLAST%=:\n\t"
      "s_add_u32 s61, s58, 2162688\n\t"
      "buffer_store_dwordx4 v[48:51], v9, %[rs], s58 offen sc1\n\t"
      "buffer_store_dwordx4 v[52:55], v9, %[rs], s61 offen sc1\n\t"
      "s_branch LEND%=\n\t"
      "LRD%=:\n\t"
      "buffer_load_dwordx4 v[72:75], v8, %[rs], s54 offen sc1\n\t"
      "buffer_load_dwordx4 v[76:79], v8, %[rs], s54 offen offset:512 sc1\n\t"
      "s_add_u32 s42, s42, 1\n\t"
      "s_cmp_gt_u32 s42, 400000\n\t"
      "s_cbranch_scc1 LFAIL%=\n\t"
      "v_mov_b32 v14, s40\n\t"
      "ds_write_b32 v12, v14\n\t"
      "s_nop 3\n\t"
      "v_mov_b64 v[24:25], 0\n\t"
      "v_mov_b64 v[26:27], 0\n\t"
      "v_mov_b64 v[28:29], 0\n\t"
      "v_mov_b64 v[30:31], 0\n\t"
      "v_mov_b64 v[32:33], 0\n\t"
      "v_mov_b64 v[34:35], 0\n\t"
      "v_mov_b64 v[36:37], 0\n\t"
      "v_mov_b64 v[38:39], 0\n\t"
      "s_waitcnt vmcnt(0) lgkmcnt(0)\n\t"
      "v_max3_u32 v14, v72, v73, v74\n\t"
      "v_max3_u32 v14, v14, v75, v75\n\t"
      "v_max3_u32 v14, v14, v76, v77\n\t"
      "v_max3_u32 v14, v14, v78, v79\n\t"
      "v_cmp_eq_u32 vcc, -1, v14\n\t"
      "s_cbranch_vccnz LRD%=\n\t"
      "v_smfmac_f32_16x16x64_bf16 v[24:27], v[72:75], a[64:71], v5\n\t"
      "v_smfmac_f32_16x16x64_bf16 v[28:31], v[72:75], a[72:79], v5\n\t"
      "v_smfmac_f32_16x16x64_bf16 v[32:35], v[72:75], a[96:103], v5\n\t"
      "v_smfmac_f32_16x16x64_bf16 v[36:39], v[72:75], a[104:111], v5\n\t"
      "v_smfmac_f32_16x16x64_bf16 v[24:27], v[76:79], a[80:87], v5\n\t"
      "v_smfmac_f32_16x16x64_bf16 v[28:31], v[76:79], a[88:95], v5\n\t"
      "v_smfmac_f32_16x16x64_bf16 v[32:35], v[76:79], a[112:119], v5\n\t"
      "v_smfmac_f32_16x16x64_bf16 v[36:39], v[76:79], a[120:127], v5\n\t"
      "buffer_store_dwordx4 v[48:51], v9, %[rs], s60 offen sc1\n\t"
      "buffer_store_dwordx4 v[52:55], v9, %[rs], s61 offen sc1\n\t"
      "s_branch LGO%=\n\t"
      "LREDO%=:\n\t"
      "s_add_u32 s42, s42, 1\n\t"
      "s_cmp_gt_u32 s42, 400000\n\t"
      "s_cbranch_scc1 LFAIL%=\n\t"
      "v_mov_b32 v14, s40\n\t"
      "ds_write_b32 v12, v14\n\t"
      "s_mov_b32 m0, s47\n\t"
      "s_waitcnt lgkmcnt(0)\n\t"
      "s_barrier\n\t"
      "buffer_load_dwordx4 v[72:75], v8, %[rs], s54 offen sc1\n\t"
      "buffer_load_dwordx4 v[76:79], v8, %[rs], s54 offen offset:512 sc1\n\t"
      "buffer_load_dwordx4 v6, %[rs], s54 offen sc1 lds\n\t"
      "s_sub_u32 m0, m0, 6144\n\t"
      "s_nop 0\n\t"
      "buffer_load_dwordx4 v7, %[rs], s54 offen sc1 lds\n\t"
      "s_mov_b32 m0, s38\n\t"
      "ds_read_b128 v[96:99], v1 offset:8192\n\t"
      "ds_read_b128 v[100:103], v1 offset:10240\n\t"
      "ds_read_b128 v[104:107], v1 offset:9216\n\t"
      "ds_read_b128 v[108:111], v1 offset:11264\n\t"
      "ds_read_b128 v[112:115], v1 offset:12288\n\t"
      "ds_read_b128 v[116:119], v1 offset:14336\n\t"
      "ds_read_b128 v[120:123], v1 offset:13312\n\t"
      "ds_read_b128 v[124:127], v1 offset:15360\n\t"
      "v_mov_b64 v[24:25], 0\n\t"
      "v_mov_b64 v[26:27], 0\n\t"
      "v_mov_b64 v[28:29], 0\n\t"
      "v_mov_b64 v[30:31], 0\n\t"
      "v_mov_b64 v[32:33], 0\n\t"
      "v_mov_b64 v[34:35], 0\n\t"
      "v_mov_b64 v[36:37], 0\n\t"
      "v_mov_b64 v[38:39], 0\n\t"
      "v_mov_b64 v[40:41], 0\n\t"
      "v_mov_b64 v[42:43], 0\n\t"
      "v_mov_b64 v[44:45], 0\n\t"
      "v_mov_b64 v[46:47], 0\n\t"
      "s_waitcnt lgkmcnt(0)\n\t"
      "ds_read_b128 v[144:147], v1 offset:4096\n\t"
      "ds_read_b128 v[148:151], v1 offset:6144\n\t"
      "ds_read_b128 v[152:155], v1 offset:5120\n\t"
      "ds_read_b128 v[156:159], v1 offset:7168\n\t"
      "ds_read_b128 v[128:131], v1 offset:0\n\t"
      "ds_read_b128 v[132:135], v1 offset:2048\n\t"
      "ds_read_b128 v[136:139], v1 offset:1024\n\t"
      "ds_read_b128 v[140:143], v1 offset:3072\n\t"
      "s_waitcnt lgkmcnt(0)\n\t"
      "s_waitcnt vmcnt(3)\n\t"
      "v_smfmac_f32_16x16x64_bf16 v[24:27], v[72:75], a[64:71], v5\n\t"
      "v_max3_u32 v14, v72, v73, v74\n\t"
      "v_smfmac_f32_16x16x64_bf16 v[28:31], v[72:75], a[72:79], v5\n\t"
      "v_max3_u32 v14, v14, v75, v75\n\t"
      "v_smfmac_f32_16x16x64_bf16 v[32:35], v[72:75], a[96:103], v5\n\t"
      "v_smfmac_f32_16x16x64_bf16 v[36:39], v[72:75], a[104:111], v5\n\t"
      "s_waitcnt vmcnt(2)\n\t"
      "v_smfmac_f32_16x16x64_bf16 v[24:27], v[76:79], a[80:87], v5\n\t"
      "v_max3_u32 v14, v14, v76, v77\n\t"
      "v_smfmac_f32_16x16x64_bf16 v[28:31], v[76:79], a[88:95], v5\n\t"
      "v_max3_u32 v14, v14, v78, v79\n\t"
      "v_smfmac_f32_16x16x64_bf16 v[32:35], v[76:79], a[112:119], v5\n\t"
      "v_cmp_eq_u32 vcc, -1, v14\n\t"
      "v_smfmac_f32_16x16x64_bf16 v[36:39], v[76:79], a[120:127], v5\n\t"
      "s_cbranch_vccnz LRD%=\n\t"
      "buffer_store_dwordx4 v[48:51], v9, %[rs], s60 offen sc1\n\t"
      "buffer_store_dwordx4 v[52:55], v9, %[rs], s61 offen sc1\n\t"
      "s_waitcnt vmcnt(2)\n\t"
      "s_branch LGO%=\n\t"
      "LFAIL%=:\n\t"
      "v_mov_b32 v14, 1\n\t"
      "s_mov_b32 s43, 688128\n\t"
      "buffer_store_dword v14, off, %[rs], s43 sc1\n\t"
      "LEND%=:\n\t"
      "s_waitcnt vmcnt(0) lgkmcnt(0)\n\t"
      "s_mov_b64 exec, -1\n\t"
      "s_mov_b32 m0, s39\n\t"
      : "+{v[0:15]}"(ad), "+{v[16:23]}"(ld)
      : [rs] "s"(rs), [rq] "s"(rq), [rk] "s"(rk), [rv] "s"(rv), [w] "s"(w), [st] "s"(uni(stage)), [cb] "s"(uni((h * 128 + w * 32) * 2)),
        [qso] "s"(uni(WS_QKV + h * (64 * 24576) + w * 2048)), [oxo] "s"(uni(WS_OX + (h >> 3) * (2 * OX_MT) + ((h & 7) * 4 + w) * 1024)),
        [ewx] "s"(uni(ring_phys(EW_OX + h * 128, 1))), [ewa] "s"(uni(ewa_word(h))),
        [t0] "s"(tag.x), [t1] "s"(tag.y), [m0] "s"(m0), [m1] "s"(m1), [m2] "s"(m2), [m3] "s"(m3)
      : "memory", "vcc", "scc", "v24", "v25", "v26", "v27", "v28", "v29", "v30", "v31", "v32", "v33", "v34", "v35", "v36", "v37", "v38", "v39", "v40", "v41",
        "v42", "v43", "v44", "v45", "v46", "v47", "v48", "v49", "v50", "v51", "v52", "v53", "v54", "v55", "v56", "v57", "v58", "v59", "v60", "v61", "v62",
        "v63", "v64", "v65", "v66", "v67", "v68", "v69", "v70", "v71", "v72", "v73", "v74", "v75", "v76", "v77", "v78", "v79", "v80", "v81", "v82", "v83",
        "v84", "v85", "v86", "v87", "v88", "v89", "v90", "v91", "v92", "v93", "v94", "v95", "v96", "v97", "v98", "v99", "v100", "v101", "v102", "v103",
        "v104", "v105", "v106", "v107", "v108", "v109", "v110", "v111", "v112", "v113", "v114", "v115", "v116", "v117", "v118", "v119", "v120", "v121",
        "v122", "v123", "v124", "v125", "v126", "v127", "v128", "v129", "v130", "v131", "v132", "v133", "v134", "v135", "v136", "v137", "v138", "v139",
        "v140", "v141", "v142", "v143", "v144", "v145", "v146", "v147", "v148", "v149", "v150", "v151", "v152", "v153", "v154", "v155", "v156", "v157",
        "v158", "v159", "v160", "v161", "v162", "v163", "v164", "v165", "v166", "v167", "v168", "v169", "v170", "v171", "v172", "v173", "v174", "v175",
        "v176", "v177", "v178", "v179", "v180", "v181", "v182", "v183", "v184", "v185", "v186", "v187", "v188", "v189", "v190", "v191", "v192", "v193",
        "v194", "v195", "v196", "v197", "v198", "v199", "v200", "v201", "v202", "v203", "v204", "v205", "v206", "v207", "v208", "v209", "v210", "v211",
        "v212", "v213", "v214", "v215", "v216", "v217", "v218", "v219", "v220", "v221", "v222", "v223", "v224", "v225", "v226", "v227", "v228", "v229",
        "v230", "v231", "v232", "v233", "v234", "v235", "v236", "v237", "v238", "v239", "v240", "v241", "v242", "v243", "v244", "v245", "v246", "v247",
        "v248", "v249", "v250", "v251", "v252", "v253", "v254", "v255", "a0", "a1", "a2", "a3", "a4", "a5", "a6", "a7", "a8", "a9", "a10", "a11", "a12",
        "a13", "a14", "a15", "a16", "a17", "a18", "a19", "a20", "a21", "a22", "a23", "a24", "a25", "a26", "a27", "a28", "a29", "a30", "a31", "a32", "a33",
        "a34", "a35", "a36", "a37", "a38", "a39", "a40", "a41", "a42", "a43", "a44", "a45", "a46", "a47", "a48", "a49", "a50", "a51", "a52", "a53", "a54",
        "a55", "a56", "a57", "a58", "a59", "a60", "a61", "a62", "a63", "a64", "a65", "a66", "a67", "a68", "a69", "a70", "a71", "a72", "a73", "a74", "a75",
        "a76", "a77", "a78", "a79", "a80", "a81", "a82", "a83", "a84", "a85", "a86", "a87", "a88", "a89", "a90", "a91", "a92", "a93", "a94", "a95", "a96",
        "a97", "a98", "a99", "a100", "a101", "a102", "a103", "a104", "a105", "a106", "a107", "a108", "a109", "a110", "a111", "a112", "a113", "a114", "a115",
        "a116", "a117", "a118", "a119", "a120", "a121", "a122", "a123", "a124", "a125", "a126", "a127", "a128", "a129", "a130", "a131", "a132", "a133",
        "a134", "a135", "a136", "a137", "a138", "a139", "a140", "a141", "a142", "a143", "a144", "a145", "a146", "a147", "a148", "a149", "a150", "a151",
        "a152", "a153", "a154", "a155", "a156", "a157", "a158", "a159", "a160", "a161", "a162", "a163", "a164", "a165", "a166", "a167", "a168", "a169",
        "a170", "a171", "a172", "a173", "a174", "a175", "a176", "a177", "a178", "a179", "a180", "a181", "a182", "a183", "a184", "a185", "a186", "a187",
        "a188", "a189", "a190", "a191", "a192", "a193", "a194", "a195", "a196", "a197", "a198", "a199", "a200", "a201", "a202", "a203", "a204", "a205",
        "a206", "a207", "a208", "a209", "a210", "a211", "a212", "a213", "a214", "a215", "a216", "a217", "a218", "a219", "a220", "a221", "a222", "a223",
        "a224", "a225", "a226", "a227", "a228", "a229", "a230", "a231", "a232", "a233", "a234", "a235", "a236", "a237", "a238", "a239", "a240", "a241",
        "a242", "a243", "a244", "a245", "a246", "a247", "a248", "a249", "a250", "a251", "a252", "a253", "a254", "a255", "s33", "s34", "s35", "s36", "s37",
        "s38", "s39", "s40", "s41", "s42", "s43", "s44", "s45", "s46", "s47", "s48", "s49", "s50", "s51", "s52", "s53", "s54", "s55", "s56", "s57", "s58",
        "s59", "s60", "s61", "s62", "s63", "s64", "s65", "s66", "s67"
  );
}

__global__ void __launch_bounds__(256, 1) rmt_block_kernel(Params p) {
  extern __shared__ __attribute__((aligned(16))) char smem[];
  LAS char* lds = (LAS char*)smem;
  {
    const rsrc_t ws = mk_rsrc(p.ws, WS_END);
    if (threadIdx.x >= 64 && threadIdx.x < 64 + WS_INIT_BYTES / 4096) st16c(ws, blockIdx.x * (WS_INIT_BYTES / 256) + (threadIdx.x - 64) * 16, 0, (u4){~0u, ~0u, ~0u, ~0u});
    unsigned r = (((unsigned)__builtin_amdgcn_s_getreg(20 | (3 << 11)) & 7u) << 5) | ((blockIdx.x >> 3) & 31u);
    unsigned long long t = 0;
    if (threadIdx.x == 0) t = __hip_atomic_load((unsigned long long*)(p.ws + ST_EPOCH), __ATOMIC_RELAXED, __HIP_MEMORY_SCOPE_AGENT);
    {
      const int x = r >> 5, sl = r & 31, lane = threadIdx.x & 63, w = uni(threadIdx.x >> 6);
      const int nf = (x < 6) ? 20 : (x == 6) ? 27 : 10, j = ((x < 7) ? (x - 4) * 12 : 29) + sl - nf;
      const char* src = nullptr;
      if (x >= 4 && sl >= nf && j < 48) src = ((j >= 32) ? p.wv + (j - 32) * 512 + w * 128 : (w < 2) ? p.wq + j * 256 + w * 128 : p.wk + ((j + 1) & 31) * 256 + (w & 1) * 128);
      else if (x < 2) src = ((sl < 16) ? p.wq : p.wk) + (x * 8 + ((sl & 15) >> 1)) * 512 + w * 128 + (size_t)(sl & 1) * (8 * 8 * 8192);
      if (src) {
        src += (size_t)(lane >> 3) * 8192 + (lane & 7) * 16;
        for (int i = 0; i < 8; ++i) touch(src + (size_t)i * 65536, (unsigned)(size_t)lds + 4096 + w * 1024);
      }
    }
    if (threadIdx.x == 0) {
      t += 1ull;
      for (int n = 0; n < 256; ++n) {
        if (__hip_atomic_exchange((unsigned long long*)(p.ws + ST_CLAIM + r * 64), t, __ATOMIC_RELAXED, __HIP_MEMORY_SCOPE_AGENT) != t) break;
        r = (r + 1) & 255u;
      }
      *(LAS u4*)lds = (u4){r, (unsigned)t, (unsigned)(t >> 32), 0u};
    }
    drain();
  }
  bar();
  const u4 st = *(LAS u4*)lds;
  const int role = uni(st.x);
  const u2 tag = {uni(st.y), uni(st.z)};
  bar();
  if (threadIdx.x == 0) st8c(mk_rsrc(p.ws, WS_END), ST_ARRIVE + blockIdx.x * 8, 0, tag);
  const int x = role >> 5, sl = role & 31;
  const int nf = (x < 2) ? 16 : (x < 6) ? 20 : (x == 6) ? 27 : 10;
  if (x < 4 && sl < nf) {
    if (x < 2) role_head(p, lds, tag, x * 16 + sl); else role_red(p, lds, tag, x - 2, sl);
    return;
  }
  if (x == 3 && sl == 31) { role_cal(p, lds, tag); return; }
  if (x >= 4 && sl >= nf) {
    const int j = ((x < 7) ? (x - 4) * 12 : 29) + sl - nf;
    if (j < 48) role_qkv(p, lds, tag, j);
    return;
  }
  if (x < 2) role_help(p, lds, x * 16 + sl - 16);
  if (x < 4 && sl >= 30 - (x == 1)) return;
  if (x >= 4 && x < 7 && sl < 24 && !do_ln(p, lds, tag, (x - 4) * 20 + sl)) return;
  unsigned flip;
  if (!get_flip(mk_rsrc(p.ws, WS_END), ST_FLIP, tag, flip)) return;
  if (x < 2 || x == 6) role_gu(p, lds, flip, (x < 2) ? x * 14 + sl - 16 : sl, x < 2);
  else if (x < 4) { __builtin_amdgcn_s_sleep(0); role_dl(p, lds, flip, x - 2, sl - 20); }
  else if (x < 6) role_wo(p, lds, flip, x - 4, sl);
  else role_dn(p, lds, flip, sl);
}

extern "C" void kernel_launch(void* const* d_in, const int* in_sizes, int n_in, void* d_out, int out_size, void* d_ws, size_t ws_size,
                              hipStream_t stream) {
  Params p;
  p.x = (const char*)d_in[0]; p.mem0 = (const char*)d_in[1]; p.wq = (const char*)d_in[2]; p.wk = (const char*)d_in[3];
  p.wv = (const char*)d_in[4]; p.wo = (const char*)d_in[5]; p.wg = (const char*)d_in[8]; p.wu = (const char*)d_in[9];
  p.wd = (const char*)d_in[10]; p.out = (char*)d_out; p.ws = (char*)d_ws;
  (void)hipFuncSetAttribute((const void*)rmt_block_kernel, hipFuncAttributeMaxDynamicSharedMemorySize, LDS_BYTES);
  rmt_block_kernel<<<256, 256, LDS_BYTES, stream>>>(p);
}
```
